```python
import torch

def _softmax(x, axis=-1):
    return torch.softmax(x, dim=axis)
def _gelu(x):
    return torch.nn.functional.gelu(x, approximate="tanh")
def _silu(x):
    return torch.nn.functional.silu(x)
def _sigmoid(x):
    return torch.sigmoid(x)
def _layernorm(x, axis=-1, eps=1e-5):
    m = x.mean(dim=axis, keepdim=True)
    v = x.var(dim=axis, keepdim=True, unbiased=False)
    return (x - m) / torch.sqrt(v + eps)
def _scatter_add(tgt, idx, src, axis=0):
    return tgt.clone().index_add_(axis, idx, src)
def _segment_sum(src, seg, n):
    out = torch.zeros((n,) + src.shape[1:], dtype=src.dtype)
    return out.index_add_(0, seg, src)
def _pad(x, pw):
    flat = tuple(v for pair in reversed(pw) for v in pair)
    return torch.nn.functional.pad(x, flat)

kernel_name = 'interleave_layers__sliding_local_dilated__mixer_token_mlp__prompt'

def setup_inputs():

    _w = (lambda a: (lambda t: t.to(torch.bfloat16) if t.is_floating_point() else t)(torch.as_tensor(a)))
    def _A_setup():
        B, S, D = 1, 2048, 512
        return {
            "x": _w((lambda *s: torch.randn(*s, dtype=torch.bfloat16) * 0.05)(B, S, D)),
            "wqkv": _w((lambda *s: torch.randn(*s, dtype=torch.bfloat16) * 0.05)(D, 3 * D)),
            "wo": _w((lambda *s: torch.randn(*s, dtype=torch.bfloat16) * 0.05)(D, D)),
        }
    _A_in = _A_setup()

    def _B_setup():
        B, S, D, F = 1, 2048, 512, 1792
        return {
            "x": _w((lambda *s: torch.randn(*s, dtype=torch.bfloat16) * 0.05)(B, S, D)),
            "w1": _w((lambda *s: torch.randn(*s, dtype=torch.bfloat16) * 0.8)(S, F) * (S ** -0.5)),
            "b1": _w((lambda *s: torch.randn(*s, dtype=torch.bfloat16) * 0.8)(F) * 0.02),
            "w2": _w((lambda *s: torch.randn(*s, dtype=torch.bfloat16) * 0.8)(F, S) * (F ** -0.5)),
            "b2": _w((lambda *s: torch.randn(*s, dtype=torch.bfloat16) * 0.8)(S) * 0.02),
        }
    _B_in = _B_setup()

    return {
        "A_x": _A_in["x"],
        "A_wqkv": _A_in["wqkv"],
        "A_wo": _A_in["wo"],
        "B_x": _B_in["x"],
        "B_w1": _B_in["w1"],
        "B_b1": _B_in["b1"],
        "B_w2": _B_in["w2"],
        "B_b2": _B_in["b2"],
    }

def reference(A_x, A_wqkv, A_wo, B_x, B_w1, B_b1, B_w2, B_b2):
    def _A_ref(x, wqkv, wo):
        B, S, D = x.shape
        H = 32; Dh = D // H
        W, dil = 8, 2
        qkv = (lambda x, *d: x.permute(*d))((x @ wqkv).reshape(B, S, 3, H, Dh), 2, 0, 3, 1, 4)
        q, k, v = qkv[0], qkv[1], qkv[2]
        scale = Dh ** -0.5
        pos = torch.arange(S)
        off = torch.arange(W) * dil
        idx = pos[:, None] - off[None, :]
        mask = idx >= 0
        idx_c = torch.where(mask, idx, 0).reshape(-1)
        kg = k[:, :, idx_c, :].reshape(B, H, S, W, Dh)
        vg = v[:, :, idx_c, :].reshape(B, H, S, W, Dh)
        logits = torch.einsum("bhsd,bhswd->bhsw", q, kg) * scale
        logits = torch.where(mask[None, None, :, :], logits, -1e9)
        attn = _softmax(logits, axis=-1)
        o = torch.einsum("bhsw,bhswd->bhsd", attn, vg)
        return (lambda x, *d: x.permute(*d))(o, 0, 2, 1, 3).reshape(B, S, D) @ wo

    def _B_ref(x, w1, b1, w2, b2):
        B, S, D = x.shape
        h = _layernorm(x, axis=-1)
        u = torch.einsum("bsd,sf->bdf", h, w1) + b1
        u = _gelu(u)
        y = torch.einsum("bdf,fs->bds", u, w2) + b2
        return (lambda x, *d: x.permute(*d))(y, 0, 2, 1)

    x = A_x
    x = x + _A_ref(_layernorm(x, axis=-1), A_wqkv, A_wo)
    x = x + _B_ref(_layernorm(x, axis=-1), B_w1, B_b1, B_w2, B_b2)
    x = x + _A_ref(_layernorm(x, axis=-1), A_wqkv, A_wo)
    x = x + _B_ref(_layernorm(x, axis=-1), B_w1, B_b1, B_w2, B_b2)
    return x
```

```cpp
#include <hip/hip_runtime.h>
#include <hip/hip_bf16.h>
typedef unsigned short u16;
__device__ __forceinline__ float bf2f(u16 b){ return __builtin_bit_cast(float,(unsigned)b<<16); }
__device__ __forceinline__ u16 f2bf(float f){ __hip_bfloat16 h=__float2bfloat16(f); return __builtin_bit_cast(u16,h);}
constexpr int S=2048,D=512,F=1792,H=32,DH=16,W=8,DIL=2;

__global__ void k_ln(const u16* x,u16* o,int dbl){
  int row=blockIdx.x; int t=threadIdx.x;
  float v[8]; float s=0;
  for(int j=0;j<8;j++){v[j]=bf2f(x[row*D+t*8+j]); s+=v[j];}
  for(int it=0;it<=dbl;it++){
    s=0; for(int j=0;j<8;j++) s+=v[j];
    for(int m=32;m;m>>=1) s+=__shfl_xor(s,m);
    float mean=s/D; float q=0; for(int j=0;j<8;j++){float d=v[j]-mean;q+=d*d;}
    for(int m=32;m;m>>=1) q+=__shfl_xor(q,m);
    float r=rsqrtf(q/D+1e-5f);
    for(int j=0;j<8;j++) v[j]=(v[j]-mean)*r;
  }
  for(int j=0;j<8;j++) o[row*D+t*8+j]=f2bf(v[j]);
}
template<int MODE> __global__ void k_gemm(const u16*A,int sam,int sak,const u16*B,int N,int K,u16*C,const u16*bias,const u16*res){
  __shared__ float As[16][17],Bs[16][17];
  int tx=threadIdx.x&15,ty=threadIdx.x>>4; int m=blockIdx.y*16+ty,n=blockIdx.x*16+tx; float acc=0;
  for(int k0=0;k0<K;k0+=16){
    As[ty][tx]=bf2f(A[(size_t)m*sam+(size_t)(k0+tx)*sak]); Bs[ty][tx]=bf2f(B[(size_t)(k0+ty)*N+n]);
    __syncthreads();
    for(int k=0;k<16;k++) acc+=As[ty][k]*Bs[k][tx];
    __syncthreads();
  }
  if(MODE==0) C[(size_t)m*N+n]=f2bf(acc);
  if(MODE==1) C[(size_t)m*N+n]=f2bf(bf2f(res[(size_t)m*N+n])+bf2f(f2bf(acc)));
  if(MODE==2){ float u=acc+bf2f(bias[n]); float g=0.5f*u*(1.f+tanhf(0.7978845608028654f*(u+0.044715f*u*u*u))); C[(size_t)m*N+n]=f2bf(g);}
  if(MODE==3){ float y=acc+bf2f(bias[n]); C[(size_t)n*D+m]=f2bf(bf2f(res[(size_t)n*D+m])+bf2f(f2bf(y))); }
}
__global__ void k_attn(const u16*qkv,u16*o){
  int idx=blockIdx.x*blockDim.x+threadIdx.x; int s=idx/H,h=idx%H; if(s>=S) return;
  float q[DH]; for(int d=0;d<DH;d++) q[d]=bf2f(qkv[(size_t)s*3*D+h*DH+d]);
  float l[W]; float mx=-1e30f;
  for(int w=0;w<W;w++){ int j=s-w*DIL; if(j>=0){ float a=0; for(int d=0;d<DH;d++) a+=q[d]*bf2f(qkv[(size_t)j*3*D+D+h*DH+d]); l[w]=a*0.25f;} else l[w]=-1e9f; mx=fmaxf(mx,l[w]); }
  float sum=0; for(int w=0;w<W;w++){ l[w]=__expf(l[w]-mx); sum+=l[w]; }
  float acc[DH]; for(int d=0;d<DH;d++) acc[d]=0;
  for(int w=0;w<W;w++){ int j=s-w*DIL; if(j<0) j=0; float p=l[w]/sum; for(int d=0;d<DH;d++) acc[d]+=p*bf2f(qkv[(size_t)j*3*D+2*D+h*DH+d]); }
  for(int d=0;d<DH;d++) o[(size_t)s*D+h*DH+d]=f2bf(acc[d]);
}
extern "C" void kernel_launch(void* const* d_in,const int* in_sizes,int n_in,void* d_out,int out_size,void* d_ws,size_t ws_size,hipStream_t stream){
  const u16* x0=(const u16*)d_in[0]; const u16* wqkv=(const u16*)d_in[1]; const u16* wo=(const u16*)d_in[2];
  const u16* w1=(const u16*)d_in[4]; const u16* b1=(const u16*)d_in[5]; const u16* w2=(const u16*)d_in[6]; const u16* b2=(const u16*)d_in[7];
  u16* ws=(u16*)d_ws; u16* h=ws; u16* qkv=h+S*D; u16* o=qkv+S*3*D; u16* x1=o+S*D; u16* u=x1+S*D; u16* x2=u+D*F; u16* out=(u16*)d_out;
  const u16* xin=x0;
  for(int it=0;it<2;it++){
    k_ln<<<S,64,0,stream>>>(xin,h,0);
    k_gemm<0><<<dim3(3*D/16,S/16),256,0,stream>>>(h,D,1,wqkv,3*D,D,qkv,nullptr,nullptr);
    k_attn<<<S*H/256,256,0,stream>>>(qkv,o);
    k_gemm<1><<<dim3(D/16,S/16),256,0,stream>>>(o,D,1,wo,D,D,x1,nullptr,xin);
    k_ln<<<S,64,0,stream>>>(x1,h,1);
    k_gemm<2><<<dim3(F/16,D/16),256,0,stream>>>(h,1,D,w1,F,S,u,b1,nullptr);
    u16* xo = it==0? x2: out;
    k_gemm<3><<<dim3(S/16,D/16),256,0,stream>>>(u,F,1,w2,S,F,xo,b2,x1);
    xin=x2;
  }
}
```

```cpp
#include <hip/hip_runtime.h>
#include <hip/hip_bf16.h>
#include <utility>

typedef unsigned short u16;
typedef unsigned v4u __attribute__((ext_vector_type(4)));
typedef unsigned v2u __attribute__((ext_vector_type(2)));
typedef float f4 __attribute__((ext_vector_type(4)));
typedef float f2 __attribute__((ext_vector_type(2)));
typedef __bf16 bf2 __attribute__((ext_vector_type(2)));
typedef __amdgpu_buffer_rsrc_t rsrc_t;
#define LAS __attribute__((address_space(3)))
#define DI __device__ __forceinline__
#define GAP() __builtin_amdgcn_sched_barrier(0)
template <int... Is> using iseq = std::integer_sequence<int, Is...>;
template <int N> using mkseq = std::make_integer_sequence<int, N>;

constexpr int S = 2048, D = 512, F = 1792;
constexpr float EPS = 1e-5f;
constexpr unsigned WS_FLAG = 4096;
constexpr unsigned WS_STAT = WS_FLAG + 8 * 256 * 128;
constexpr unsigned WS_O = 1u << 20;
constexpr unsigned WS_H = WS_O + 2 * (2u << 20);
constexpr unsigned WS_U = WS_H + 2 * (2u << 20);
constexpr unsigned WS_X2 = WS_U + 2 * (2u << 20);
constexpr unsigned WS_DBG = WS_X2 + (2u << 20);
constexpr unsigned WS_END = WS_DBG + (1u << 20);
constexpr unsigned L_Z = 0;
constexpr unsigned L_RES = 36 * 4096;
constexpr unsigned L_ST = L_RES + 8192;
constexpr unsigned L_END = L_ST + 1024;
constexpr unsigned SPIN_CAP = 1u << 20;

#define ACLOB "a0","a1","a2","a3","a4","a5","a6","a7","a8","a9","a10","a11","a12","a13","a14","a15","a16","a17","a18","a19","a20","a21","a22","a23","a24","a25","a26","a27","a28","a29","a30","a31","a32","a33","a34","a35","a36","a37","a38","a39","a40","a41","a42","a43","a44","a45","a46","a47","a48","a49","a50","a51","a52","a53","a54","a55","a56","a57","a58","a59","a60","a61","a62","a63","a64","a65","a66","a67","a68","a69","a70","a71","a72","a73","a74","a75","a76","a77","a78","a79","a80","a81","a82","a83","a84","a85","a86","a87","a88","a89","a90","a91","a92","a93","a94","a95","a96","a97","a98","a99","a100","a101","a102","a103","a104","a105","a106","a107","a108","a109","a110","a111","a112","a113","a114","a115","a116","a117","a118","a119","a120","a121","a122","a123","a124","a125","a126","a127","a128","a129","a130","a131","a132","a133","a134","a135","a136","a137","a138","a139","a140","a141","a142","a143","a144","a145","a146","a147","a148","a149","a150","a151","a152","a153","a154","a155","a156","a157","a158","a159","a160","a161","a162","a163","a164","a165","a166","a167","a168","a169","a170","a171","a172","a173","a174","a175","a176","a177","a178","a179","a180","a181","a182","a183","a184","a185","a186","a187","a188","a189","a190","a191","a192","a193","a194","a195","a196","a197","a198","a199","a200","a201","a202","a203","a204","a205","a206","a207","a208","a209","a210","a211","a212","a213","a214","a215","a216","a217","a218","a219","a220","a221","a222","a223","a224","a225","a226","a227","a228","a229","a230","a231","a232","a233","a234","a235","a236","a237","a238","a239","a240","a241","a242","a243","a244","a245","a246","a247","a248","a249","a250","a251","a252","a253","a254","a255"

DI unsigned cvtpk(float lo, float hi) {
  f2 v = {lo, hi};
  bf2 b = __builtin_convertvector(v, bf2);
  return __builtin_bit_cast(unsigned, b);
}
DI float bflo(unsigned u) { return __builtin_bit_cast(float, u << 16); }
DI float bfhi(unsigned u) { return __builtin_bit_cast(float, u & 0xffff0000u); }
template <int CTRL> DI float dpp(float x) {
  return __builtin_bit_cast(float, __builtin_amdgcn_update_dpp(0, __builtin_bit_cast(int, x), CTRL, 0xf, 0xf, true));
}
DI void bar() { asm volatile("s_waitcnt lgkmcnt(0)\n\ts_barrier" ::: "memory"); }
template <int N> DI void waitvm() { asm volatile("s_waitcnt vmcnt(%0)" ::"i"(N) : "memory"); }
DI void waitlgkm() { asm volatile("s_waitcnt lgkmcnt(0)" ::: "memory"); }
template <unsigned LI, unsigned SI> DI void dma(unsigned lds, unsigned voff, rsrc_t rs, unsigned sb) {
  unsigned t;
  asm volatile("s_add_u32 m0, %1, %5\n\ts_add_u32 %0, %4, %6\n\tbuffer_load_dwordx4 %2, %3, %0 offen lds"
               : "=&s"(t) : "s"(lds), "v"(voff), "s"(rs), "s"(sb), "i"(LI), "i"(SI) : "memory", "scc");
}
template <unsigned SI> DI void lda(v4u& d, unsigned voff, rsrc_t rs, unsigned sb) {
  if constexpr (SI < 4096) {
    asm volatile("buffer_load_dwordx4 %0, %1, %2, %3 offen offset:%4 sc1" : "=v"(d) : "v"(voff), "s"(rs), "s"(sb), "i"(SI) : "memory");
  } else {
    unsigned t;
    asm volatile("s_add_u32 %1, %4, %5\n\tbuffer_load_dwordx4 %0, %2, %3, %1 offen sc1" : "=v"(d), "=&s"(t) : "v"(voff), "s"(rs), "s"(sb), "i"(SI) : "memory", "scc");
  }
}
template <unsigned OFF> DI void ldx(v4u& d, unsigned voff, rsrc_t rs) {
  asm volatile("buffer_load_dwordx4 %0, %1, %2, 0 offen offset:%3 sc1" : "+v"(d) : "v"(voff), "s"(rs), "i"(OFF) : "memory");
}
DI int rfl(int x) { return __builtin_amdgcn_readfirstlane(x); }
DI int fresh_v(int x) { asm volatile("" : "+v"(x)); return x; }
DI int fresh_s(int x) { asm volatile("" : "+s"(x)); return x; }
template <int N> DI void wait1(v4u& a) { asm volatile("s_waitcnt vmcnt(%1)" : "+v"(a) : "i"(N) : "memory"); }
template <int N> DI void wait4(v4u& a, v4u& b, v4u& c, v4u& d) {
  asm volatile("s_waitcnt vmcnt(%4)" : "+v"(a), "+v"(b), "+v"(c), "+v"(d) : "i"(N) : "memory");
}
template <int AG, int OFF, int STEP> DI void trA(unsigned addr) {
  asm volatile("ds_read_b64_tr_b16 a[%c1:%c2], %0 offset:%c5\n\tds_read_b64_tr_b16 a[%c3:%c4], %0 offset:%c6" ::"v"(addr), "i"(AG), "i"(AG + 1), "i"(AG + 2),
               "i"(AG + 3), "i"(OFF), "i"(OFF + STEP)
               : "memory");
}
typedef short s4 __attribute__((ext_vector_type(4)));
DI v2u trV(unsigned addr) { return __builtin_bit_cast(v2u, __builtin_amdgcn_ds_read_tr16_b64_v4i16((LAS s4*)addr)); }
template <int AG> DI void mf_aw(f4& c, const v4u& b) {
  asm volatile("v_mfma_f32_16x16x32_bf16 %0, a[%c2:%c3], %1, %0" : "+v"(c) : "v"(b), "i"(AG), "i"(AG + 3));
}
template <int AG> DI void mf_wa(f4& c, const v4u& a) {
  asm volatile("v_mfma_f32_16x16x32_bf16 %0, %1, a[%c2:%c3], %0" : "+v"(c) : "v"(a), "i"(AG), "i"(AG + 3));
}
DI void mf_vv(f4& c, const v4u& a, const v4u& b) { asm volatile("v_mfma_f32_16x16x32_bf16 %0, %1, %2, %0" : "+v"(c) : "v"(a), "v"(b)); }
DI void pin4(f4& a, f4& b, f4& c, f4& d) { asm volatile("" : "+v"(a), "+v"(b), "+v"(c), "+v"(d)); }
DI void nops() { asm volatile("s_nop 7\n\ts_nop 1"); }
DI void st16(v4u v, rsrc_t rs, unsigned voff, unsigned soff) { __builtin_amdgcn_raw_buffer_store_b128(v, rs, voff, soff, 16); }

struct Tag { unsigned lo, hi; };
DI void set_flag(rsrc_t rws, Tag t, int lane, unsigned soff) {
  if (lane == 0) __builtin_amdgcn_raw_buffer_store_b64(v2u{t.lo, t.hi}, rws, 0, soff, 16);
}
DI void poll(rsrc_t rws, Tag t, unsigned voff, unsigned soff) {
  for (unsigned spin = 0; spin < SPIN_CAP; ++spin) {
    v2u f;
    asm volatile("buffer_load_dwordx2 %0, %1, %2, %3 offen sc1\n\ts_waitcnt vmcnt(0)" : "=&v"(f) : "v"(voff), "s"(rws), "s"(soff) : "memory");
    if (__all(f.x == t.lo && f.y == t.hi)) break;
  }
}

DI void stamp(int) {}

template <unsigned LI, unsigned SI, unsigned STRIDE> DI void dma_slot(unsigned lds, unsigned ve, unsigned vo, rsrc_t rs, unsigned sb) {
  dma<LI, SI>(lds, ve, rs, sb);
  dma<LI + 1024, SI + 8 * STRIDE>(lds, vo, rs, sb);
  dma<LI + 2048, SI + 16 * STRIDE>(lds, ve, rs, sb);
  dma<LI + 3072, SI + 24 * STRIDE>(lds, vo, rs, sb);
}
DI unsigned tr_nat(int lane) { const int g = lane >> 4, q = (lane >> 2) & 3, p = lane & 3; return (8 * g + q) * 128 + p * 8 + ((((q >> 1) & 1) | ((g & 1) << 1)) * 32); }
DI unsigned tr_prm(int lane) { const int g = lane >> 4, q = (lane >> 2) & 3, p = lane & 3; return (4 * g + q) * 128 + p * 8 + ((((q >> 1) & 1) | ((g & 1) << 1)) * 32); }
DI unsigned src_nat(int lane, unsigned stride) { const int r = lane >> 3, pc = lane & 7; return r * stride + (pc ^ (r & 2)) * 16; }
DI unsigned src_prm(int lane, unsigned stride) { const int r = lane >> 3, pc = lane & 7; return r * stride + (pc ^ (r & 6)) * 16; }

template <int KS, int OFF, int STEP> DI void rd_slot(const unsigned (&t)[4]) {
  trA<KS * 16 + 0, OFF, STEP>(t[0]);
  trA<KS * 16 + 4, OFF, STEP>(t[1]);
  trA<KS * 16 + 8, OFF, STEP>(t[2]);
  trA<KS * 16 + 12, OFF, STEP>(t[3]);
}
template <int K0, int STEP> DI void rd_slot5(const unsigned (&t)[4]) {
  rd_slot<K0, 0, STEP>(t); rd_slot<K0 + 1, 4096, STEP>(t); rd_slot<K0 + 2, 8192, STEP>(t); rd_slot<K0 + 3, 12288, STEP>(t); rd_slot<K0 + 4, 16384, STEP>(t);
}
template <int BLK, int... I> DI void rd_qkv(unsigned addr, iseq<I...>) {
  (trA<4 * (12 * BLK + I), I * 4096, 512>(addr), ...);
}
template <int BLK, unsigned LI, int PART> DI void dma_qkv(unsigned lds, unsigned v, rsrc_t rs, unsigned sb) {
  dma_slot<LI + PART * 4096, BLK * 128 * 3072 + PART * 1024, 3072>(lds, v, v ^ 64, rs, sb);
}
template <int K0, unsigned STRIDE> DI void dma5(unsigned lds, unsigned ve, unsigned vo, rsrc_t rs, unsigned sb) {
  dma_slot<0, (K0 + 0) * 32 * STRIDE, STRIDE>(lds, ve, vo, rs, sb);
  dma_slot<4096, (K0 + 1) * 32 * STRIDE, STRIDE>(lds, ve, vo, rs, sb);
  dma_slot<8192, (K0 + 2) * 32 * STRIDE, STRIDE>(lds, ve, vo, rs, sb);
  dma_slot<12288, (K0 + 3) * 32 * STRIDE, STRIDE>(lds, ve, vo, rs, sb);
  dma_slot<16384, (K0 + 4) * 32 * STRIDE, STRIDE>(lds, ve, vo, rs, sb);
}

struct P1S {
  f4 Q[5], K[5], V[5];
  v4u xf[2][5];
  float s1[5], s2[5];
};
template <int I> DI void p1_fill(P1S& p, const v4u (&X)[20]) {
  if constexpr (I < 160) {
    constexpr int ld = I >> 3, e = (I >> 1) & 3, q = ld >> 2;
    const unsigned xu = X[ld][e];
    const bf2 v = __builtin_bit_cast(bf2, xu);
    if constexpr (I & 1) p.s2[q] = __builtin_amdgcn_fdot2_f32_bf16(v, v, p.s2[q], false);
    else p.s1[q] = __builtin_amdgcn_fdot2_f32_bf16(v, __builtin_bit_cast(bf2, 0x3f803f80u), p.s1[q], false);
  } else if constexpr (I < 200) {
    constexpr int j = I - 160, st = j / 10, q = (j % 10) >> 1;
    constexpr int ctrl = 0x120 + (1 << st);
    if constexpr (j & 1) p.s2[q] += dpp<ctrl>(p.s2[q]);
    else p.s1[q] += dpp<ctrl>(p.s1[q]);
  }
}
template <int KS, int M> DI void p1_gap(P1S& p, const v4u (&X)[20], const unsigned (&xa)[4]) {
  constexpr int rb = M / 3, part = M % 3, cur = KS & 1;
  if constexpr (part == 0) mf_aw<4 * (3 * KS + 1)>(p.K[rb], p.xf[cur][rb]);
  if constexpr (part == 1) mf_aw<4 * (3 * KS + 0)>(p.Q[rb], p.xf[cur][rb]);
  if constexpr (part == 2) mf_wa<4 * (3 * KS + 2)>(p.V[rb], p.xf[cur][rb]);
  if constexpr (KS < 15 && M < 5) {
    if constexpr (M < 4) p.xf[cur ^ 1][M] = *(LAS v4u*)(size_t)(xa[(KS + 1) & 1] + (M * 16 + KS + 1) * 1024);
    else p.xf[cur ^ 1][M] = *(LAS v4u*)(size_t)(xa[2 + ((KS + 1) & 1)] + (KS + 1) * 1024);
  }
  p1_fill<KS * 15 + M>(p, X);
  GAP();
}
template <int KS, int... M> DI void p1_step(P1S& p, const v4u (&X)[20], const unsigned (&xa)[4], iseq<M...>) { (p1_gap<KS, M>(p, X, xa), ...); }
template <int K0, int... KS> DI void p1_steps(P1S& p, const v4u (&X)[20], const unsigned (&xa)[4], iseq<KS...>) {
  (p1_step<K0 + KS>(p, X, xa, mkseq<15>{}), ...);
}

template <int NK, bool WA, int KS, class HOOK>
DI void big_step(f4 (&acc)[16], v4u (&A)[32], const unsigned (&vo)[4], rsrc_t rs, unsigned sb, const HOOK& hook) {
  constexpr int s = (KS & 7) * 4;
  constexpr int NREF = NK - 8;
  constexpr int ndma = KS <= NREF ? 0 : (KS - NREF > 6 ? 6 : KS - NREF);
  constexpr int younger = (KS < NREF ? 28 : (NK - 1 - KS) * 4) + ndma * 4;
  wait4<younger>(A[s], A[s + 1], A[s + 2], A[s + 3]);
#define ROW(m) \
  if constexpr (WA) { mf_aw<KS * 16 + 0>(acc[0 + m], A[s + m]); mf_aw<KS * 16 + 4>(acc[4 + m], A[s + m]); mf_aw<KS * 16 + 8>(acc[8 + m], A[s + m]); mf_aw<KS * 16 + 12>(acc[12 + m], A[s + m]); } \
  else { mf_wa<KS * 16 + 0>(acc[4 * m + 0], A[s + m]); mf_wa<KS * 16 + 4>(acc[4 * m + 1], A[s + m]); mf_wa<KS * 16 + 8>(acc[4 * m + 2], A[s + m]); mf_wa<KS * 16 + 12>(acc[4 * m + 3], A[s + m]); } \
  if constexpr (KS < NREF) lda<(KS + 8) * 4096>(A[s + m], vo[m], rs, sb);
  ROW(0) ROW(1) ROW(2) ROW(3)
#undef ROW
  if constexpr (KS >= NREF && KS < NREF + 6) hook.template run<KS - NREF>();
}
template <int... I> DI void big_first(v4u (&A)[32], const unsigned (&vo)[4], rsrc_t rs, unsigned sb, iseq<I...>) {
  (lda<(I >> 2) * 4096>(A[I], vo[I & 3], rs, sb), ...);
}
DI void pin16(f4 (&acc)[16]) {
  pin4(acc[0], acc[1], acc[2], acc[3]); pin4(acc[4], acc[5], acc[6], acc[7]);
  pin4(acc[8], acc[9], acc[10], acc[11]); pin4(acc[12], acc[13], acc[14], acc[15]);
}
template <int NK, bool WA, class HOOK, int... KS>
DI void big_loop(f4 (&acc)[16], const unsigned (&vo)[4], rsrc_t rs, unsigned sb, const HOOK& hook, iseq<KS...>) {
  v4u A[32];
  pin16(acc);
  big_first(A, vo, rs, sb, mkseq<32>{});
  (big_step<NK, WA, KS>(acc, A, vo, rs, sb, hook), ...);
  nops();
  pin16(acc);
}
struct HookW2 {
  unsigned stg, stg5, v; rsrc_t rs; unsigned sb;
  template <int J> DI void run() const {
    if constexpr (J < 5) dma_slot<J * 4096, J * 32 * 4096, 4096>(stg, v, v, rs, sb);
    else dma_slot<0, J * 32 * 4096, 4096>(stg5, v, v, rs, sb);
  }
};
struct HookQkv {
  unsigned lds, v; rsrc_t rs; unsigned sb;
  template <int J> DI void run() const { dma_qkv<J / 3, (J / 3) * 49152, J % 3>(lds, v, rs, sb); }
};
DI void reduce4(int lane, int w, f4 (&acc)[16]) {
  const unsigned base = L_Z + 24 * 4096 + lane * 16;
#pragma unroll
  for (int m = 1; m < 4; ++m) {
    const unsigned dst = base + (unsigned)((((w + m) & 3) * 3 + m - 1) * 4096);
#pragma unroll
    for (int i = 0; i < 4; ++i) *(LAS f4*)(size_t)(dst + i * 1024) = acc[4 * i + m];
  }
  bar();
  const unsigned src = base + w * 3 * 4096;
#pragma unroll
  for (int i = 0; i < 4; ++i) {
    const f4 a = *(LAS f4*)(size_t)(src + i * 1024), b = *(LAS f4*)(size_t)(src + 4096 + i * 1024), c = *(LAS f4*)(size_t)(src + 8192 + i * 1024);
    acc[4 * i] = (acc[4 * i] + a) + (b + c);
  }
}
DI v4u swap_pair(v2u a, v2u b) {
  auto x = __builtin_amdgcn_permlane16_swap(a.x, b.x, false, false);
  auto y = __builtin_amdgcn_permlane16_swap(a.y, b.y, false, false);
  return v4u{x[0], y[0], x[1], y[1]};
}
DI float gelu(float u) {
  const float t = u * u * (-2.0f * 0.7978845608028654f * 0.044715f * 1.4426950408889634f) + (-2.0f * 0.7978845608028654f * 1.4426950408889634f);
  const float e = __builtin_amdgcn_exp2f(u * t);
  return u * __builtin_amdgcn_rcpf(1.0f + e);
}
DI void load_x(v4u (&X)[20], const unsigned (&xo)[5], rsrc_t r) {
#pragma unroll
  for (int i = 0; i < 5; ++i) { ldx<0>(X[4 * i], xo[i], r); ldx<256>(X[4 * i + 1], xo[i], r); ldx<512>(X[4 * i + 2], xo[i], r); ldx<768>(X[4 * i + 3], xo[i], r); }
}
template <int... I> DI void load_blk3(v4u (&W)[12], unsigned v, rsrc_t rs, unsigned sb, iseq<I...>) {
  (lda<3 * 128 * 3072 + (I >> 2) * 1024 + (I & 3) * 8 * 3072>(W[I], (I & 1) ? v ^ 64 : v, rs, sb), ...);
}
template <int... I> DI void load_o(v4u (&A)[16], unsigned v, rsrc_t rs, unsigned sb, iseq<I...>) { (lda<I * 1024>(A[I], v, rs, sb), ...); }

__global__ void __launch_bounds__(256, 1)
fused_kernel(const u16* __restrict__ x0, const u16* __restrict__ wqkv, const u16* __restrict__ wo, const u16* __restrict__ w1,
             const u16* __restrict__ b1, const u16* __restrict__ w2, const u16* __restrict__ b2, u16* __restrict__ out, char* __restrict__ ws) {
  __shared__ __attribute__((aligned(16))) char smem[163840];
  asm volatile("" ::"v"((LAS char*)smem) : ACLOB);
  const int lane0 = threadIdx.x & 63;
  const int w0 = rfl(threadIdx.x >> 6);
  const int bx = blockIdx.x & 7, bj = blockIdx.x >> 3;
  const int tb0 = 8 * (bx >> 1) + (bj & 7), ct0 = 4 * (bx & 1) + (bj >> 3);
  const bool act3 = bj < 28;
  const int db0 = 4 * (bx & 1) + (bj & 3), fb0 = act3 ? 7 * (bx >> 1) + (bj >> 2) : 0;
  const rsrc_t rws = __builtin_amdgcn_make_buffer_rsrc(ws, 0, WS_END, 0x00020000);
  const rsrc_t rx = __builtin_amdgcn_make_buffer_rsrc((void*)x0, 0, S * D * 2, 0x00020000);
  const rsrc_t rqkv = __builtin_amdgcn_make_buffer_rsrc((void*)wqkv, 0, D * 3 * D * 2, 0x00020000);
  const rsrc_t rwo = __builtin_amdgcn_make_buffer_rsrc((void*)wo, 0, D * D * 2, 0x00020000);
  const rsrc_t rw1 = __builtin_amdgcn_make_buffer_rsrc((void*)w1, 0, S * F * 2, 0x00020000);
  const rsrc_t rw2 = __builtin_amdgcn_make_buffer_rsrc((void*)w2, 0, F * S * 2, 0x00020000);
  const rsrc_t rout = __builtin_amdgcn_make_buffer_rsrc((void*)out, 0, S * D * 2, 0x00020000);
  const rsrc_t rx2 = __builtin_amdgcn_make_buffer_rsrc(ws + WS_X2, 0, S * D * 2, 0x00020000);
  stamp(0);
#define LOCALS \
  const int lane = fresh_v(lane0), g = lane >> 4, c = lane & 15; \
  const int w = fresh_s(w0), tb = fresh_s(tb0), ct = fresh_s(ct0); \
  (void)g; (void)c; (void)w; (void)tb; (void)ct;

  v2u ep, b1v[4];
  unsigned b2u;
  {
    LOCALS
    asm volatile("buffer_load_dwordx2 %0, off, %1, 0 sc1" : "=v"(ep) : "s"(rws) : "memory");
    asm volatile("global_load_dwordx2 %0, %5, %7\n\tglobal_load_dwordx2 %1, %5, %7 offset:32\n\tglobal_load_dwordx2 %2, %5, %7 offset:64\n\t"
                 "global_load_dwordx2 %3, %5, %7 offset:96\n\tglobal_load_ushort %4, %6, %8"
                 : "=&v"(b1v[0]), "=&v"(b1v[1]), "=&v"(b1v[2]), "=&v"(b1v[3]), "=&v"(b2u)
                 : "v"(fb0 * 128 + g * 8), "v"((tb * 64 + w * 16 + c) * 2), "s"(b1), "s"(b2) : "memory");
    if (lane < 16) st16(v4u{~0u, ~0u, ~0u, ~0u}, rws, (lane >> 3) * 32 * 4096 + (lane & 7) * 16, WS_STAT + (unsigned)((tb * 4 + w) * 8 + ct) * 128u);
    const HookQkv hq{L_Z + w * 12288u, src_nat(lane, 3072), rqkv, w * 32u * 3072u + ct * 128u};
    hq.run<0>(); hq.run<1>(); hq.run<2>(); hq.run<3>(); hq.run<4>(); hq.run<5>();
    const unsigned v = src_prm(lane, 1024), sb = (tb * 64 + w * 16) * 1024 + ct * 128;
    dma<L_RES, 0>(w * 2048, v, rx, sb);
    dma<L_RES + 1024, 8192>(w * 2048, v, rx, sb);
  }
  Tag tag{0u, 0u};

#pragma unroll 1
  for (int it = 0; it < 2; ++it) {
    const unsigned itof = (unsigned)it << 21, fl0 = WS_FLAG + (unsigned)it * (4 * 256 * 128);
    P1S p;
    {
      LOCALS
      v4u X[20], W[12];
      {
        const unsigned v = src_nat(lane, 3072), sb = w * 32u * 3072u + ct * 128u, lds = L_Z + w * 12288u;
        dma_qkv<2, 2 * 49152, 0>(lds, v, rqkv, sb); dma_qkv<2, 2 * 49152, 1>(lds, v, rqkv, sb); dma_qkv<2, 2 * 49152, 2>(lds, v, rqkv, sb);
        load_blk3(W, v, rqkv, sb, mkseq<12>{});
      }
      unsigned xo[5];
#pragma unroll
      for (int i = 0; i < 5; ++i) { int r = tb * 64 - 16 + (w * 5 + i) * 4 + g; r = r < 0 ? 0 : r; xo[i] = r * 1024 + c * 16; }
#pragma unroll
      for (int i = 0; i < 20; ++i) asm volatile("" : "=v"(X[i]));
      if (it == 0) {
        load_x(X, xo, rx);
        waitvm<20>();
        asm volatile("" : "+v"(ep), "+v"(b1v[0]), "+v"(b1v[1]), "+v"(b1v[2]), "+v"(b1v[3]), "+v"(b2u));
        const unsigned long long t = (((unsigned long long)(unsigned)rfl(ep.y) << 32) | (unsigned)rfl(ep.x)) + 1ull;
        tag.lo = (unsigned)t; tag.hi = (unsigned)(t >> 32);
      } else {
        waitvm<24>();
        set_flag(rws, tag, lane, WS_FLAG + (unsigned)(3 * 256 + tb * 8 + ct) * 128u + w * 8);
        waitvm<0>();
      }
      bar();
      const unsigned ta = tr_nat(lane) ^ (w << 5);
      rd_qkv<0>(ta, mkseq<12>{});
      rd_qkv<1>(ta + 49152, mkseq<12>{});
      rd_qkv<2>(ta + 2 * 49152, mkseq<12>{});
      bar();
      wait4<63>(W[0], W[1], W[2], W[3]); wait4<63>(W[4], W[5], W[6], W[7]); wait4<63>(W[8], W[9], W[10], W[11]);
#pragma unroll
      for (int j = 0; j < 12; ++j) *(LAS v4u*)(size_t)(L_Z + 2 * 49152 + (w * 12 + j) * 1024 + lane * 16) = W[j];
      if (it == 1) {
        bar();
        rd_qkv<3>(ta + 2 * 49152, mkseq<12>{});
        int l = lane < 40 ? lane : lane - 32;
        if (tb == 0 && l < 8) l += 8;
        const int rbi = l >> 3, pid = (rbi == 0 ? 0 : 8) + (l & 7), pw = (rbi + 3) & 3;
        stamp(12);
        poll(rws, tag, pid * 128 + pw * 8, WS_FLAG + (unsigned)(3 * 256 + tb * 8 - 8) * 128u);
        stamp(13);
        load_x(X, xo, rx2);
      }
      {
        const int gg = c & 3, kk = c >> 2;
#pragma unroll
        for (int i = 0; i < 5; ++i) {
          if (i == 0) wait4<16>(X[0], X[1], X[2], X[3]);
          if (i == 1) wait4<12>(X[4], X[5], X[6], X[7]);
          if (i == 2) wait4<8>(X[8], X[9], X[10], X[11]);
          if (i == 3) wait4<4>(X[12], X[13], X[14], X[15]);
          if (i == 4) wait4<0>(X[16], X[17], X[18], X[19]);
          const int row = (w * 5 + i) * 4 + g;
          const unsigned a = L_Z + ((row >> 4) * 16 + kk) * 1024 + gg * 256 + (((row & 15) ^ (gg + 12 * (kk & 1))) * 16);
#pragma unroll
          for (int cb = 0; cb < 4; ++cb) {
            v4u v = X[4 * i + cb];
            if (i == 0 && row == 0) v = v4u{0x3f803f80u, 0x3f803f80u, 0x3f803f80u, 0x3f803f80u};
            *(LAS v4u*)(size_t)(a + cb * 4096) = v;
          }
        }
      }
      bar();
      if (it == 0) { rd_qkv<3>(ta + 2 * 49152, mkseq<12>{}); bar(); }
      stamp(1 + it * 13);
      {
        const unsigned v = src_nat(lane, 1024), sb = w * 128u * 1024u + ct * 128u, lds = L_Z + (20 + w * 4) * 4096u;
        dma_slot<0, 0, 1024>(lds, v, v ^ 64, rwo, sb); dma_slot<4096, 32768, 1024>(lds, v, v ^ 64, rwo, sb);
        dma_slot<8192, 65536, 1024>(lds, v, v ^ 64, rwo, sb); dma_slot<12288, 98304, 1024>(lds, v, v ^ 64, rwo, sb);
      }
      const unsigned xe = L_Z + g * 256u + ((c ^ g) * 16), xd = L_Z + g * 256u + ((c ^ (g + 12)) * 16);
      const unsigned xa[4] = {xe, xd, xe + 65536, xd + 65536};
#pragma unroll
      for (int i = 0; i < 5; ++i) {
        p.Q[i] = p.K[i] = p.V[i] = f4{0, 0, 0, 0}; p.s1[i] = p.s2[i] = 0.f;
        p.xf[0][i] = *(LAS v4u*)(size_t)(xa[0] + i * 16 * 1024);
      }
      pin4(p.Q[0], p.Q[1], p.Q[2], p.Q[3]); pin4(p.K[0], p.K[1], p.K[2], p.K[3]); pin4(p.V[0], p.V[1], p.V[2], p.V[3]); pin4(p.Q[4], p.K[4], p.V[4], p.V[0]);
      asm volatile("s_nop 1");
      GAP();
      p1_steps<0>(p, X, xa, mkseq<14>{});
      if (c == 0) {
#pragma unroll
        for (int i = 0; i < 5; ++i) {
          const float mean = p.s1[i] * (1.f / D), var = fmaxf(p.s2[i] * (1.f / D) - mean * mean, 0.f);
          *(LAS f2*)(size_t)(L_ST + ((w * 5 + i) * 4 + g) * 8) = f2{mean, __builtin_amdgcn_rsqf(var + EPS)};
        }
      }
      asm volatile("s_nop 4");
      GAP();
      p1_steps<14>(p, X, xa, mkseq<2>{});
      nops();
      pin4(p.Q[0], p.Q[1], p.Q[2], p.Q[3]); pin4(p.K[0], p.K[1], p.K[2], p.K[3]); pin4(p.V[0], p.V[1], p.V[2], p.V[3]); pin4(p.Q[4], p.K[4], p.V[4], p.V[0]);
    }
    waitvm<0>();
    bar();
    stamp(2 + it * 13);
    {
      LOCALS
      f4 csq, csk;
#pragma unroll
      for (int j = 0; j < 4; ++j) { csq[j] = dpp<0x150>(p.Q[0][j]); csk[j] = dpp<0x150>(p.K[0][j]); }
      const float csv = __builtin_bit_cast(float, __builtin_amdgcn_ds_bpermute(c * 4, __float_as_int(p.V[0][0])));
      v2u qb[5], kb[5], vb[5];
#pragma unroll
      for (int rb = 0; rb < 5; ++rb) {
        const f2 st = *(LAS f2*)(size_t)(L_ST + (rb * 16 + c) * 8);
        const f4 sa = *(LAS f4*)(size_t)(L_ST + (rb * 16 + g * 4) * 8), sb = *(LAS f4*)(size_t)(L_ST + (rb * 16 + g * 4 + 2) * 8);
        const f4 kk = (p.K[rb] - st.x * csk) * st.y;
        kb[rb] = v2u{cvtpk(kk[0], kk[1]), cvtpk(kk[2], kk[3])};
        if (rb) { const f4 qq = (p.Q[rb] - st.x * csq) * st.y; qb[rb] = v2u{cvtpk(qq[0], qq[1]), cvtpk(qq[2], qq[3])}; }
        vb[rb] = v2u{cvtpk((p.V[rb][0] - sa[0] * csv) * sa[1], (p.V[rb][1] - sa[2] * csv) * sa[3]),
                     cvtpk((p.V[rb][2] - sb[0] * csv) * sb[1], (p.V[rb][3] - sb[2] * csv) * sb[3])};
      }
      f4 sp[4], sc[4];
#pragma unroll
      for (int qi = 0; qi < 4; ++qi)
        asm volatile("s_nop 1\n\tv_mfma_f32_16x16x16_bf16 %0, %2, %4, 0\n\tv_mfma_f32_16x16x16_bf16 %1, %3, %4, 0"
                     : "=&v"(sp[qi]), "=&v"(sc[qi]) : "v"(kb[qi]), "v"(kb[qi + 1]), "v"(qb[qi + 1]));
      nops();
      pin4(sp[0], sp[1], sp[2], sp[3]); pin4(sc[0], sc[1], sc[2], sc[3]);
      const int par = c & 1;
      const int d0 = 16 + c - (4 * g + par);
      const float NEG = -__builtin_inff();
      const float m0 = (d0 <= 14) ? 0.f : NEG, m1 = (d0 - 2 <= 14) ? 0.f : NEG;
      const float m2 = (d0 - 16 >= 0) ? 0.f : NEG, m3 = (d0 - 18 >= 0) ? 0.f : NEG;
      const float SC = 0.25f * 1.4426950408889634f;
      f4 ot[4]; float rs[4];
#pragma unroll
      for (int qi = 0; qi < 4; ++qi) {
        const bool first = (qi == 0) && (tb == 0);
        const float t0 = (par ? sp[qi][1] : sp[qi][0]) * SC + (first ? NEG : m0), t1 = (par ? sp[qi][3] : sp[qi][2]) * SC + (first ? NEG : m1);
        const float t2 = (par ? sc[qi][1] : sc[qi][0]) * SC + m2, t3 = (par ? sc[qi][3] : sc[qi][2]) * SC + m3;
        float mx = fmaxf(fmaxf(t0, t1), fmaxf(t2, t3));
        { auto r = __builtin_amdgcn_permlane16_swap(__float_as_uint(mx), __float_as_uint(mx), false, false); mx = fmaxf(__uint_as_float(r[0]), __uint_as_float(r[1])); }
        { auto r = __builtin_amdgcn_permlane32_swap(__float_as_uint(mx), __float_as_uint(mx), false, false); mx = fmaxf(__uint_as_float(r[0]), __uint_as_float(r[1])); }
        const float e0 = __builtin_amdgcn_exp2f(t0 - mx), e1 = __builtin_amdgcn_exp2f(t1 - mx), e2 = __builtin_amdgcn_exp2f(t2 - mx), e3 = __builtin_amdgcn_exp2f(t3 - mx);
        float sm = (e0 + e1) + (e2 + e3);
        { auto r = __builtin_amdgcn_permlane16_swap(__float_as_uint(sm), __float_as_uint(sm), false, false); sm = __uint_as_float(r[0]) + __uint_as_float(r[1]); }
        { auto r = __builtin_amdgcn_permlane32_swap(__float_as_uint(sm), __float_as_uint(sm), false, false); sm = __uint_as_float(r[0]) + __uint_as_float(r[1]); }
        rs[qi] = __builtin_amdgcn_rcpf(sm);
        const unsigned sh = par * 16;
        const v4u pf = {cvtpk(e0, 0.f) << sh, cvtpk(e1, 0.f) << sh, cvtpk(e2, 0.f) << sh, cvtpk(e3, 0.f) << sh};
        const v4u vf = {vb[qi].x, vb[qi].y, vb[qi + 1].x, vb[qi + 1].y};
        asm volatile("s_nop 1\n\tv_mfma_f32_16x16x32_bf16 %0, %1, %2, 0" : "=&v"(ot[qi]) : "v"(vf), "v"(pf));
      }
      nops();
      pin4(ot[0], ot[1], ot[2], ot[3]);
      v2u ob[4];
#pragma unroll
      for (int qi = 0; qi < 4; ++qi) { const f4 o = ot[qi] * rs[qi]; ob[qi] = v2u{cvtpk(o[0], o[1]), cvtpk(o[2], o[3])}; }
      const unsigned vo = (unsigned)((g & 1) * 16 * 1024 + ((g >> 1) * 16 + c) * 16);
      const unsigned so = WS_O + itof + (unsigned)((tb * 4 * 16 + ct * 2 + (w >> 1)) * 1024 + (w & 1) * 512);
      st16(swap_pair(ob[0], ob[1]), rws, vo, so);
      st16(swap_pair(ob[2], ob[3]), rws, vo, so + 2 * 16 * 1024);
    }
    f4 acc[16];
    {
      LOCALS
      const unsigned v1 = src_nat(lane, 3584), w1s = (unsigned)(w * 512 * 3584 + fresh_s(fb0) * 128);
      const unsigned stg = L_Z + w * 5 * 4096, stg5 = L_Z + (20 + w) * 4096;
      unsigned tN[4], t[4];
#pragma unroll
      for (int n = 0; n < 4; ++n) { tN[n] = tr_nat(lane) ^ (n << 5); t[n] = tN[n] + stg; }
      if (act3) { dma5<0, 3584>(stg, v1, v1 ^ 64, rw1, w1s); waitvm<20>(); } else waitvm<0>();
      set_flag(rws, tag, lane, fl0 + (unsigned)(tb * 8 + ct) * 128u + w * 8);
      stamp(3 + it * 13);

#pragma unroll
      for (int n = 0; n < 4; ++n) {
        const v2u r = trV(L_RES + w * 2048 + (tr_prm(lane) ^ (n << 5)));
        acc[n] = f4{bflo(r.x), bfhi(r.x), bflo(r.y), bfhi(r.y)};
      }
      if (act3) { waitvm<0>(); rd_slot5<0, 512>(t); waitlgkm(); }
      poll(rws, tag, ((lane & 31) >> 2) * 128 + (lane & 3) * 8, fl0 + (unsigned)(tb * 8) * 128u);
      stamp(4 + it * 13);
      v4u A[16];
      load_o(A, lane * 16, rws, WS_O + itof + (unsigned)((tb * 4 + w) * 16 * 1024), mkseq<16>{});
      dma5<5, 3584>(stg, v1, v1 ^ 64, rw1, w1s);
      pin4(acc[0], acc[1], acc[2], acc[3]);
#pragma unroll
      for (int n = 0; n < 4; ++n) tN[n] += L_Z + 20 * 4096;
#pragma unroll
      for (int ks = 0; ks < 16; ++ks) {
        v4u bf[4];
#pragma unroll
        for (int n = 0; n < 4; ++n) {
          const unsigned a = tN[n] + ks * 4096;
          const v2u lo = trV(a), hi = trV(a + 512);
          bf[n] = v4u{lo.x, lo.y, hi.x, hi.y};
        }
        if (ks == 0) wait4<32>(A[0], A[1], A[2], A[3]);
        if (ks == 4) wait4<28>(A[4], A[5], A[6], A[7]);
        if (ks == 8) wait4<24>(A[8], A[9], A[10], A[11]);
        if (ks == 12) wait4<20>(A[12], A[13], A[14], A[15]);
#pragma unroll
        for (int n = 0; n < 4; ++n) mf_vv(acc[n], A[ks], bf[n]);
      }
      nops();
      pin4(acc[0], acc[1], acc[2], acc[3]);
      stamp(5 + it * 13);
      f4 xr[4], s1 = {0, 0, 0, 0}, s2 = {0, 0, 0, 0};
#pragma unroll
      for (int n = 0; n < 4; ++n) {
        const v2u pk = {cvtpk(acc[n][0], acc[n][1]), cvtpk(acc[n][2], acc[n][3])};
        *(LAS v2u*)(size_t)(L_RES + w * 2048 + (n * 16 + c) * 32 + g * 8) = pk;
        xr[n] = f4{bflo(pk.x), bfhi(pk.x), bflo(pk.y), bfhi(pk.y)};
        s1 += xr[n]; s2 += xr[n] * xr[n];
      }
#pragma unroll
      for (int j = 0; j < 4; ++j) {
        s1[j] += dpp<0x121>(s1[j]); s2[j] += dpp<0x121>(s2[j]);
        s1[j] += dpp<0x122>(s1[j]); s2[j] += dpp<0x122>(s2[j]);
        s1[j] += dpp<0x124>(s1[j]); s2[j] += dpp<0x124>(s2[j]);
        s1[j] += dpp<0x128>(s1[j]); s2[j] += dpp<0x128>(s2[j]);
      }
      const unsigned sst = WS_STAT + (unsigned)(((it * 32 + tb) * 4 + w) * 1024);
      {
        v4u v = (c & 1) ? __builtin_bit_cast(v4u, f4{s1[2], s2[2], s1[3], s2[3]}) : __builtin_bit_cast(v4u, f4{s1[0], s2[0], s1[1], s2[1]});
#pragma unroll
        for (int e = 0; e < 4; ++e) v[e] = v[e] == ~0u ? 0x7fc00000u : v[e];
        if (c < 2) st16(v, rws, (unsigned)((2 * g + c) * 16), sst + ct * 128);
      }
      bar();
      waitvm<1>();
      if (act3) rd_slot5<5, 512>(t);
      v4u sv;
      {
        const unsigned vo = (unsigned)((c >> 1) * 128 + (2 * g + (c & 1)) * 16);
        for (unsigned spin = 0; spin < SPIN_CAP; ++spin) {
          asm volatile("buffer_load_dwordx4 %0, %1, %2, %3 offen sc1\n\ts_waitcnt vmcnt(0)" : "=&v"(sv) : "v"(vo), "s"(rws), "s"(sst) : "memory");
          if (__all(sv.x != ~0u && sv.y != ~0u && sv.z != ~0u && sv.w != ~0u)) break;
        }
      }
      stamp(6 + it * 13);
      f4 tt = __builtin_bit_cast(f4, sv);
#pragma unroll
      for (int e = 0; e < 4; ++e) { tt[e] += dpp<0x122>(tt[e]); tt[e] += dpp<0x124>(tt[e]); tt[e] += dpp<0x128>(tt[e]); }
      float rr[2], mm[2];
#pragma unroll
      for (int e = 0; e < 2; ++e) {
        const float mean = tt[2 * e] * (1.f / D), var = fmaxf(tt[2 * e + 1] * (1.f / D) - mean * mean, 0.f);
        const float r1 = __builtin_amdgcn_rsqf(var + EPS);
        const float var2 = var * r1 * r1;
        rr[e] = r1 * __builtin_amdgcn_rsqf(var2 + EPS);
        mm[e] = -mean * rr[e];
      }
      const f4 r4 = {dpp<0xA0>(rr[0]), dpp<0xA0>(rr[1]), dpp<0xF5>(rr[0]), dpp<0xF5>(rr[1])};
      const f4 m4 = {dpp<0xA0>(mm[0]), dpp<0xA0>(mm[1]), dpp<0xF5>(mm[0]), dpp<0xF5>(mm[1])};
      v2u hb[4];
#pragma unroll
      for (int n = 0; n < 4; ++n) { const f4 h = xr[n] * r4 + m4; hb[n] = v2u{cvtpk(h[0], h[1]), cvtpk(h[2], h[3])}; }
      const unsigned vo = (unsigned)((g & 1) * 1024 + ((g >> 1) * 16 + c) * 16);
      const unsigned sh = WS_H + itof + (unsigned)((ct * 64 + tb * 2 + (w >> 1)) * 4096 + (w & 1) * 512);
      st16(swap_pair(hb[0], hb[1]), rws, vo, sh);
      st16(swap_pair(hb[2], hb[3]), rws, vo, sh + 2048);
      if (act3) {
        waitlgkm();
        dma5<10, 3584>(stg, v1, v1 ^ 64, rw1, w1s);
        dma_slot<0, 15 * 32 * 3584, 3584>(stg5, v1, v1 ^ 64, rw1, w1s);
        waitvm<24>();
      } else waitvm<0>();
      set_flag(rws, tag, lane, fl0 + (unsigned)(256 + tb * 8 + ct) * 128u + w * 8);
      stamp(7 + it * 13);
      if (act3) {
        waitvm<0>();
        rd_slot5<10, 512>(t);
#pragma unroll
        for (int n = 0; n < 4; ++n) t[n] += stg5 - stg;
        rd_slot<15, 0, 512>(t);
      }
    }
    {
      LOCALS
      const int db = fresh_s(db0), fb = fresh_s(fb0);
      const unsigned stg = L_Z + w * 5 * 4096, stg5 = L_Z + (20 + w) * 4096, tmp = L_Z + (24 + w * 3) * 4096;
      const HookW2 hw{stg, stg5, src_prm(lane, 4096), rw2, (unsigned)(w * 448 * 4096 + tb * 128)};
      if (act3) {
#pragma unroll
        for (int i = 0; i < 4; ++i) {
          acc[4 * i] = f4{bflo(b1v[i].x), bfhi(b1v[i].x), bflo(b1v[i].y), bfhi(b1v[i].y)};
          acc[4 * i + 1] = acc[4 * i + 2] = acc[4 * i + 3] = f4{0, 0, 0, 0};
        }
        unsigned vo[4];
#pragma unroll
        for (int m = 0; m < 4; ++m) vo[m] = lane * 16 + ((m + w) & 3) * 1024;
        waitlgkm();
        poll(rws, tag, ((lane & 31) >> 2) * 1024 + (lane & 3) * 8, fl0 + (unsigned)(256 + w * 64 + db) * 128u);
        stamp(8 + it * 13);
        big_loop<16, true>(acc, vo, rws, WS_H + itof + (unsigned)((db * 64 + w * 16) * 4096), hw, mkseq<16>{});
        stamp(9 + it * 13);
        reduce4(lane, w, acc);
        v2u ub[4];
#pragma unroll
        for (int i = 0; i < 4; ++i) ub[i] = v2u{cvtpk(gelu(acc[4 * i][0]), gelu(acc[4 * i][1])), cvtpk(gelu(acc[4 * i][2]), gelu(acc[4 * i][3]))};
        const unsigned su = WS_U + itof + (unsigned)(((db * 56 + fb * 2) * 4 + w) * 1024);
        st16(v4u{ub[0].x, ub[0].y, ub[1].x, ub[1].y}, rws, lane * 16, su);
        st16(v4u{ub[2].x, ub[2].y, ub[3].x, ub[3].y}, rws, lane * 16, su + 4096);
        waitvm<2>();
      } else {
        hw.run<0>(); hw.run<1>(); hw.run<2>(); hw.run<3>(); hw.run<4>(); hw.run<5>();
        waitvm<0>();
      }
      unsigned t[4], t5[4];
#pragma unroll
      for (int n = 0; n < 4; ++n) { const unsigned tp = tr_prm(lane) ^ (((n + w) & 3) << 5); t[n] = tp + stg; t5[n] = tp + stg5; }
      rd_slot5<0, 2048>(t); rd_slot<5, 0, 2048>(t5);
      waitlgkm();
      dma_slot<0, 12 * 32 * 4096, 4096>(tmp, hw.v, hw.v, rw2, hw.sb);
      dma_slot<4096, 13 * 32 * 4096, 4096>(tmp, hw.v, hw.v, rw2, hw.sb);
      waitvm<8>();
      if (act3) set_flag(rws, tag, lane, fl0 + (unsigned)(512 + db * 32 + fb) * 128u + w * 8);
      dma5<6, 4096>(stg, hw.v, hw.v, rw2, hw.sb);
      dma_slot<0, 11 * 32 * 4096, 4096>(stg5, hw.v, hw.v, rw2, hw.sb);
      stamp(10 + it * 13);
      const float b2v = bflo(b2u);
#pragma unroll
      for (int m = 0; m < 4; ++m) {
        const v2u r = trV(L_RES + w * 2048 + m * 512 + lane * 8);
        acc[4 * m] = f4{bflo(r.x) + b2v, bfhi(r.x) + b2v, bflo(r.y) + b2v, bfhi(r.y) + b2v};
        acc[4 * m + 1] = acc[4 * m + 2] = acc[4 * m + 3] = f4{0, 0, 0, 0};
      }
      waitvm<0>();
      rd_slot5<6, 2048>(t); rd_slot<11, 0, 2048>(t5);
#pragma unroll
      for (int n = 0; n < 4; ++n) t[n] += tmp - stg;
      rd_slot<12, 0, 2048>(t); rd_slot<13, 4096, 2048>(t);
    }
    bar();
    {
      LOCALS
      unsigned vo[4];
#pragma unroll
      for (int m = 0; m < 4; ++m) vo[m] = lane * 16 + m * 1024;
      const HookQkv hq{L_Z + w * 12288u, src_nat(lane, 3072), rqkv, w * 32u * 3072u + ct * 128u};
      const int l = lane < 28 ? lane : (lane < 56 ? lane - 28 : lane - 56);
      poll(rws, tag, (l >> 2) * 128 + (l & 3) * 8, fl0 + (unsigned)(512 + ct * 32 + w * 7) * 128u);
      stamp(11 + it * 13);
      big_loop<14, false>(acc, vo, rws, WS_U + itof + (unsigned)((ct * 56 + w * 14) * 4096), hq, mkseq<14>{});
      reduce4(lane, w, acc);
      v2u yb[4];
#pragma unroll
      for (int m = 0; m < 4; ++m) {
        yb[m] = v2u{cvtpk(acc[4 * m][0], acc[4 * m][1]), cvtpk(acc[4 * m][2], acc[4 * m][3])};
        *(LAS v2u*)(size_t)(L_RES + w * 2048 + c * 128 + ((m ^ ((c >> 1) & 3)) * 32) + g * 8) = yb[m];
      }
      const unsigned vx = (unsigned)(c * 1024 + (g & 1) * 32 + (g >> 1) * 16);
      const unsigned sx = (unsigned)((tb * 64 + w * 16) * 1024 + ct * 128);
      const rsrc_t ro = it == 0 ? rx2 : rout;
      st16(swap_pair(yb[0], yb[1]), ro, vx, sx);
      st16(swap_pair(yb[2], yb[3]), ro, vx, sx + 64);
      waitlgkm();
    }
  }
  if (blockIdx.x == 0 && threadIdx.x == 0) __builtin_amdgcn_raw_buffer_store_b64(v2u{tag.lo, tag.hi}, rws, 0, 0, 16);
  waitvm<0>();
}

extern "C" void kernel_launch(void* const* d_in, const int* in_sizes, int n_in, void* d_out, int out_size, void* d_ws, size_t ws_size,
                              hipStream_t stream) {
  fused_kernel<<<256, 256, 0, stream>>>((const u16*)d_in[0], (const u16*)d_in[1], (const u16*)d_in[2], (const u16*)d_in[4], (const u16*)d_in[5],
                                        (const u16*)d_in[6], (const u16*)d_in[7], (u16*)d_out, (char*)d_ws);
}
```

```cpp
#include <hip/hip_runtime.h>
typedef unsigned short u16;

__global__ void __launch_bounds__(256, 1)
fused_kernel(const u16* x0, const u16* wqkv, const u16* wo, const u16* w1, const u16* b1, const u16* w2, const u16* b2, u16* out, char* ws) {
  __shared__ __attribute__((aligned(16))) char smem[163840];
#if defined(__HIP_DEVICE_COMPILE__)
  asm volatile(
R"ASM(
s_load_dwordx16 s[36:51], %0, 0
s_load_dwordx2 s[52:53], %0, 64
s_getpc_b64 s[68:69]
.LPC_%=:
s_lshr_b32 s70, %1, 3
s_lshl_b32 s70, s70, 11
v_lshlrev_b32 v246, 3, %2
v_add_u32 v246, s70, v246
v_min_u32 v246, .LDONE_%=-.LPC_%=-4, v246
global_load_dword v77, v246, s[68:69]
s_lshl_b32 s70, %1, 13
v_lshl_add_u32 v236, %2, 5, s70
s_mul_i32 s70, %1, 6144
v_mul_u32_u24 v238, 24, %2
v_add_u32 v238, s70, v238
s_waitcnt lgkmcnt(0)
global_load_dword v77, v238, s[38:39]
global_load_dword v77, v236, s[36:37]
v_lshrrev_b32 v18, 6, %2
v_and_b32 v0, 63, %2
v_readfirstlane_b32 s54, v18
s_mov_b32 s73, %1
s_and_b32 s70, s73, 7
s_lshr_b32 s71, s73, 3
s_lshr_b32 s72, s70, 1
s_lshl_b32 s55, s72, 3
s_and_b32 s74, s71, 7
s_or_b32 s55, s55, s74
s_and_b32 s70, s70, 1
s_lshl_b32 s70, s70, 2
s_lshr_b32 s74, s71, 3
s_or_b32 s56, s70, s74
s_lshl_b32 s97, s71, 2
s_add_u32 s97, s97, s54
s_mov_b32 s57, s56
s_mul_i32 s58, s72, 7
s_mul_i32 s60, s54, 36864
s_mov_b32 s59, 0
s_mov_b32 s76, 262144
v_lshlrev_b32 v1, 4, v0
v_lshrrev_b32 v24, 4, v0
v_and_b32 v25, 15, v0
s_lshl_b32 s91, s55, 16
s_sub_i32 s90, s91, 16384
s_max_i32 s90, s90, 0
v_lshrrev_b32 v19, 3, v0
v_and_b32 v20, 7, v0
v_and_b32 v21, 2, v19
v_xor_b32 v21, v21, v20
v_lshlrev_b32 v21, 4, v21
v_and_b32 v22, 6, v19
v_xor_b32 v22, v22, v20
v_lshlrev_b32 v22, 4, v22
v_mul_u32_u24 v36, 3072, v19
v_add_u32 v36, v36, v21
v_xor_b32 v37, 64, v36
v_add_u32 v38, 47104, v36
v_add_u32 v39, 70656, v37
v_add_u32 v37, 23552, v37
s_mul_i32 s77, s54, 98304
s_lshl_b32 s71, s56, 7
s_add_u32 s77, s77, s71
s_mov_b32 s12, s36
s_and_b32 s13, s37, 0xffff
s_mov_b32 s14, 2097152
s_mov_b32 s15, 0x20000
s_mov_b32 s16, s38
s_and_b32 s17, s39, 0xffff
s_mov_b32 s18, 1572864
s_mov_b32 s19, 0x20000
s_lshr_b32 s98, s16, 7
s_sub_u32 s98, 3, s98
s_and_b32 s98, s98, 7
s_xor_b32 s99, s98, s56
s_and_b32 s99, s99, 4
s_lshl_b32 s99, s99, 28
s_lshl_b32 s98, s98, 7
s_or_b32 s99, s99, s98
s_mul_i32 s98, s97, 3072
v_bfe_u32 v236, v0, 2, 2
v_mul_u32_u24 v236, 384, v236
v_and_b32 v237, 3, v0
v_min_u32 v237, 2, v237
v_add_lshl_u32 v236, v236, v237, 10
v_or_b32 v236, s99, v236
s_bfm_b64 exec, 16, 0
buffer_load_dword v77, v236, s[16:19], s98 offen
s_mov_b64 exec, -1
s_lshr_b32 s72, s46, 7
s_sub_u32 s72, 3, s72
s_and_b32 s72, s72, 7
s_and_b32 s70, s55, 7
s_add_u32 s58, s58, s70
s_cmp_gt_u32 s70, s72
s_subb_u32 s58, s58, 0
s_cmp_lg_u32 s70, s72
s_cselect_b32 s72, -1, 0
s_and_b32 s58, s58, s72
s_mov_b32 s24, s42
s_and_b32 s25, s43, 0xffff
s_mov_b32 s26, 7340032
s_mov_b32 s27, 0x20000
s_mov_b32 s20, s40
s_and_b32 s21, s41, 0xffff
s_mov_b32 s22, 524288
s_mov_b32 s23, 0x20000
s_and_b32 s43, %1, 1
s_lshl_b32 s43, s43, 9
s_lshr_b32 s70, s97, 5
s_and_b32 s73, s97, 31
s_bfe_u32 s99, %1, 0x20001
s_lshl_b32 s66, s70, 9
s_add_u32 s66, s66, s73
s_mul_i32 s66, s66, 3584
s_mul_i32 s98, s99, 896
s_add_u32 s66, s66, s98
s_mul_i32 s67, s70, 448
s_add_u32 s67, s67, s73
s_lshl_b32 s67, s67, 12
s_lshl_b32 s98, s99, 10
s_add_u32 s67, s67, s98
v_lshrrev_b32 v236, 3, v0
v_min_u32 v236, 1, v236
v_mul_u32_u24 v236, 1792, v236
v_and_b32 v237, 7, v0
v_min_u32 v237, 6, v237
v_add_lshl_u32 v236, v236, v237, 7
s_mul_i32 s98, s43, 224
s_add_u32 s98, s98, s66
s_add_u32 s98, s98, 1376256
s_bfm_b64 exec, 16, 0
buffer_load_dword v77, v236, s[24:27], s98 offen
s_mov_b64 exec, -1
s_lshl_b32 s70, %1, 2
s_add_u32 s70, s70, s54
s_mul_i32 s70, s70, 4
v_min_u32 v240, 3, v0
v_add_u32 v240, s70, v240
v_lshlrev_b32 v240, 7, v240
v_min_u32 v240, 524160, v240
s_bfm_b64 exec, 4, 0
global_load_dword v77, v240, s[20:21]
s_mov_b64 exec, -1
v_mov_b32 v244, 0
global_load_dwordx2 v[254:255], v244, s[52:53] sc1
s_lshl_b32 s70, s58, 7
s_add_u32 s44, s44, s70
s_addc_u32 s45, s45, 0
s_lshl_b32 s70, s55, 7
s_add_u32 s48, s48, s70
s_addc_u32 s49, s49, 0
v_mov_b32 v240, s44
v_mov_b32 v241, s45
v_mov_b32 v242, s48
v_mov_b32 v243, s49
v_cmp_lt_u32 vcc, 31, v0
s_nop 1
v_cndmask_b32 v240, v240, v242, vcc
v_cndmask_b32 v241, v241, v243, vcc
v_and_b32 v242, 31, v0
v_lshlrev_b32 v242, 2, v242
v_add_co_u32 v240, vcc, v240, v242
s_nop 1
v_addc_co_u32 v241, vcc, 0, v241, vcc
s_lshl_b32 s70, s54, 8
s_add_u32 m0, s70, 158720
s_nop 0
global_load_lds_dword v[240:241], off
v_lshrrev_b32 v62, 1, v19
v_xor_b32 v62, v62, v20
v_lshlrev_b32 v62, 4, v62
v_lshl_or_b32 v62, v19, 10, v62
s_lshr_b32 s70, s54, 1
s_mul_i32 s70, s70, 8256
s_and_b32 s73, s54, 1
s_lshl_b32 s73, s73, 7
s_or_b32 s70, s70, s73
v_xor_b32 v62, s70, v62
s_mov_b32 s4, s52
s_and_b32 s5, s53, 0xffff
s_mov_b32 s6, 18350080
s_mov_b32 s7, 0x20000
s_mov_b32 s8, s52
s_and_b32 s9, s53, 0xffff
s_mov_b32 s10, 18350080
s_mov_b32 s11, 0x20000
s_and_b32 s10, s10, s72
s_andn2_b32 s72, 0x70000000, s72
s_mov_b32 s28, s46
s_and_b32 s29, s47, 0xffff
s_mov_b32 s30, 7340032
s_mov_b32 s31, 0x20000
s_add_u32 s32, s52, 15204352
s_addc_u32 s33, s53, 0
s_and_b32 s33, s33, 0xffff
s_mov_b32 s34, 2097152
s_mov_b32 s35, 0x20000
v_mul_u32_u24 v40, 3584, v19
v_add_u32 v40, v40, v21
v_xor_b32 v41, 64, v40
v_add_u32 v42, 55296, v40
v_add_u32 v43, 82944, v41
v_add_u32 v41, 27648, v41
v_mul_u32_u24 v44, 4096, v19
v_add_u32 v44, v44, v21
v_xor_b32 v45, 64, v44
v_add_u32 v46, 63488, v44
v_add_u32 v47, 95232, v45
v_add_u32 v45, 31744, v45
v_mul_u32_u24 v48, 1024, v19
v_add_u32 v48, v48, v21
v_xor_b32 v49, 64, v48
v_add_u32 v50, 14336, v48
v_add_u32 v51, 21504, v49
v_add_u32 v49, 7168, v49
v_or_b32 v40, s72, v40
v_or_b32 v41, s72, v41
v_or_b32 v42, s72, v42
s_add_u32 m0, s60, 8192
s_add_u32 s74, s77, 0
buffer_load_dwordx4 v36, s[16:19], s74 offen offset:0 lds
v_or_b32 v43, s72, v43
v_lshl_or_b32 v234, v19, 10, v22
s_lshl_b32 s78, s54, 17
s_add_u32 s78, s78, s71
s_mul_i32 s79, s54, 1835008
s_lshl_b32 s70, s58, 7
s_add_u32 s79, s79, s70
s_mul_i32 s80, s54, 1835008
s_lshl_b32 s70, s55, 7
s_add_u32 s80, s80, s70
s_lshl_b32 s81, s55, 16
s_lshl_b32 s70, s54, 14
s_add_u32 s86, s81, s70
s_lshl_b32 s82, s86, 0
s_add_u32 s82, s82, 1048576
s_add_u32 s86, s86, s71
s_lshl_b32 s70, s56, 11
s_add_u32 s81, s81, s70
s_lshl_b32 s70, s54, 9
s_add_u32 s81, s81, s70
s_lshl_b32 s81, s81, 0
s_add_u32 s81, s81, 1048576
s_lshl_b32 s83, s56, 2
s_lshr_b32 s70, s55, 3
s_add_u32 s83, s83, s70
s_mul_i32 s83, s83, 65536
s_and_b32 s70, s55, 7
s_lshl_b32 s70, s70, 13
s_add_u32 s83, s83, s70
s_lshr_b32 s70, s54, 1
s_lshl_b32 s70, s70, 12
s_add_u32 s83, s83, s70
s_and_b32 s70, s54, 1
s_lshl_b32 s70, s70, 9
s_add_u32 s83, s83, s70
s_add_u32 s83, s83, 5767168
s_mul_i32 s84, s57, 229376
s_lshl_b32 s70, s58, 13
s_add_u32 s84, s84, s70
s_lshr_b32 s70, s54, 1
buffer_load_dwordx4 v37, s[16:19], s74 offen offset:1024 lds
s_lshl_b32 s70, s70, 12
s_add_u32 s84, s84, s70
s_and_b32 s70, s54, 1
s_lshl_b32 s70, s70, 9
s_add_u32 s84, s84, s70
s_lshr_b32 s70, s71, 1
s_sub_u32 s93, s86, s70
s_lshl_b32 s84, s84, 0
s_add_u32 s84, s84, 10485760
s_mul_i32 s85, s56, 229376
s_mul_i32 s70, s54, 57344
s_add_u32 s85, s85, s70
s_lshl_b32 s85, s85, 0
s_add_u32 s85, s85, 10485760
s_lshl_b32 s89, s55, 12
s_lshl_b32 s70, s54, 10
s_add_u32 s87, s89, s70
s_add_u32 s88, s87, s71
s_add_u32 s88, s88, 393216
s_add_u32 s87, s87, 131072
s_add_u32 s89, s89, 392192
s_lshl_b32 s92, s57, 2
s_add_u32 s92, s92, s54
s_mul_i32 s92, s92, 65536
s_add_u32 s92, s92, 5767168
v_bfe_u32 v18, v0, 2, 2
v_and_b32 v19, 3, v0
v_bfe_u32 v20, v0, 3, 2
v_lshlrev_b32 v21, 3, v19
v_lshl_or_b32 v21, v20, 5, v21
v_lshl_or_b32 v21, v18, 7, v21
v_lshl_or_b32 v26, v24, 10, v21
v_lshl_or_b32 v72, v24, 9, v21
s_lshl_b32 s70, s54, 5
v_xor_b32 v30, s70, v26
v_add_u32 v31, 36864, v30
v_add_u32 v32, 73728, v30
v_add_u32 v33, 110592, v30
v_add_u32 v21, s60, v26
s_add_u32 s70, s54, 0
buffer_load_dwordx4 v38, s[16:19], s74 offen offset:2048 lds
s_and_b32 s70, s70, 3
s_lshl_b32 s70, s70, 5
v_xor_b32 v26, s70, v21
s_add_u32 s70, s54, 1
s_and_b32 s70, s70, 3
s_lshl_b32 s70, s70, 5
v_xor_b32 v27, s70, v21
s_add_u32 s70, s54, 2
s_and_b32 s70, s70, 3
s_lshl_b32 s70, s70, 5
v_xor_b32 v28, s70, v21
s_add_u32 s70, s54, 3
s_and_b32 s70, s70, 3
s_lshl_b32 s70, s70, 5
v_xor_b32 v29, s70, v21
v_mov_b32 v34, 0xc0135761
v_mov_b32 v35, 0xc0135761
s_mov_b32 s40, 0xbdd2d3e8
s_mov_b32 s41, 0xbdd2d3e8
s_lshl_b32 s70, s54, 11
s_add_u32 s70, s70, 147456
v_add_u32 v72, s70, v72
v_lshrrev_b32 v63, 1, v25
v_xor_b32 v63, v63, v24
v_lshlrev_b32 v63, 4, v63
v_and_b32 v21, 7, v0
v_lshl_or_b32 v63, v21, 7, v63
v_bfe_u32 v21, v0, 3, 1
v_mul_u32_u24 v21, 73728, v21
v_add_u32 v63, v63, v21
v_xor_b32 v64, 64, v63
v_lshrrev_b32 v18, 1, v24
v_and_b32 v19, 1, v24
v_lshlrev_b32 v20, 4, v25
v_lshl_or_b32 v20, v18, 8, v20
v_lshl_or_b32 v65, v19, 14, v20
v_lshl_or_b32 v66, v19, 10, v20
v_lshlrev_b32 v67, 10, v25
v_lshl_or_b32 v67, v19, 5, v67
v_lshl_or_b32 v67, v18, 4, v67
buffer_load_dwordx4 v39, s[16:19], s74 offen offset:3072 lds
v_lshlrev_b32 v68, 2, v25
v_add_u32 v68, 155648, v68
v_lshlrev_b32 v69, 4, v24
v_add_u32 v69, 155648, v69
v_and_b32 v18, 1, v25
v_lshlrev_b32 v71, 4, v18
v_lshl_or_b32 v71, v24, 5, v71
v_lshrrev_b32 v19, 1, v25
v_lshl_or_b32 v70, v19, 7, v71
v_cmp_eq_u32 s[64:65], 1, v18
v_lshlrev_b32 v19, 2, v24
v_sub_u32 v19, v25, v19
v_sub_u32 v19, v19, v18
v_mov_b32 v20, 0xff800000
v_cmp_lt_i32 vcc, -2, v19
s_nop 1
v_cndmask_b32 v54, 0, v20, vcc
v_cmp_lt_i32 vcc, 0, v19
s_nop 1
v_cndmask_b32 v55, 0, v20, vcc
v_cmp_gt_i32 vcc, 0, v19
s_nop 1
v_cndmask_b32 v56, 0, v20, vcc
v_cmp_gt_i32 vcc, 2, v19
s_nop 1
v_cndmask_b32 v57, 0, v20, vcc
s_cmp_eq_u32 s55, 0
s_cselect_b32 s70, 0xff800000, 0
v_or_b32 v58, s70, v54
v_or_b32 v59, s70, v55
v_mov_b32 v60, 0x0c0c0100
v_mov_b32 v20, 0x01000c0c
v_cndmask_b32 v60, v60, v20, s[64:65]
v_mov_b32 v61, 0x0c0c0302
v_mov_b32 v20, 0x03020c0c
v_cndmask_b32 v61, v61, v20, s[64:65]
v_mov_b32 v10, 0x3f803f80
v_mov_b32 v11, 0x3f803f80
v_mov_b32 v12, 0x3f803f80
v_mov_b32 v13, 0x3f803f80
s_add_u32 s70, s54, 0
s_and_b32 s70, s70, 3
s_mul_i32 s70, s70, 36864
v_add_u32 v73, s70, v1
s_add_u32 m0, s60, 12288
s_add_u32 s74, s77, 1024
buffer_load_dwordx4 v36, s[16:19], s74 offen offset:0 lds
s_add_u32 s70, s54, 1
s_and_b32 s70, s70, 3
s_mul_i32 s70, s70, 36864
v_add_u32 v74, s70, v1
s_add_u32 s70, s54, 2
s_and_b32 s70, s70, 3
s_mul_i32 s70, s70, 36864
v_add_u32 v75, s70, v1
s_add_u32 s70, s54, 3
s_and_b32 s70, s70, 3
s_mul_i32 s70, s70, 36864
v_add_u32 v76, s70, v1
v_mov_b32 v228, -1
v_mov_b32 v229, -1
v_mov_b32 v230, -1
v_mov_b32 v231, -1
v_lshrrev_b32 v232, 3, v0
v_lshlrev_b32 v232, 17, v232
v_and_b32 v233, 0x70, v1
v_or_b32 v232, v232, v233
v_mov_b32 v233, 0x7ffffff0
v_cmp_gt_u32 vcc, 24, v0
s_nop 1
v_cndmask_b32 v232, v233, v232, vcc
s_lshl_b32 s70, s55, 3
s_add_u32 s70, s70, s56
s_lshl_b32 s70, s70, 7
s_lshl_b32 s71, s57, 5
s_add_u32 s71, s71, s58
s_lshl_b32 s71, s71, 7
s_add_u32 s70, s70, 4096
s_add_u32 s71, s71, 36864
s_cmp_eq_u32 s10, 0
s_cselect_b32 s71, 0x7ffffff0, s71
s_cmp_lg_u32 s54, 0
s_cselect_b32 s71, 0x7ffffff0, s71
s_cselect_b32 s70, 0x7ffffff0, s70
v_mov_b32 v252, 0x7ffffff0
v_writelane_b32 v252, s70, 0
v_writelane_b32 v252, s71, 1
s_add_u32 s98, s24, s66
s_lshr_b32 s98, s98, 7
s_sub_u32 s98, 3, s98
s_and_b32 s98, s98, 7
s_min_u32 s98, s98, 6
s_lshl_b32 s98, s98, 7
s_add_u32 s98, s98, s66
v_and_b32 v236, 15, v0
v_mul_u32_u24 v236, 114688, v236
s_bfm_b64 exec, 16, 0
buffer_load_dword v77, v236, s[24:27], s98 offen
s_mov_b64 exec, -1
s_lshr_b32 s98, s20, 7
s_sub_u32 s98, 3, s98
s_and_b32 s98, s98, 7
s_xor_b32 s99, s98, s56
s_and_b32 s99, s99, 4
s_lshl_b32 s99, s99, 28
s_lshl_b32 s98, s98, 7
s_or_b32 s99, s99, s98
s_lshl_b32 s98, s97, 12
v_and_b32 v236, 3, v0
v_lshlrev_b32 v236, 10, v236
v_or_b32 v236, s99, v236
s_bfm_b64 exec, 4, 0
buffer_load_dword v77, v236, s[20:23], s98 offen
s_mov_b64 exec, -1
buffer_load_dwordx4 v37, s[16:19], s74 offen offset:1024 lds
buffer_load_dwordx4 v38, s[16:19], s74 offen offset:2048 lds
buffer_load_dwordx4 v39, s[16:19], s74 offen offset:3072 lds
s_add_u32 m0, s60, 16384
s_add_u32 s74, s77, 2048
buffer_load_dwordx4 v36, s[16:19], s74 offen offset:0 lds
buffer_load_dwordx4 v37, s[16:19], s74 offen offset:1024 lds
buffer_load_dwordx4 v38, s[16:19], s74 offen offset:2048 lds
buffer_load_dwordx4 v39, s[16:19], s74 offen offset:3072 lds
)ASM"
R"ASM(
s_add_u32 m0, s60, 0
s_nop 0
buffer_load_dwordx4 v62, s[12:15], s90 offen offset:0 sc1 lds
s_add_u32 s75, s91, 0
s_add_u32 m0, s60, 1024
s_nop 0
buffer_load_dwordx4 v62, s[12:15], s75 offen offset:0 sc1 lds
s_add_u32 s75, s91, 16384
s_add_u32 m0, s60, 2048
s_nop 0
buffer_load_dwordx4 v62, s[12:15], s75 offen offset:0 sc1 lds
s_add_u32 s75, s91, 32768
s_add_u32 m0, s60, 3072
s_nop 0
buffer_load_dwordx4 v62, s[12:15], s75 offen offset:0 sc1 lds
s_add_u32 s75, s91, 49152
s_add_u32 m0, s60, 4096
s_nop 0
buffer_load_dwordx4 v62, s[12:15], s75 offen offset:0 sc1 lds
s_add_u32 m0, s60, 20480
s_add_u32 s74, s77, 393216
buffer_load_dwordx4 v36, s[16:19], s74 offen offset:0 lds
buffer_load_dwordx4 v37, s[16:19], s74 offen offset:1024 lds
buffer_load_dwordx4 v38, s[16:19], s74 offen offset:2048 lds
buffer_load_dwordx4 v39, s[16:19], s74 offen offset:3072 lds
s_add_u32 m0, s60, 24576
s_add_u32 s74, s77, 394240
buffer_load_dwordx4 v36, s[16:19], s74 offen offset:0 lds
buffer_load_dwordx4 v37, s[16:19], s74 offen offset:1024 lds
buffer_load_dwordx4 v38, s[16:19], s74 offen offset:2048 lds
buffer_load_dwordx4 v39, s[16:19], s74 offen offset:3072 lds
s_add_u32 m0, s60, 28672
s_add_u32 s74, s77, 395264
buffer_load_dwordx4 v36, s[16:19], s74 offen offset:0 lds
buffer_load_dwordx4 v37, s[16:19], s74 offen offset:1024 lds
buffer_load_dwordx4 v38, s[16:19], s74 offen offset:2048 lds
buffer_load_dwordx4 v39, s[16:19], s74 offen offset:3072 lds
v_and_b32 v246, 56, v0
v_lshlrev_b32 v246, 1, v246
s_lshr_b32 s70, s54, 1
s_lshl_b32 s70, s70, 7
s_and_b32 s71, s54, 1
s_lshl_b32 s71, s71, 3
s_add_u32 s70, s70, s71
s_add_u32 s70, s70, 156672
v_add_u32 v246, s70, v246
s_mov_b32 s42, %1
s_mov_b32 s49, 0
s_waitcnt vmcnt(32)
v_add_co_u32 v254, vcc, 1, v254
s_nop 1
v_addc_co_u32 v255, vcc, 0, v255, vcc
v_readfirstlane_b32 s62, v254
v_readfirstlane_b32 s63, v255
v_mov_b32 v216, 0
v_mov_b32 v217, 0
v_mov_b32 v218, 0
v_mov_b32 v219, 0
v_mov_b32 v220, 0
v_mov_b32 v221, 0
v_mov_b32 v222, 0
v_mov_b32 v223, 0
v_mov_b32 v224, 0
v_mov_b32 v225, 0
s_waitcnt vmcnt(12)
s_barrier
ds_read_b128 v[96:99], v73 offset:0
ds_read_b128 v[100:103], v73 offset:1024
ds_read_b128 v[104:107], v73 offset:2048
ds_read_b128 v[108:111], v73 offset:3072
ds_read_b128 v[112:115], v73 offset:4096
ds_read_b64_tr_b16 a[64:65], v30 offset:8192
ds_read_b64_tr_b16 a[66:67], v30 offset:8704
ds_read_b64_tr_b16 a[76:77], v31 offset:8192
ds_read_b64_tr_b16 a[78:79], v31 offset:8704
ds_read_b64_tr_b16 a[88:89], v32 offset:8192
ds_read_b64_tr_b16 a[90:91], v32 offset:8704
ds_read_b64_tr_b16 a[100:101], v33 offset:8192
ds_read_b64_tr_b16 a[102:103], v33 offset:8704
ds_read_b64_tr_b16 a[68:69], v30 offset:12288
ds_read_b64_tr_b16 a[70:71], v30 offset:12800
ds_read_b64_tr_b16 a[80:81], v31 offset:12288
ds_read_b64_tr_b16 a[82:83], v31 offset:12800
ds_read_b64_tr_b16 a[92:93], v32 offset:12288
ds_read_b64_tr_b16 a[94:95], v32 offset:12800
ds_read_b64_tr_b16 a[104:105], v33 offset:12288
ds_read_b64_tr_b16 a[106:107], v33 offset:12800
ds_read_b64_tr_b16 a[72:73], v30 offset:16384
ds_read_b64_tr_b16 a[74:75], v30 offset:16896
ds_read_b64_tr_b16 a[84:85], v31 offset:16384
ds_read_b64_tr_b16 a[86:87], v31 offset:16896
ds_read_b64_tr_b16 a[96:97], v32 offset:16384
ds_read_b64_tr_b16 a[98:99], v32 offset:16896
ds_read_b64_tr_b16 a[108:109], v33 offset:16384
ds_read_b64_tr_b16 a[110:111], v33 offset:16896
s_waitcnt lgkmcnt(15)
v_dot2c_f32_bf16 v216, v10, v96
s_add_u32 m0, s60, 32768
s_add_u32 s74, s77, 786432
buffer_load_dwordx4 v36, s[16:19], s74 offen offset:0 lds
v_dot2c_f32_bf16 v217, v96, v96
v_dot2c_f32_bf16 v218, v10, v100
v_dot2c_f32_bf16 v219, v100, v100
v_dot2c_f32_bf16 v220, v10, v104
v_dot2c_f32_bf16 v221, v104, v104
v_dot2c_f32_bf16 v222, v10, v108
v_dot2c_f32_bf16 v223, v108, v108
v_dot2c_f32_bf16 v224, v10, v112
v_dot2c_f32_bf16 v225, v112, v112
v_dot2c_f32_bf16 v216, v10, v97
buffer_load_dwordx4 v37, s[16:19], s74 offen offset:1024 lds
v_dot2c_f32_bf16 v217, v97, v97
v_dot2c_f32_bf16 v218, v10, v101
v_dot2c_f32_bf16 v219, v101, v101
v_dot2c_f32_bf16 v220, v10, v105
v_dot2c_f32_bf16 v221, v105, v105
v_dot2c_f32_bf16 v222, v10, v109
v_dot2c_f32_bf16 v223, v109, v109
v_dot2c_f32_bf16 v224, v10, v113
v_dot2c_f32_bf16 v225, v113, v113
v_dot2c_f32_bf16 v216, v10, v98
buffer_load_dwordx4 v38, s[16:19], s74 offen offset:2048 lds
v_dot2c_f32_bf16 v217, v98, v98
v_dot2c_f32_bf16 v218, v10, v102
v_dot2c_f32_bf16 v219, v102, v102
v_dot2c_f32_bf16 v220, v10, v106
v_dot2c_f32_bf16 v221, v106, v106
v_dot2c_f32_bf16 v222, v10, v110
v_dot2c_f32_bf16 v223, v110, v110
v_dot2c_f32_bf16 v224, v10, v114
v_dot2c_f32_bf16 v225, v114, v114
v_dot2c_f32_bf16 v216, v10, v99
buffer_load_dwordx4 v39, s[16:19], s74 offen offset:3072 lds
v_dot2c_f32_bf16 v217, v99, v99
v_dot2c_f32_bf16 v218, v10, v103
v_dot2c_f32_bf16 v219, v103, v103
v_dot2c_f32_bf16 v220, v10, v107
v_dot2c_f32_bf16 v221, v107, v107
v_dot2c_f32_bf16 v222, v10, v111
v_dot2c_f32_bf16 v223, v111, v111
v_dot2c_f32_bf16 v224, v10, v115
v_dot2c_f32_bf16 v225, v115, v115
s_waitcnt lgkmcnt(0)
s_barrier
ds_read_b128 v[116:119], v63 offset:0
ds_read_b128 v[120:123], v63 offset:1024
ds_read_b128 v[124:127], v63 offset:2048
ds_read_b128 v[128:131], v63 offset:3072
ds_read_b128 v[132:135], v63 offset:4096
s_waitcnt lgkmcnt(4)
v_mfma_f32_16x16x32_bf16 v[156:159], a[68:71], v[116:119], 0
s_add_u32 m0, s60, 4864
ds_read_b128 v[136:139], v64 offset:0
v_mfma_f32_16x16x32_bf16 v[192:195], v[116:119], a[72:75], 0
s_waitcnt lgkmcnt(4)
buffer_load_dwordx4 v62, s[12:15], s90 offen offset:256 sc1 lds
v_mfma_f32_16x16x32_bf16 v[160:163], a[68:71], v[120:123], 0
s_add_u32 s75, s91, 0
s_add_u32 m0, s60, 5888
ds_read_b128 v[140:143], v64 offset:1024
v_mfma_f32_16x16x32_bf16 v[176:179], a[64:67], v[120:123], 0
v_mfma_f32_16x16x32_bf16 v[196:199], v[120:123], a[72:75], 0
s_waitcnt lgkmcnt(4)
ds_read_b128 v[144:147], v64 offset:2048
v_mfma_f32_16x16x32_bf16 v[164:167], a[68:71], v[124:127], 0
v_mfma_f32_16x16x32_bf16 v[180:183], a[64:67], v[124:127], 0
ds_read_b128 v[148:151], v64 offset:3072
v_mfma_f32_16x16x32_bf16 v[200:203], v[124:127], a[72:75], 0
s_waitcnt lgkmcnt(5)
buffer_load_dwordx4 v62, s[12:15], s75 offen offset:256 sc1 lds
v_mfma_f32_16x16x32_bf16 v[168:171], a[68:71], v[128:131], 0
s_add_u32 s75, s91, 16384
s_add_u32 m0, s60, 6912
ds_read_b128 v[152:155], v64 offset:4096
v_mfma_f32_16x16x32_bf16 v[184:187], a[64:67], v[128:131], 0
v_mfma_f32_16x16x32_bf16 v[204:207], v[128:131], a[72:75], 0
s_waitcnt lgkmcnt(5)
v_mfma_f32_16x16x32_bf16 v[172:175], a[68:71], v[132:135], 0
v_mfma_f32_16x16x32_bf16 v[188:191], a[64:67], v[132:135], 0
buffer_load_dwordx4 v62, s[12:15], s75 offen offset:256 sc1 lds
v_mfma_f32_16x16x32_bf16 v[208:211], v[132:135], a[72:75], 0
s_add_u32 s75, s91, 32768
s_add_u32 m0, s60, 7936
v_mfma_f32_16x16x32_bf16 v[2:5], a[64:67], v[10:13], 0
v_mfma_f32_16x16x32_bf16 v[6:9], a[68:71], v[10:13], 0
v_mfma_f32_16x16x32_bf16 v[212:215], v[10:13], a[72:75], 0
buffer_load_dwordx4 v62, s[12:15], s75 offen offset:256 sc1 lds
s_add_u32 s75, s91, 49152
s_add_u32 m0, s60, 8960
s_nop 0
buffer_load_dwordx4 v62, s[12:15], s75 offen offset:256 sc1 lds
s_waitcnt vmcnt(9)
s_barrier
s_waitcnt lgkmcnt(4)
v_mfma_f32_16x16x32_bf16 v[156:159], a[80:83], v[136:139], v[156:159]
s_add_u32 m0, s60, 9728
ds_read_b128 v[116:119], v63 offset:36864
ds_read_b64_tr_b16 a[0:1], v30 offset:20480
v_mfma_f32_16x16x32_bf16 v[192:195], v[136:139], a[84:87], v[192:195]
s_waitcnt lgkmcnt(5)
buffer_load_dwordx4 v62, s[12:15], s90 offen offset:512 sc1 lds
v_mfma_f32_16x16x32_bf16 v[160:163], a[80:83], v[140:143], v[160:163]
s_add_u32 s75, s91, 0
s_add_u32 m0, s60, 10752
ds_read_b128 v[120:123], v63 offset:37888
v_mfma_f32_16x16x32_bf16 v[176:179], a[76:79], v[140:143], v[176:179]
ds_read_b64_tr_b16 a[2:3], v30 offset:20992
ds_read_b64_tr_b16 a[12:13], v31 offset:20480
v_mfma_f32_16x16x32_bf16 v[196:199], v[140:143], a[84:87], v[196:199]
s_waitcnt lgkmcnt(7)
ds_read_b128 v[124:127], v63 offset:38912
ds_read_b64_tr_b16 a[14:15], v31 offset:20992
v_mfma_f32_16x16x32_bf16 v[164:167], a[80:83], v[144:147], v[164:167]
ds_read_b64_tr_b16 a[24:25], v32 offset:20480
ds_read_b64_tr_b16 a[26:27], v32 offset:20992
v_mfma_f32_16x16x32_bf16 v[180:183], a[76:79], v[144:147], v[180:183]
ds_read_b128 v[128:131], v63 offset:39936
ds_read_b64_tr_b16 a[36:37], v33 offset:20480
v_mfma_f32_16x16x32_bf16 v[200:203], v[144:147], a[84:87], v[200:203]
s_waitcnt lgkmcnt(12)
buffer_load_dwordx4 v62, s[12:15], s75 offen offset:512 sc1 lds
v_mfma_f32_16x16x32_bf16 v[168:171], a[80:83], v[148:151], v[168:171]
s_add_u32 s75, s91, 16384
s_add_u32 m0, s60, 11776
ds_read_b128 v[132:135], v63 offset:40960
v_mfma_f32_16x16x32_bf16 v[184:187], a[76:79], v[148:151], v[184:187]
ds_read_b64_tr_b16 a[38:39], v33 offset:20992
ds_read_b64_tr_b16 a[4:5], v30 offset:24576
v_mfma_f32_16x16x32_bf16 v[204:207], v[148:151], a[84:87], v[204:207]
s_waitcnt lgkmcnt(14)
ds_read_b64_tr_b16 a[6:7], v30 offset:25088
ds_read_b64_tr_b16 a[16:17], v31 offset:24576
v_mfma_f32_16x16x32_bf16 v[172:175], a[80:83], v[152:155], v[172:175]
ds_read_b64_tr_b16 a[18:19], v31 offset:25088
ds_read_b64_tr_b16 a[28:29], v32 offset:24576
v_mfma_f32_16x16x32_bf16 v[188:191], a[76:79], v[152:155], v[188:191]
buffer_load_dwordx4 v62, s[12:15], s75 offen offset:512 sc1 lds
v_mfma_f32_16x16x32_bf16 v[208:211], v[152:155], a[84:87], v[208:211]
s_add_u32 s75, s91, 32768
s_add_u32 m0, s60, 12800
ds_read_b64_tr_b16 a[30:31], v32 offset:25088
v_mfma_f32_16x16x32_bf16 v[2:5], a[76:79], v[10:13], v[2:5]
ds_read_b64_tr_b16 a[40:41], v33 offset:24576
ds_read_b64_tr_b16 a[42:43], v33 offset:25088
v_mfma_f32_16x16x32_bf16 v[6:9], a[80:83], v[10:13], v[6:9]
ds_read_b64_tr_b16 a[8:9], v30 offset:28672
ds_read_b64_tr_b16 a[10:11], v30 offset:29184
v_mfma_f32_16x16x32_bf16 v[212:215], v[10:13], a[84:87], v[212:215]
s_waitcnt lgkmcnt(15)
ds_read_b64_tr_b16 a[20:21], v31 offset:28672
ds_read_b64_tr_b16 a[22:23], v31 offset:29184
v_mfma_f32_16x16x32_bf16 v[156:159], a[92:95], v[116:119], v[156:159]
ds_read_b128 v[136:139], v64 offset:36864
ds_read_b64_tr_b16 a[32:33], v32 offset:28672
v_mfma_f32_16x16x32_bf16 v[192:195], v[116:119], a[96:99], v[192:195]
buffer_load_dwordx4 v62, s[12:15], s75 offen offset:512 sc1 lds
v_mfma_f32_16x16x32_bf16 v[160:163], a[92:95], v[120:123], v[160:163]
s_add_u32 s75, s91, 49152
s_add_u32 m0, s60, 13824
ds_read_b128 v[140:143], v64 offset:37888
v_mfma_f32_16x16x32_bf16 v[176:179], a[88:91], v[120:123], v[176:179]
ds_read_b64_tr_b16 a[34:35], v32 offset:29184
ds_read_b64_tr_b16 a[44:45], v33 offset:28672
v_mfma_f32_16x16x32_bf16 v[196:199], v[120:123], a[96:99], v[196:199]
ds_read_b128 v[144:147], v64 offset:38912
ds_read_b64_tr_b16 a[46:47], v33 offset:29184
v_mfma_f32_16x16x32_bf16 v[164:167], a[92:95], v[124:127], v[164:167]
v_mfma_f32_16x16x32_bf16 v[180:183], a[88:91], v[124:127], v[180:183]
ds_read_b128 v[148:151], v64 offset:39936
v_mfma_f32_16x16x32_bf16 v[200:203], v[124:127], a[96:99], v[200:203]
s_waitcnt lgkmcnt(15)
buffer_load_dwordx4 v62, s[12:15], s75 offen offset:512 sc1 lds
v_mfma_f32_16x16x32_bf16 v[168:171], a[92:95], v[128:131], v[168:171]
s_lshl_b32 s70, s54, 11
s_add_u32 m0, s70, 147456
ds_read_b128 v[152:155], v64 offset:40960
v_mfma_f32_16x16x32_bf16 v[184:187], a[88:91], v[128:131], v[184:187]
s_add_u32 s71, s86, 7168
v_mfma_f32_16x16x32_bf16 v[204:207], v[128:131], a[96:99], v[204:207]
v_mfma_f32_16x16x32_bf16 v[172:175], a[92:95], v[132:135], v[172:175]
v_mfma_f32_16x16x32_bf16 v[188:191], a[88:91], v[132:135], v[188:191]
buffer_load_dwordx4 v234, s[12:15], s86 offen lds
v_mfma_f32_16x16x32_bf16 v[208:211], v[132:135], a[96:99], v[208:211]
v_mfma_f32_16x16x32_bf16 v[2:5], a[88:91], v[10:13], v[2:5]
v_mfma_f32_16x16x32_bf16 v[6:9], a[92:95], v[10:13], v[6:9]
v_mfma_f32_16x16x32_bf16 v[212:215], v[10:13], a[96:99], v[212:215]
buffer_load_dwordx4 v234, s[12:15], s71 offen offset:1024 lds
s_sub_u32 s73, s88, 262144
buffer_store_dwordx4 v[228:231], v232, s[4:7], s73 offen sc1
s_add_u32 s73, s81, 0
)ASM"
R"ASM(
buffer_store_dwordx4 v[228:231], v65, s[4:7], s73 offen sc1
s_add_u32 s73, s81, 32768
buffer_store_dwordx4 v[228:231], v65, s[4:7], s73 offen sc1
s_add_u32 s73, s83, 0
buffer_store_dwordx4 v[228:231], v66, s[4:7], s73 offen sc1
s_add_u32 s73, s83, 2048
buffer_store_dwordx4 v[228:231], v66, s[4:7], s73 offen sc1
s_add_u32 s73, s84, 0
buffer_store_dwordx4 v[228:231], v66, s[8:11], s73 offen sc1
s_add_u32 s73, s84, 2048
buffer_store_dwordx4 v[228:231], v66, s[8:11], s73 offen sc1
s_waitcnt vmcnt(14) lgkmcnt(2)
s_barrier
v_mfma_f32_16x16x32_bf16 v[156:159], a[104:107], v[136:139], v[156:159]
s_add_u32 m0, s60, 20480
s_add_u32 s74, s77, 787456
ds_read_b128 v[116:119], v63 offset:5120
v_mfma_f32_16x16x32_bf16 v[192:195], v[136:139], a[108:111], v[192:195]
buffer_load_dwordx4 v36, s[16:19], s74 offen offset:0 lds
v_mfma_f32_16x16x32_bf16 v[160:163], a[104:107], v[140:143], v[160:163]
ds_read_b128 v[120:123], v63 offset:6144
ds_read_b64_tr_b16 a[48:49], v30 offset:32768
v_mfma_f32_16x16x32_bf16 v[176:179], a[100:103], v[140:143], v[176:179]
ds_read_b64_tr_b16 a[50:51], v30 offset:33280
ds_read_b64_tr_b16 a[60:61], v31 offset:32768
v_mfma_f32_16x16x32_bf16 v[196:199], v[140:143], a[108:111], v[196:199]
ds_read_b128 v[124:127], v63 offset:7168
ds_read_b64_tr_b16 a[62:63], v31 offset:33280
v_mfma_f32_16x16x32_bf16 v[164:167], a[104:107], v[144:147], v[164:167]
ds_read_b64_tr_b16 a[120:121], v32 offset:32768
ds_read_b64_tr_b16 a[122:123], v32 offset:33280
v_mfma_f32_16x16x32_bf16 v[180:183], a[100:103], v[144:147], v[180:183]
ds_read_b128 v[128:131], v63 offset:8192
ds_read_b64_tr_b16 a[132:133], v33 offset:32768
v_mfma_f32_16x16x32_bf16 v[200:203], v[144:147], a[108:111], v[200:203]
s_waitcnt lgkmcnt(12)
buffer_load_dwordx4 v37, s[16:19], s74 offen offset:1024 lds
v_mfma_f32_16x16x32_bf16 v[168:171], a[104:107], v[148:151], v[168:171]
ds_read_b128 v[132:135], v63 offset:9216
ds_read_b64_tr_b16 a[134:135], v33 offset:33280
v_mfma_f32_16x16x32_bf16 v[184:187], a[100:103], v[148:151], v[184:187]
ds_read_b128 v[96:99], v73 offset:5120
ds_read_b128 v[100:103], v73 offset:6144
v_mfma_f32_16x16x32_bf16 v[204:207], v[148:151], a[108:111], v[204:207]
s_waitcnt lgkmcnt(15)
ds_read_b128 v[104:107], v73 offset:7168
ds_read_b128 v[108:111], v73 offset:8192
v_mfma_f32_16x16x32_bf16 v[172:175], a[104:107], v[152:155], v[172:175]
ds_read_b128 v[112:115], v73 offset:9216
v_mfma_f32_16x16x32_bf16 v[188:191], a[100:103], v[152:155], v[188:191]
buffer_load_dwordx4 v38, s[16:19], s74 offen offset:2048 lds
v_mfma_f32_16x16x32_bf16 v[208:211], v[152:155], a[108:111], v[208:211]
v_mfma_f32_16x16x32_bf16 v[2:5], a[100:103], v[10:13], v[2:5]
v_mfma_f32_16x16x32_bf16 v[6:9], a[104:107], v[10:13], v[6:9]
v_mfma_f32_16x16x32_bf16 v[212:215], v[10:13], a[108:111], v[212:215]
s_waitcnt lgkmcnt(15)
v_mfma_f32_16x16x32_bf16 v[156:159], a[4:7], v[116:119], v[156:159]
ds_read_b128 v[136:139], v64 offset:5120
v_mfma_f32_16x16x32_bf16 v[192:195], v[116:119], a[8:11], v[192:195]
buffer_load_dwordx4 v39, s[16:19], s74 offen offset:3072 lds
v_mfma_f32_16x16x32_bf16 v[160:163], a[4:7], v[120:123], v[160:163]
s_add_u32 m0, s60, 24576
s_add_u32 s74, s77, 788480
ds_read_b128 v[140:143], v64 offset:6144
v_mfma_f32_16x16x32_bf16 v[176:179], a[0:3], v[120:123], v[176:179]
v_mfma_f32_16x16x32_bf16 v[196:199], v[120:123], a[8:11], v[196:199]
s_waitcnt lgkmcnt(14)
ds_read_b128 v[144:147], v64 offset:7168
v_mfma_f32_16x16x32_bf16 v[164:167], a[4:7], v[124:127], v[164:167]
v_mfma_f32_16x16x32_bf16 v[180:183], a[0:3], v[124:127], v[180:183]
ds_read_b128 v[148:151], v64 offset:8192
v_mfma_f32_16x16x32_bf16 v[200:203], v[124:127], a[8:11], v[200:203]
s_waitcnt lgkmcnt(12)
buffer_load_dwordx4 v36, s[16:19], s74 offen offset:0 lds
v_mfma_f32_16x16x32_bf16 v[168:171], a[4:7], v[128:131], v[168:171]
ds_read_b128 v[152:155], v64 offset:9216
v_mfma_f32_16x16x32_bf16 v[184:187], a[0:3], v[128:131], v[184:187]
v_mfma_f32_16x16x32_bf16 v[204:207], v[128:131], a[8:11], v[204:207]
s_waitcnt lgkmcnt(11)
v_mfma_f32_16x16x32_bf16 v[172:175], a[4:7], v[132:135], v[172:175]
v_mfma_f32_16x16x32_bf16 v[188:191], a[0:3], v[132:135], v[188:191]
buffer_load_dwordx4 v37, s[16:19], s74 offen offset:1024 lds
v_mfma_f32_16x16x32_bf16 v[208:211], v[132:135], a[8:11], v[208:211]
v_mfma_f32_16x16x32_bf16 v[2:5], a[0:3], v[10:13], v[2:5]
v_mfma_f32_16x16x32_bf16 v[6:9], a[4:7], v[10:13], v[6:9]
v_mfma_f32_16x16x32_bf16 v[212:215], v[10:13], a[8:11], v[212:215]
s_waitcnt lgkmcnt(4)
v_mfma_f32_16x16x32_bf16 v[156:159], a[16:19], v[136:139], v[156:159]
ds_read_b128 v[116:119], v63 offset:41984
v_mfma_f32_16x16x32_bf16 v[192:195], v[136:139], a[20:23], v[192:195]
s_waitcnt lgkmcnt(4)
buffer_load_dwordx4 v38, s[16:19], s74 offen offset:2048 lds
v_mfma_f32_16x16x32_bf16 v[160:163], a[16:19], v[140:143], v[160:163]
ds_read_b128 v[120:123], v63 offset:43008
v_mfma_f32_16x16x32_bf16 v[176:179], a[12:15], v[140:143], v[176:179]
v_mfma_f32_16x16x32_bf16 v[196:199], v[140:143], a[20:23], v[196:199]
s_waitcnt lgkmcnt(4)
ds_read_b128 v[124:127], v63 offset:44032
v_mfma_f32_16x16x32_bf16 v[164:167], a[16:19], v[144:147], v[164:167]
v_mfma_f32_16x16x32_bf16 v[180:183], a[12:15], v[144:147], v[180:183]
ds_read_b128 v[128:131], v63 offset:45056
v_mfma_f32_16x16x32_bf16 v[200:203], v[144:147], a[20:23], v[200:203]
s_waitcnt lgkmcnt(5)
buffer_load_dwordx4 v39, s[16:19], s74 offen offset:3072 lds
v_mfma_f32_16x16x32_bf16 v[168:171], a[16:19], v[148:151], v[168:171]
s_add_u32 m0, s60, 28672
s_add_u32 s74, s77, 1179648
ds_read_b128 v[132:135], v63 offset:46080
v_mfma_f32_16x16x32_bf16 v[184:187], a[12:15], v[148:151], v[184:187]
v_mfma_f32_16x16x32_bf16 v[204:207], v[148:151], a[20:23], v[204:207]
s_waitcnt lgkmcnt(5)
v_mfma_f32_16x16x32_bf16 v[172:175], a[16:19], v[152:155], v[172:175]
v_mfma_f32_16x16x32_bf16 v[188:191], a[12:15], v[152:155], v[188:191]
buffer_load_dwordx4 v36, s[16:19], s74 offen offset:0 lds
v_mfma_f32_16x16x32_bf16 v[208:211], v[152:155], a[20:23], v[208:211]
v_mfma_f32_16x16x32_bf16 v[2:5], a[12:15], v[10:13], v[2:5]
v_mfma_f32_16x16x32_bf16 v[6:9], a[16:19], v[10:13], v[6:9]
v_mfma_f32_16x16x32_bf16 v[212:215], v[10:13], a[20:23], v[212:215]
v_dot2c_f32_bf16 v216, v10, v96
buffer_load_dwordx4 v37, s[16:19], s74 offen offset:1024 lds
v_dot2c_f32_bf16 v217, v96, v96
v_dot2c_f32_bf16 v218, v10, v100
v_dot2c_f32_bf16 v219, v100, v100
v_dot2c_f32_bf16 v220, v10, v104
v_dot2c_f32_bf16 v221, v104, v104
v_dot2c_f32_bf16 v222, v10, v108
v_dot2c_f32_bf16 v223, v108, v108
v_dot2c_f32_bf16 v224, v10, v112
v_dot2c_f32_bf16 v225, v112, v112
v_dot2c_f32_bf16 v216, v10, v97
buffer_load_dwordx4 v38, s[16:19], s74 offen offset:2048 lds
v_dot2c_f32_bf16 v217, v97, v97
v_dot2c_f32_bf16 v218, v10, v101
v_dot2c_f32_bf16 v219, v101, v101
v_dot2c_f32_bf16 v220, v10, v105
v_dot2c_f32_bf16 v221, v105, v105
v_dot2c_f32_bf16 v222, v10, v109
v_dot2c_f32_bf16 v223, v109, v109
v_dot2c_f32_bf16 v224, v10, v113
v_dot2c_f32_bf16 v225, v113, v113
v_dot2c_f32_bf16 v216, v10, v98
buffer_load_dwordx4 v39, s[16:19], s74 offen offset:3072 lds
v_dot2c_f32_bf16 v217, v98, v98
v_dot2c_f32_bf16 v218, v10, v102
v_dot2c_f32_bf16 v219, v102, v102
v_dot2c_f32_bf16 v220, v10, v106
v_dot2c_f32_bf16 v221, v106, v106
v_dot2c_f32_bf16 v222, v10, v110
v_dot2c_f32_bf16 v223, v110, v110
v_dot2c_f32_bf16 v224, v10, v114
v_dot2c_f32_bf16 v225, v114, v114
v_dot2c_f32_bf16 v216, v10, v99
v_dot2c_f32_bf16 v217, v99, v99
v_dot2c_f32_bf16 v218, v10, v103
v_dot2c_f32_bf16 v219, v103, v103
v_dot2c_f32_bf16 v220, v10, v107
v_dot2c_f32_bf16 v221, v107, v107
v_dot2c_f32_bf16 v222, v10, v111
v_dot2c_f32_bf16 v223, v111, v111
v_dot2c_f32_bf16 v224, v10, v115
v_dot2c_f32_bf16 v225, v115, v115
s_waitcnt vmcnt(21)
s_barrier
s_waitcnt lgkmcnt(4)
v_mfma_f32_16x16x32_bf16 v[156:159], a[28:31], v[116:119], v[156:159]
s_add_u32 m0, s60, 32768
s_add_u32 s74, s77, 1180672
ds_read_b128 v[136:139], v64 offset:41984
v_mfma_f32_16x16x32_bf16 v[192:195], v[116:119], a[32:35], v[192:195]
s_waitcnt lgkmcnt(4)
buffer_load_dwordx4 v36, s[16:19], s74 offen offset:0 lds
v_mfma_f32_16x16x32_bf16 v[160:163], a[28:31], v[120:123], v[160:163]
ds_read_b128 v[140:143], v64 offset:43008
ds_read_b128 v[96:99], v73 offset:10240
v_mfma_f32_16x16x32_bf16 v[176:179], a[24:27], v[120:123], v[176:179]
ds_read_b128 v[100:103], v73 offset:11264
ds_read_b128 v[104:107], v73 offset:12288
v_mfma_f32_16x16x32_bf16 v[196:199], v[120:123], a[32:35], v[196:199]
s_waitcnt lgkmcnt(7)
ds_read_b128 v[144:147], v64 offset:44032
ds_read_b128 v[108:111], v73 offset:13312
v_mfma_f32_16x16x32_bf16 v[164:167], a[28:31], v[124:127], v[164:167]
ds_read_b128 v[112:115], v73 offset:14336
v_mfma_f32_16x16x32_bf16 v[180:183], a[24:27], v[124:127], v[180:183]
ds_read_b128 v[148:151], v64 offset:45056
v_mfma_f32_16x16x32_bf16 v[200:203], v[124:127], a[32:35], v[200:203]
s_waitcnt lgkmcnt(10)
buffer_load_dwordx4 v37, s[16:19], s74 offen offset:1024 lds
v_mfma_f32_16x16x32_bf16 v[168:171], a[28:31], v[128:131], v[168:171]
ds_read_b128 v[152:155], v64 offset:46080
v_mfma_f32_16x16x32_bf16 v[184:187], a[24:27], v[128:131], v[184:187]
v_mfma_f32_16x16x32_bf16 v[204:207], v[128:131], a[32:35], v[204:207]
s_waitcnt lgkmcnt(10)
v_mfma_f32_16x16x32_bf16 v[172:175], a[28:31], v[132:135], v[172:175]
v_mfma_f32_16x16x32_bf16 v[188:191], a[24:27], v[132:135], v[188:191]
buffer_load_dwordx4 v38, s[16:19], s74 offen offset:2048 lds
v_mfma_f32_16x16x32_bf16 v[208:211], v[132:135], a[32:35], v[208:211]
v_mfma_f32_16x16x32_bf16 v[2:5], a[24:27], v[10:13], v[2:5]
v_mfma_f32_16x16x32_bf16 v[6:9], a[28:31], v[10:13], v[6:9]
v_mfma_f32_16x16x32_bf16 v[212:215], v[10:13], a[32:35], v[212:215]
s_waitcnt lgkmcnt(9)
v_mfma_f32_16x16x32_bf16 v[156:159], a[40:43], v[136:139], v[156:159]
ds_read_b128 v[116:119], v63 offset:10240
v_mfma_f32_16x16x32_bf16 v[192:195], v[136:139], a[44:47], v[192:195]
s_waitcnt lgkmcnt(9)
buffer_load_dwordx4 v39, s[16:19], s74 offen offset:3072 lds
v_mfma_f32_16x16x32_bf16 v[160:163], a[40:43], v[140:143], v[160:163]
s_add_u32 m0, s60, 14592
ds_read_b128 v[120:123], v63 offset:11264
v_mfma_f32_16x16x32_bf16 v[176:179], a[36:39], v[140:143], v[176:179]
v_mfma_f32_16x16x32_bf16 v[196:199], v[140:143], a[44:47], v[196:199]
s_waitcnt lgkmcnt(6)
ds_read_b128 v[124:127], v63 offset:12288
v_mfma_f32_16x16x32_bf16 v[164:167], a[40:43], v[144:147], v[164:167]
v_mfma_f32_16x16x32_bf16 v[180:183], a[36:39], v[144:147], v[180:183]
ds_read_b128 v[128:131], v63 offset:13312
v_mfma_f32_16x16x32_bf16 v[200:203], v[144:147], a[44:47], v[200:203]
s_waitcnt lgkmcnt(5)
buffer_load_dwordx4 v62, s[12:15], s90 offen offset:768 sc1 lds
v_mfma_f32_16x16x32_bf16 v[168:171], a[40:43], v[148:151], v[168:171]
s_add_u32 s75, s91, 0
s_add_u32 m0, s60, 15616
ds_read_b128 v[132:135], v63 offset:14336
v_mfma_f32_16x16x32_bf16 v[184:187], a[36:39], v[148:151], v[184:187]
v_mfma_f32_16x16x32_bf16 v[204:207], v[148:151], a[44:47], v[204:207]
s_waitcnt lgkmcnt(5)
v_mfma_f32_16x16x32_bf16 v[172:175], a[40:43], v[152:155], v[172:175]
v_mfma_f32_16x16x32_bf16 v[188:191], a[36:39], v[152:155], v[188:191]
buffer_load_dwordx4 v62, s[12:15], s75 offen offset:768 sc1 lds
v_mfma_f32_16x16x32_bf16 v[208:211], v[152:155], a[44:47], v[208:211]
s_add_u32 s75, s91, 16384
s_add_u32 m0, s60, 16640
v_mfma_f32_16x16x32_bf16 v[2:5], a[36:39], v[10:13], v[2:5]
v_mfma_f32_16x16x32_bf16 v[6:9], a[40:43], v[10:13], v[6:9]
v_mfma_f32_16x16x32_bf16 v[212:215], v[10:13], a[44:47], v[212:215]
v_dot2c_f32_bf16 v216, v10, v96
buffer_load_dwordx4 v62, s[12:15], s75 offen offset:768 sc1 lds
v_dot2c_f32_bf16 v217, v96, v96
v_dot2c_f32_bf16 v218, v10, v100
v_dot2c_f32_bf16 v219, v100, v100
v_dot2c_f32_bf16 v220, v10, v104
v_dot2c_f32_bf16 v221, v104, v104
v_dot2c_f32_bf16 v222, v10, v108
v_dot2c_f32_bf16 v223, v108, v108
v_dot2c_f32_bf16 v224, v10, v112
v_dot2c_f32_bf16 v225, v112, v112
v_dot2c_f32_bf16 v216, v10, v97
s_add_u32 s75, s91, 32768
s_add_u32 m0, s60, 17664
s_nop 0
buffer_load_dwordx4 v62, s[12:15], s75 offen offset:768 sc1 lds
v_dot2c_f32_bf16 v217, v97, v97
)ASM"
R"ASM(
v_dot2c_f32_bf16 v218, v10, v101
v_dot2c_f32_bf16 v219, v101, v101
v_dot2c_f32_bf16 v220, v10, v105
v_dot2c_f32_bf16 v221, v105, v105
v_dot2c_f32_bf16 v222, v10, v109
v_dot2c_f32_bf16 v223, v109, v109
v_dot2c_f32_bf16 v224, v10, v113
v_dot2c_f32_bf16 v225, v113, v113
v_dot2c_f32_bf16 v216, v10, v98
s_add_u32 s75, s91, 49152
s_add_u32 m0, s60, 18688
s_nop 0
buffer_load_dwordx4 v62, s[12:15], s75 offen offset:768 sc1 lds
v_dot2c_f32_bf16 v217, v98, v98
v_dot2c_f32_bf16 v218, v10, v102
v_dot2c_f32_bf16 v219, v102, v102
v_dot2c_f32_bf16 v220, v10, v106
v_dot2c_f32_bf16 v221, v106, v106
v_dot2c_f32_bf16 v222, v10, v110
v_dot2c_f32_bf16 v223, v110, v110
v_dot2c_f32_bf16 v224, v10, v114
v_dot2c_f32_bf16 v225, v114, v114
v_dot2c_f32_bf16 v216, v10, v99
v_lshrrev_b32 v18, 3, v0
v_min_u32 v18, 3, v18
v_mul_u32_u24 v18, 1792, v18
v_and_b32 v19, 7, v0
v_min_u32 v19, 6, v19
v_add_lshl_u32 v18, v18, v19, 7
s_mul_i32 s98, s43, 224
s_add_u32 s98, s98, s66
s_add_u32 s98, s98, 0
s_bfm_b64 exec, 32, 0
buffer_load_dword v77, v18, s[24:27], s98 offen
s_mov_b64 exec, -1
v_dot2c_f32_bf16 v217, v99, v99
v_dot2c_f32_bf16 v218, v10, v103
v_dot2c_f32_bf16 v219, v103, v103
v_dot2c_f32_bf16 v220, v10, v107
v_dot2c_f32_bf16 v221, v107, v107
v_dot2c_f32_bf16 v222, v10, v111
v_dot2c_f32_bf16 v223, v111, v111
v_dot2c_f32_bf16 v224, v10, v115
v_dot2c_f32_bf16 v225, v115, v115
s_waitcnt vmcnt(10)
s_barrier
ds_read_b64_tr_b16 a[52:53], v30 offset:20480
ds_read_b64_tr_b16 a[54:55], v30 offset:20992
ds_read_b64_tr_b16 a[112:113], v31 offset:20480
ds_read_b64_tr_b16 a[114:115], v31 offset:20992
ds_read_b64_tr_b16 a[124:125], v32 offset:20480
ds_read_b64_tr_b16 a[126:127], v32 offset:20992
ds_read_b64_tr_b16 a[136:137], v33 offset:20480
ds_read_b64_tr_b16 a[138:139], v33 offset:20992
ds_read_b64_tr_b16 a[56:57], v30 offset:24576
ds_read_b64_tr_b16 a[58:59], v30 offset:25088
ds_read_b64_tr_b16 a[116:117], v31 offset:24576
ds_read_b64_tr_b16 a[118:119], v31 offset:25088
ds_read_b64_tr_b16 a[128:129], v32 offset:24576
ds_read_b64_tr_b16 a[130:131], v32 offset:25088
ds_read_b64_tr_b16 a[140:141], v33 offset:24576
ds_read_b64_tr_b16 a[142:143], v33 offset:25088
s_waitcnt lgkmcnt(14)
v_mfma_f32_16x16x32_bf16 v[156:159], a[52:55], v[116:119], v[156:159]
s_add_u32 m0, s60, 0
s_add_u32 s74, s79, 1376256
ds_read_b128 v[136:139], v64 offset:10240
s_waitcnt lgkmcnt(7)
v_mfma_f32_16x16x32_bf16 v[192:195], v[116:119], a[56:59], v[192:195]
buffer_load_dwordx4 v40, s[24:27], s74 offen offset:0 lds
v_mfma_f32_16x16x32_bf16 v[160:163], a[52:55], v[120:123], v[160:163]
ds_read_b128 v[140:143], v64 offset:11264
ds_read_b64_tr_b16 a[64:65], v30 offset:28672
v_mfma_f32_16x16x32_bf16 v[176:179], a[48:51], v[120:123], v[176:179]
ds_read_b64_tr_b16 a[66:67], v30 offset:29184
ds_read_b64_tr_b16 a[76:77], v31 offset:28672
v_mfma_f32_16x16x32_bf16 v[196:199], v[120:123], a[56:59], v[196:199]
ds_read_b128 v[144:147], v64 offset:12288
ds_read_b64_tr_b16 a[78:79], v31 offset:29184
v_mfma_f32_16x16x32_bf16 v[164:167], a[52:55], v[124:127], v[164:167]
ds_read_b64_tr_b16 a[88:89], v32 offset:28672
ds_read_b64_tr_b16 a[90:91], v32 offset:29184
v_mfma_f32_16x16x32_bf16 v[180:183], a[48:51], v[124:127], v[180:183]
ds_read_b128 v[148:151], v64 offset:13312
ds_read_b64_tr_b16 a[100:101], v33 offset:28672
v_mfma_f32_16x16x32_bf16 v[200:203], v[124:127], a[56:59], v[200:203]
buffer_load_dwordx4 v41, s[24:27], s74 offen offset:1024 lds
v_mfma_f32_16x16x32_bf16 v[168:171], a[52:55], v[128:131], v[168:171]
ds_read_b128 v[152:155], v64 offset:14336
ds_read_b64_tr_b16 a[102:103], v33 offset:29184
v_mfma_f32_16x16x32_bf16 v[184:187], a[48:51], v[128:131], v[184:187]
v_mfma_f32_16x16x32_bf16 v[204:207], v[128:131], a[56:59], v[204:207]
v_mfma_f32_16x16x32_bf16 v[172:175], a[52:55], v[132:135], v[172:175]
v_mfma_f32_16x16x32_bf16 v[188:191], a[48:51], v[132:135], v[188:191]
buffer_load_dwordx4 v42, s[24:27], s74 offen offset:2048 lds
v_mfma_f32_16x16x32_bf16 v[208:211], v[132:135], a[56:59], v[208:211]
v_mfma_f32_16x16x32_bf16 v[2:5], a[48:51], v[10:13], v[2:5]
v_mfma_f32_16x16x32_bf16 v[6:9], a[52:55], v[10:13], v[6:9]
v_mfma_f32_16x16x32_bf16 v[212:215], v[10:13], a[56:59], v[212:215]
buffer_load_dwordx4 v43, s[24:27], s74 offen offset:3072 lds
s_waitcnt vmcnt(10) lgkmcnt(0)
s_barrier
v_mfma_f32_16x16x32_bf16 v[156:159], a[112:115], v[136:139], v[156:159]
s_add_u32 m0, s60, 20480
s_add_u32 s74, s77, 1181696
ds_read_b128 v[116:119], v63 offset:47104
v_mfma_f32_16x16x32_bf16 v[192:195], v[136:139], a[116:119], v[192:195]
buffer_load_dwordx4 v36, s[16:19], s74 offen offset:0 lds
v_mfma_f32_16x16x32_bf16 v[160:163], a[112:115], v[140:143], v[160:163]
ds_read_b128 v[120:123], v63 offset:48128
ds_read_b64_tr_b16 a[68:69], v30 offset:32768
v_mfma_f32_16x16x32_bf16 v[176:179], a[60:63], v[140:143], v[176:179]
ds_read_b64_tr_b16 a[70:71], v30 offset:33280
ds_read_b64_tr_b16 a[80:81], v31 offset:32768
v_mfma_f32_16x16x32_bf16 v[196:199], v[140:143], a[116:119], v[196:199]
ds_read_b128 v[124:127], v63 offset:49152
ds_read_b64_tr_b16 a[82:83], v31 offset:33280
v_mfma_f32_16x16x32_bf16 v[164:167], a[112:115], v[144:147], v[164:167]
ds_read_b64_tr_b16 a[92:93], v32 offset:32768
ds_read_b64_tr_b16 a[94:95], v32 offset:33280
v_mfma_f32_16x16x32_bf16 v[180:183], a[60:63], v[144:147], v[180:183]
ds_read_b128 v[128:131], v63 offset:50176
ds_read_b64_tr_b16 a[104:105], v33 offset:32768
v_mfma_f32_16x16x32_bf16 v[200:203], v[144:147], a[116:119], v[200:203]
buffer_load_dwordx4 v37, s[16:19], s74 offen offset:1024 lds
v_mfma_f32_16x16x32_bf16 v[168:171], a[112:115], v[148:151], v[168:171]
ds_read_b128 v[132:135], v63 offset:51200
ds_read_b64_tr_b16 a[106:107], v33 offset:33280
v_mfma_f32_16x16x32_bf16 v[184:187], a[60:63], v[148:151], v[184:187]
v_mfma_f32_16x16x32_bf16 v[204:207], v[148:151], a[116:119], v[204:207]
v_mfma_f32_16x16x32_bf16 v[172:175], a[112:115], v[152:155], v[172:175]
v_mfma_f32_16x16x32_bf16 v[188:191], a[60:63], v[152:155], v[188:191]
buffer_load_dwordx4 v38, s[16:19], s74 offen offset:2048 lds
v_mfma_f32_16x16x32_bf16 v[208:211], v[152:155], a[116:119], v[208:211]
v_mfma_f32_16x16x32_bf16 v[2:5], a[60:63], v[10:13], v[2:5]
v_mfma_f32_16x16x32_bf16 v[6:9], a[112:115], v[10:13], v[6:9]
v_mfma_f32_16x16x32_bf16 v[212:215], v[10:13], a[116:119], v[212:215]
s_waitcnt lgkmcnt(12)
v_mfma_f32_16x16x32_bf16 v[156:159], a[124:127], v[116:119], v[156:159]
ds_read_b128 v[136:139], v64 offset:47104
v_mfma_f32_16x16x32_bf16 v[192:195], v[116:119], a[128:131], v[192:195]
s_waitcnt lgkmcnt(12)
buffer_load_dwordx4 v39, s[16:19], s74 offen offset:3072 lds
v_mfma_f32_16x16x32_bf16 v[160:163], a[124:127], v[120:123], v[160:163]
s_add_u32 m0, s60, 4096
s_add_u32 s74, s79, 1490944
ds_read_b128 v[140:143], v64 offset:48128
v_mfma_f32_16x16x32_bf16 v[176:179], a[120:123], v[120:123], v[176:179]
v_mfma_f32_16x16x32_bf16 v[196:199], v[120:123], a[128:131], v[196:199]
s_waitcnt lgkmcnt(9)
ds_read_b128 v[144:147], v64 offset:49152
v_mfma_f32_16x16x32_bf16 v[164:167], a[124:127], v[124:127], v[164:167]
v_mfma_f32_16x16x32_bf16 v[180:183], a[120:123], v[124:127], v[180:183]
ds_read_b128 v[148:151], v64 offset:50176
v_mfma_f32_16x16x32_bf16 v[200:203], v[124:127], a[128:131], v[200:203]
s_waitcnt lgkmcnt(7)
buffer_load_dwordx4 v40, s[24:27], s74 offen offset:0 lds
v_mfma_f32_16x16x32_bf16 v[168:171], a[124:127], v[128:131], v[168:171]
ds_read_b128 v[152:155], v64 offset:51200
v_mfma_f32_16x16x32_bf16 v[184:187], a[120:123], v[128:131], v[184:187]
v_mfma_f32_16x16x32_bf16 v[204:207], v[128:131], a[128:131], v[204:207]
s_waitcnt lgkmcnt(6)
v_mfma_f32_16x16x32_bf16 v[172:175], a[124:127], v[132:135], v[172:175]
v_mfma_f32_16x16x32_bf16 v[188:191], a[120:123], v[132:135], v[188:191]
buffer_load_dwordx4 v41, s[24:27], s74 offen offset:1024 lds
v_mfma_f32_16x16x32_bf16 v[208:211], v[132:135], a[128:131], v[208:211]
v_mfma_f32_16x16x32_bf16 v[2:5], a[120:123], v[10:13], v[2:5]
v_mfma_f32_16x16x32_bf16 v[6:9], a[124:127], v[10:13], v[6:9]
v_mfma_f32_16x16x32_bf16 v[212:215], v[10:13], a[128:131], v[212:215]
buffer_load_dwordx4 v42, s[24:27], s74 offen offset:2048 lds
buffer_load_dwordx4 v43, s[24:27], s74 offen offset:3072 lds
s_add_u32 m0, s60, 24576
s_add_u32 s74, s78, 32768
buffer_load_dwordx4 v48, s[20:23], s74 offen offset:0 lds
buffer_load_dwordx4 v49, s[20:23], s74 offen offset:1024 lds
buffer_load_dwordx4 v50, s[20:23], s74 offen offset:2048 lds
buffer_load_dwordx4 v51, s[20:23], s74 offen offset:3072 lds
s_waitcnt vmcnt(17) lgkmcnt(5)
s_barrier
s_waitcnt vmcnt(12)
ds_read_b64_tr_b16 a[144:145], v26 offset:0
ds_read_b64_tr_b16 a[146:147], v26 offset:512
ds_read_b64_tr_b16 a[148:149], v27 offset:0
ds_read_b64_tr_b16 a[150:151], v27 offset:512
ds_read_b64_tr_b16 a[152:153], v28 offset:0
ds_read_b64_tr_b16 a[154:155], v28 offset:512
ds_read_b64_tr_b16 a[156:157], v29 offset:0
ds_read_b64_tr_b16 a[158:159], v29 offset:512
s_waitcnt lgkmcnt(12)
v_mfma_f32_16x16x32_bf16 v[156:159], a[136:139], v[136:139], v[156:159]
s_add_u32 m0, s60, 28672
s_add_u32 s74, s78, 65536
ds_read_b128 v[116:119], v63 offset:15360
v_mfma_f32_16x16x32_bf16 v[192:195], v[136:139], a[140:143], v[192:195]
s_waitcnt lgkmcnt(12)
buffer_load_dwordx4 v48, s[20:23], s74 offen offset:0 lds
v_mfma_f32_16x16x32_bf16 v[160:163], a[136:139], v[140:143], v[160:163]
ds_read_b128 v[120:123], v63 offset:16384
ds_read_b128 v[96:99], v73 offset:15360
v_mfma_f32_16x16x32_bf16 v[176:179], a[132:135], v[140:143], v[176:179]
ds_read_b128 v[100:103], v73 offset:16384
ds_read_b128 v[104:107], v73 offset:17408
v_mfma_f32_16x16x32_bf16 v[196:199], v[140:143], a[140:143], v[196:199]
s_waitcnt lgkmcnt(15)
ds_read_b128 v[124:127], v63 offset:17408
ds_read_b128 v[108:111], v73 offset:18432
v_mfma_f32_16x16x32_bf16 v[164:167], a[136:139], v[144:147], v[164:167]
ds_read_b128 v[112:115], v73 offset:19456
v_mfma_f32_16x16x32_bf16 v[180:183], a[132:135], v[144:147], v[180:183]
ds_read_b128 v[128:131], v63 offset:18432
v_mfma_f32_16x16x32_bf16 v[200:203], v[144:147], a[140:143], v[200:203]
s_waitcnt lgkmcnt(15)
buffer_load_dwordx4 v49, s[20:23], s74 offen offset:1024 lds
v_mfma_f32_16x16x32_bf16 v[168:171], a[136:139], v[148:151], v[168:171]
ds_read_b128 v[132:135], v63 offset:19456
v_mfma_f32_16x16x32_bf16 v[184:187], a[132:135], v[148:151], v[184:187]
v_mfma_f32_16x16x32_bf16 v[204:207], v[148:151], a[140:143], v[204:207]
v_mfma_f32_16x16x32_bf16 v[172:175], a[136:139], v[152:155], v[172:175]
v_mfma_f32_16x16x32_bf16 v[188:191], a[132:135], v[152:155], v[188:191]
buffer_load_dwordx4 v50, s[20:23], s74 offen offset:2048 lds
v_mfma_f32_16x16x32_bf16 v[208:211], v[152:155], a[140:143], v[208:211]
v_mfma_f32_16x16x32_bf16 v[2:5], a[132:135], v[10:13], v[2:5]
v_mfma_f32_16x16x32_bf16 v[6:9], a[136:139], v[10:13], v[6:9]
v_mfma_f32_16x16x32_bf16 v[212:215], v[10:13], a[140:143], v[212:215]
s_waitcnt lgkmcnt(7)
v_dot2c_f32_bf16 v216, v10, v96
buffer_load_dwordx4 v51, s[20:23], s74 offen offset:3072 lds
v_dot2c_f32_bf16 v217, v96, v96
s_waitcnt lgkmcnt(6)
v_dot2c_f32_bf16 v218, v10, v100
v_dot2c_f32_bf16 v219, v100, v100
s_waitcnt lgkmcnt(5)
v_dot2c_f32_bf16 v220, v10, v104
v_dot2c_f32_bf16 v221, v104, v104
s_waitcnt lgkmcnt(3)
v_dot2c_f32_bf16 v222, v10, v108
v_dot2c_f32_bf16 v223, v108, v108
s_waitcnt lgkmcnt(2)
v_dot2c_f32_bf16 v224, v10, v112
v_dot2c_f32_bf16 v225, v112, v112
v_dot2c_f32_bf16 v216, v10, v97
s_add_u32 m0, s60, 0
s_add_u32 s74, s79, 1605632
buffer_load_dwordx4 v40, s[24:27], s74 offen offset:0 lds
v_dot2c_f32_bf16 v217, v97, v97
v_dot2c_f32_bf16 v218, v10, v101
v_dot2c_f32_bf16 v219, v101, v101
v_dot2c_f32_bf16 v220, v10, v105
v_dot2c_f32_bf16 v221, v105, v105
v_dot2c_f32_bf16 v222, v10, v109
v_dot2c_f32_bf16 v223, v109, v109
v_dot2c_f32_bf16 v224, v10, v113
v_dot2c_f32_bf16 v225, v113, v113
v_dot2c_f32_bf16 v216, v10, v98
buffer_load_dwordx4 v41, s[24:27], s74 offen offset:1024 lds
v_dot2c_f32_bf16 v217, v98, v98
v_dot2c_f32_bf16 v218, v10, v102
v_dot2c_f32_bf16 v219, v102, v102
v_dot2c_f32_bf16 v220, v10, v106
v_dot2c_f32_bf16 v221, v106, v106
)ASM"
R"ASM(
v_dot2c_f32_bf16 v222, v10, v110
v_dot2c_f32_bf16 v223, v110, v110
v_dot2c_f32_bf16 v224, v10, v114
v_dot2c_f32_bf16 v225, v114, v114
v_dot2c_f32_bf16 v216, v10, v99
buffer_load_dwordx4 v42, s[24:27], s74 offen offset:2048 lds
v_dot2c_f32_bf16 v217, v99, v99
v_dot2c_f32_bf16 v218, v10, v103
v_dot2c_f32_bf16 v219, v103, v103
v_dot2c_f32_bf16 v220, v10, v107
v_dot2c_f32_bf16 v221, v107, v107
v_dot2c_f32_bf16 v222, v10, v111
v_dot2c_f32_bf16 v223, v111, v111
v_dot2c_f32_bf16 v224, v10, v115
v_dot2c_f32_bf16 v225, v115, v115
buffer_load_dwordx4 v43, s[24:27], s74 offen offset:3072 lds
v_lshrrev_b32 v18, 3, v0
v_min_u32 v18, 1, v18
v_mul_u32_u24 v18, 1792, v18
v_and_b32 v19, 7, v0
v_min_u32 v19, 6, v19
v_add_lshl_u32 v18, v18, v19, 7
s_mul_i32 s98, s43, 224
s_add_u32 s98, s98, s66
s_add_u32 s98, s98, 917504
s_bfm_b64 exec, 16, 0
buffer_load_dword v77, v18, s[24:27], s98 offen
s_mov_b64 exec, -1
v_lshrrev_b32 v18, 3, v0
v_min_u32 v18, 1, v18
v_mul_u32_u24 v18, 2048, v18
v_and_b32 v19, 7, v0
v_add_lshl_u32 v18, v18, v19, 7
s_mul_i32 s98, s43, 256
s_add_u32 s98, s98, s67
s_add_u32 s98, s98, 0
s_bfm_b64 exec, 16, 0
buffer_load_dword v77, v18, s[28:31], s98 offen
s_mov_b64 exec, -1
s_waitcnt vmcnt(18)
s_barrier
ds_read_b64_tr_b16 a[72:73], v30 offset:20480
ds_read_b64_tr_b16 a[74:75], v30 offset:20992
ds_read_b64_tr_b16 a[84:85], v31 offset:20480
ds_read_b64_tr_b16 a[86:87], v31 offset:20992
ds_read_b64_tr_b16 a[96:97], v32 offset:20480
ds_read_b64_tr_b16 a[98:99], v32 offset:20992
ds_read_b64_tr_b16 a[108:109], v33 offset:20480
ds_read_b64_tr_b16 a[110:111], v33 offset:20992
s_getpc_b64 s[68:69]
.LPC_1_%=:
s_lshl_b32 s98, s97, 7
s_add_u32 s98, s98, .LATT_%=-.LPC_1_%=
s_min_u32 s98, s98, .LDONE_%=-.LPC_1_%=-4
v_mov_b32 v22, s98
s_mov_b64 exec, 1
global_load_dword v77, v22, s[68:69]
s_mov_b64 exec, -1
v_mfma_f32_16x16x32_bf16 v[156:159], a[68:71], v[116:119], v[156:159]
s_add_u32 m0, s60, 32768
s_add_u32 s74, s78, 98304
ds_read_b128 v[136:139], v64 offset:15360
s_waitcnt lgkmcnt(7)
v_mfma_f32_16x16x32_bf16 v[192:195], v[116:119], a[72:75], v[192:195]
buffer_load_dwordx4 v48, s[20:23], s74 offen offset:0 lds
v_mfma_f32_16x16x32_bf16 v[160:163], a[68:71], v[120:123], v[160:163]
ds_read_b128 v[140:143], v64 offset:16384
s_waitcnt vmcnt(16)
ds_read_b64_tr_b16 a[160:161], v26 offset:4096
v_mfma_f32_16x16x32_bf16 v[176:179], a[64:67], v[120:123], v[176:179]
ds_read_b64_tr_b16 a[162:163], v26 offset:4608
ds_read_b64_tr_b16 a[164:165], v27 offset:4096
v_mfma_f32_16x16x32_bf16 v[196:199], v[120:123], a[72:75], v[196:199]
ds_read_b128 v[144:147], v64 offset:17408
ds_read_b64_tr_b16 a[166:167], v27 offset:4608
v_mfma_f32_16x16x32_bf16 v[164:167], a[68:71], v[124:127], v[164:167]
ds_read_b64_tr_b16 a[168:169], v28 offset:4096
ds_read_b64_tr_b16 a[170:171], v28 offset:4608
v_mfma_f32_16x16x32_bf16 v[180:183], a[64:67], v[124:127], v[180:183]
ds_read_b128 v[148:151], v64 offset:18432
ds_read_b64_tr_b16 a[172:173], v29 offset:4096
v_mfma_f32_16x16x32_bf16 v[200:203], v[124:127], a[72:75], v[200:203]
buffer_load_dwordx4 v49, s[20:23], s74 offen offset:1024 lds
v_mfma_f32_16x16x32_bf16 v[168:171], a[68:71], v[128:131], v[168:171]
ds_read_b128 v[152:155], v64 offset:19456
ds_read_b64_tr_b16 a[174:175], v29 offset:4608
v_mfma_f32_16x16x32_bf16 v[184:187], a[64:67], v[128:131], v[184:187]
v_add_f32_dpp v216, v216, v216 quad_perm:[1,0,3,2] row_mask:0xf bank_mask:0xf
v_add_f32_dpp v217, v217, v217 quad_perm:[1,0,3,2] row_mask:0xf bank_mask:0xf
v_mfma_f32_16x16x32_bf16 v[204:207], v[128:131], a[72:75], v[204:207]
v_add_f32_dpp v218, v218, v218 quad_perm:[1,0,3,2] row_mask:0xf bank_mask:0xf
v_add_f32_dpp v219, v219, v219 quad_perm:[1,0,3,2] row_mask:0xf bank_mask:0xf
v_mfma_f32_16x16x32_bf16 v[172:175], a[68:71], v[132:135], v[172:175]
v_add_f32_dpp v220, v220, v220 quad_perm:[1,0,3,2] row_mask:0xf bank_mask:0xf
v_add_f32_dpp v221, v221, v221 quad_perm:[1,0,3,2] row_mask:0xf bank_mask:0xf
v_mfma_f32_16x16x32_bf16 v[188:191], a[64:67], v[132:135], v[188:191]
buffer_load_dwordx4 v50, s[20:23], s74 offen offset:2048 lds
v_mfma_f32_16x16x32_bf16 v[208:211], v[132:135], a[72:75], v[208:211]
v_add_f32_dpp v222, v222, v222 quad_perm:[1,0,3,2] row_mask:0xf bank_mask:0xf
v_add_f32_dpp v223, v223, v223 quad_perm:[1,0,3,2] row_mask:0xf bank_mask:0xf
v_mfma_f32_16x16x32_bf16 v[2:5], a[64:67], v[10:13], v[2:5]
v_add_f32_dpp v224, v224, v224 quad_perm:[1,0,3,2] row_mask:0xf bank_mask:0xf
v_add_f32_dpp v225, v225, v225 quad_perm:[1,0,3,2] row_mask:0xf bank_mask:0xf
v_mfma_f32_16x16x32_bf16 v[6:9], a[68:71], v[10:13], v[6:9]
v_add_f32_dpp v216, v216, v216 quad_perm:[2,3,0,1] row_mask:0xf bank_mask:0xf
v_add_f32_dpp v217, v217, v217 quad_perm:[2,3,0,1] row_mask:0xf bank_mask:0xf
v_mfma_f32_16x16x32_bf16 v[212:215], v[10:13], a[72:75], v[212:215]
v_add_f32_dpp v218, v218, v218 quad_perm:[2,3,0,1] row_mask:0xf bank_mask:0xf
v_add_f32_dpp v219, v219, v219 quad_perm:[2,3,0,1] row_mask:0xf bank_mask:0xf
v_add_f32_dpp v220, v220, v220 quad_perm:[2,3,0,1] row_mask:0xf bank_mask:0xf
buffer_load_dwordx4 v51, s[20:23], s74 offen offset:3072 lds
v_add_f32_dpp v221, v221, v221 quad_perm:[2,3,0,1] row_mask:0xf bank_mask:0xf
v_add_f32_dpp v222, v222, v222 quad_perm:[2,3,0,1] row_mask:0xf bank_mask:0xf
v_add_f32_dpp v223, v223, v223 quad_perm:[2,3,0,1] row_mask:0xf bank_mask:0xf
v_add_f32_dpp v224, v224, v224 quad_perm:[2,3,0,1] row_mask:0xf bank_mask:0xf
v_add_f32_dpp v225, v225, v225 quad_perm:[2,3,0,1] row_mask:0xf bank_mask:0xf
v_add_f32_dpp v216, v216, v216 row_half_mirror row_mask:0xf bank_mask:0xf
v_add_f32_dpp v217, v217, v217 row_half_mirror row_mask:0xf bank_mask:0xf
v_add_f32_dpp v218, v218, v218 row_half_mirror row_mask:0xf bank_mask:0xf
v_add_f32_dpp v219, v219, v219 row_half_mirror row_mask:0xf bank_mask:0xf
v_add_f32_dpp v220, v220, v220 row_half_mirror row_mask:0xf bank_mask:0xf
v_add_f32_dpp v221, v221, v221 row_half_mirror row_mask:0xf bank_mask:0xf
v_add_f32_dpp v222, v222, v222 row_half_mirror row_mask:0xf bank_mask:0xf
v_add_f32_dpp v223, v223, v223 row_half_mirror row_mask:0xf bank_mask:0xf
v_add_f32_dpp v224, v224, v224 row_half_mirror row_mask:0xf bank_mask:0xf
v_add_f32_dpp v225, v225, v225 row_half_mirror row_mask:0xf bank_mask:0xf
ds_write_b64 v246, v[216:217] offset:0
ds_write_b64 v246, v[218:219] offset:256
ds_write_b64 v246, v[220:221] offset:512
ds_write_b64 v246, v[222:223] offset:768
ds_write_b64 v246, v[224:225] offset:1024
s_waitcnt lgkmcnt(0)
s_barrier
v_mfma_f32_16x16x32_bf16 v[156:159], a[80:83], v[136:139], v[156:159]
s_mul_i32 s70, s54, 20
ds_read_b128 v[116:119], v63 offset:52224
v_min_u32 v20, 19, v0
v_mfma_f32_16x16x32_bf16 v[192:195], v[136:139], a[84:87], v[192:195]
buffer_store_dwordx2 v[254:255], v252, s[4:7], 0 offen sc1
v_mfma_f32_16x16x32_bf16 v[160:163], a[80:83], v[140:143], v[160:163]
s_add_u32 m0, s60, 20480
s_add_u32 s74, s78, 0
ds_read_b128 v[120:123], v63 offset:53248
v_mfma_f32_16x16x32_bf16 v[176:179], a[76:79], v[140:143], v[176:179]
v_add_u32 v20, s70, v20
v_lshlrev_b32 v21, 4, v20
v_mfma_f32_16x16x32_bf16 v[196:199], v[140:143], a[84:87], v[196:199]
ds_read_b128 v[124:127], v63 offset:54272
v_add_u32 v21, 156672, v21
v_mfma_f32_16x16x32_bf16 v[164:167], a[80:83], v[144:147], v[164:167]
ds_read_b128 v[216:219], v21
v_lshlrev_b32 v20, 2, v20
v_mfma_f32_16x16x32_bf16 v[180:183], a[76:79], v[144:147], v[180:183]
ds_read_b128 v[128:131], v63 offset:55296
v_add_u32 v20, 155648, v20
v_mfma_f32_16x16x32_bf16 v[200:203], v[144:147], a[84:87], v[200:203]
buffer_load_dwordx4 v48, s[20:23], s74 offen offset:0 lds
v_mfma_f32_16x16x32_bf16 v[168:171], a[80:83], v[148:151], v[168:171]
ds_read_b128 v[132:135], v63 offset:56320
s_waitcnt lgkmcnt(2)
v_add_f32 v216, v216, v218
v_mfma_f32_16x16x32_bf16 v[184:187], a[76:79], v[148:151], v[184:187]
v_add_f32 v217, v217, v219
v_mul_f32 v216, 0xbb000000, v216
v_mfma_f32_16x16x32_bf16 v[204:207], v[148:151], a[84:87], v[204:207]
v_mul_f32 v217, 0x3b000000, v217
v_fma_f32 v217, -v216, v216, v217
v_mfma_f32_16x16x32_bf16 v[172:175], a[80:83], v[152:155], v[172:175]
v_max_f32 v217, 0, v217
v_add_f32 v217, 0x3727c5ac, v217
v_mfma_f32_16x16x32_bf16 v[188:191], a[76:79], v[152:155], v[188:191]
buffer_load_dwordx4 v49, s[20:23], s74 offen offset:1024 lds
v_mfma_f32_16x16x32_bf16 v[208:211], v[152:155], a[84:87], v[208:211]
v_rsq_f32 v217, v217
v_mfma_f32_16x16x32_bf16 v[2:5], a[76:79], v[10:13], v[2:5]
ds_write2_b32 v20, v216, v217 offset0:0 offset1:128
v_mfma_f32_16x16x32_bf16 v[6:9], a[80:83], v[10:13], v[6:9]
v_mfma_f32_16x16x32_bf16 v[212:215], v[10:13], a[84:87], v[212:215]
v_mfma_f32_16x16x32_bf16 v[156:159], a[92:95], v[116:119], v[156:159]
ds_read_b128 v[136:139], v64 offset:52224
v_mfma_f32_16x16x32_bf16 v[192:195], v[116:119], a[96:99], v[192:195]
buffer_load_dwordx4 v50, s[20:23], s74 offen offset:2048 lds
v_mfma_f32_16x16x32_bf16 v[160:163], a[92:95], v[120:123], v[160:163]
ds_read_b128 v[140:143], v64 offset:53248
v_mfma_f32_16x16x32_bf16 v[176:179], a[88:91], v[120:123], v[176:179]
v_mfma_f32_16x16x32_bf16 v[196:199], v[120:123], a[96:99], v[196:199]
ds_read_b128 v[144:147], v64 offset:54272
v_mfma_f32_16x16x32_bf16 v[164:167], a[92:95], v[124:127], v[164:167]
v_mfma_f32_16x16x32_bf16 v[180:183], a[88:91], v[124:127], v[180:183]
ds_read_b128 v[148:151], v64 offset:55296
v_mfma_f32_16x16x32_bf16 v[200:203], v[124:127], a[96:99], v[200:203]
s_waitcnt lgkmcnt(6)
buffer_load_dwordx4 v51, s[20:23], s74 offen offset:3072 lds
v_mfma_f32_16x16x32_bf16 v[168:171], a[92:95], v[128:131], v[168:171]
s_add_u32 m0, s60, 4096
s_add_u32 s74, s79, 1720320
ds_read_b128 v[152:155], v64 offset:56320
v_mfma_f32_16x16x32_bf16 v[184:187], a[88:91], v[128:131], v[184:187]
v_mfma_f32_16x16x32_bf16 v[204:207], v[128:131], a[96:99], v[204:207]
s_waitcnt lgkmcnt(6)
v_mfma_f32_16x16x32_bf16 v[172:175], a[92:95], v[132:135], v[172:175]
v_mfma_f32_16x16x32_bf16 v[188:191], a[88:91], v[132:135], v[188:191]
buffer_load_dwordx4 v40, s[24:27], s74 offen offset:0 lds
v_mfma_f32_16x16x32_bf16 v[208:211], v[132:135], a[96:99], v[208:211]
v_mfma_f32_16x16x32_bf16 v[2:5], a[88:91], v[10:13], v[2:5]
v_mfma_f32_16x16x32_bf16 v[6:9], a[92:95], v[10:13], v[6:9]
v_mfma_f32_16x16x32_bf16 v[212:215], v[10:13], a[96:99], v[212:215]
s_waitcnt lgkmcnt(4)
v_mfma_f32_16x16x32_bf16 v[156:159], a[104:107], v[136:139], v[156:159]
buffer_load_dwordx4 v41, s[24:27], s74 offen offset:1024 lds
v_mfma_f32_16x16x32_bf16 v[192:195], v[136:139], a[108:111], v[192:195]
s_waitcnt lgkmcnt(3)
v_mfma_f32_16x16x32_bf16 v[160:163], a[104:107], v[140:143], v[160:163]
v_mfma_f32_16x16x32_bf16 v[176:179], a[100:103], v[140:143], v[176:179]
v_mfma_f32_16x16x32_bf16 v[196:199], v[140:143], a[108:111], v[196:199]
s_waitcnt lgkmcnt(2)
v_mfma_f32_16x16x32_bf16 v[164:167], a[104:107], v[144:147], v[164:167]
buffer_load_dwordx4 v42, s[24:27], s74 offen offset:2048 lds
v_mfma_f32_16x16x32_bf16 v[180:183], a[100:103], v[144:147], v[180:183]
v_mfma_f32_16x16x32_bf16 v[200:203], v[144:147], a[108:111], v[200:203]
s_waitcnt lgkmcnt(1)
v_mfma_f32_16x16x32_bf16 v[168:171], a[104:107], v[148:151], v[168:171]
v_mfma_f32_16x16x32_bf16 v[184:187], a[100:103], v[148:151], v[184:187]
v_mfma_f32_16x16x32_bf16 v[204:207], v[148:151], a[108:111], v[204:207]
s_waitcnt lgkmcnt(0)
buffer_load_dwordx4 v43, s[24:27], s74 offen offset:3072 lds
v_mfma_f32_16x16x32_bf16 v[172:175], a[104:107], v[152:155], v[172:175]
s_waitcnt vmcnt(16)
ds_read_b64_tr_b16 a[176:177], v26 offset:0
ds_read_b64_tr_b16 a[178:179], v26 offset:512
v_mfma_f32_16x16x32_bf16 v[188:191], a[100:103], v[152:155], v[188:191]
ds_read_b64_tr_b16 a[180:181], v27 offset:0
ds_read_b64_tr_b16 a[182:183], v27 offset:512
v_mfma_f32_16x16x32_bf16 v[208:211], v[152:155], a[108:111], v[208:211]
ds_read_b64_tr_b16 a[184:185], v28 offset:0
ds_read_b64_tr_b16 a[186:187], v28 offset:512
v_mfma_f32_16x16x32_bf16 v[2:5], a[100:103], v[10:13], v[2:5]
ds_read_b64_tr_b16 a[188:189], v29 offset:0
ds_read_b64_tr_b16 a[190:191], v29 offset:512
v_mfma_f32_16x16x32_bf16 v[6:9], a[104:107], v[10:13], v[6:9]
)ASM"
R"ASM(
s_add_u32 m0, s60, 8192
s_add_u32 s74, s79, 114688
v_mfma_f32_16x16x32_bf16 v[212:215], v[10:13], a[108:111], v[212:215]
buffer_load_dwordx4 v40, s[24:27], s74 offen offset:0 lds
buffer_load_dwordx4 v41, s[24:27], s74 offen offset:1024 lds
buffer_load_dwordx4 v42, s[24:27], s74 offen offset:2048 lds
buffer_load_dwordx4 v43, s[24:27], s74 offen offset:3072 lds
s_waitcnt lgkmcnt(0)
s_add_u32 m0, s60, 0
s_add_u32 s74, s79, 0
buffer_load_dwordx4 v40, s[24:27], s74 offen offset:0 lds
buffer_load_dwordx4 v41, s[24:27], s74 offen offset:1024 lds
buffer_load_dwordx4 v42, s[24:27], s74 offen offset:2048 lds
buffer_load_dwordx4 v43, s[24:27], s74 offen offset:3072 lds
v_mov_b32 v52, v212
s_mul_i32 s70, s54, 98304
s_add_u32 s77, s77, s70
s_barrier
ds_read2_b32 v[212:213], v68 offset0:0 offset1:128
ds_read2_b32 v[214:215], v68 offset0:16 offset1:144
ds_read2_b32 v[216:217], v68 offset0:32 offset1:160
ds_read2_b32 v[218:219], v68 offset0:48 offset1:176
ds_read2_b32 v[220:221], v68 offset0:64 offset1:192
ds_read_b128 v[96:99], v69 offset:0
ds_read_b128 v[100:103], v69 offset:64
ds_read_b128 v[104:107], v69 offset:128
ds_read_b128 v[108:111], v69 offset:192
ds_read_b128 v[112:115], v69 offset:256
ds_read_b128 v[228:231], v69 offset:512
ds_read_b128 v[232:235], v69 offset:576
ds_read_b128 v[236:239], v69 offset:640
ds_read_b128 v[240:243], v69 offset:704
ds_read_b128 v[244:247], v69 offset:768
s_barrier
s_add_u32 s72, s81, 32768
s_add_u32 m0, s60, 12288
s_add_u32 s74, s79, 229376
buffer_load_dwordx4 v40, s[24:27], s74 offen offset:0 lds
v_mov_b32 v20, v52
s_waitcnt lgkmcnt(14)
v_pk_fma_f32 v[156:157], v[212:213], v[6:7], v[156:157] op_sel_hi:[0,1,1]
v_pk_fma_f32 v[158:159], v[212:213], v[8:9], v[158:159] op_sel_hi:[0,1,1]
s_waitcnt lgkmcnt(13)
v_pk_fma_f32 v[160:161], v[214:215], v[6:7], v[160:161] op_sel_hi:[0,1,1]
v_pk_fma_f32 v[176:177], v[214:215], v[2:3], v[176:177] op_sel_hi:[0,1,1]
v_pk_fma_f32 v[162:163], v[214:215], v[8:9], v[162:163] op_sel_hi:[0,1,1]
v_pk_fma_f32 v[178:179], v[214:215], v[4:5], v[178:179] op_sel_hi:[0,1,1]
s_waitcnt lgkmcnt(12)
v_pk_fma_f32 v[164:165], v[216:217], v[6:7], v[164:165] op_sel_hi:[0,1,1]
v_pk_fma_f32 v[180:181], v[216:217], v[2:3], v[180:181] op_sel_hi:[0,1,1]
v_pk_fma_f32 v[166:167], v[216:217], v[8:9], v[166:167] op_sel_hi:[0,1,1]
v_pk_fma_f32 v[182:183], v[216:217], v[4:5], v[182:183] op_sel_hi:[0,1,1]
s_waitcnt lgkmcnt(11)
v_pk_fma_f32 v[168:169], v[218:219], v[6:7], v[168:169] op_sel_hi:[0,1,1]
buffer_load_dwordx4 v41, s[24:27], s74 offen offset:1024 lds
v_pk_fma_f32 v[184:185], v[218:219], v[2:3], v[184:185] op_sel_hi:[0,1,1]
v_pk_fma_f32 v[170:171], v[218:219], v[8:9], v[170:171] op_sel_hi:[0,1,1]
v_pk_fma_f32 v[186:187], v[218:219], v[4:5], v[186:187] op_sel_hi:[0,1,1]
s_waitcnt lgkmcnt(10)
v_pk_fma_f32 v[172:173], v[220:221], v[6:7], v[172:173] op_sel_hi:[0,1,1]
v_pk_fma_f32 v[188:189], v[220:221], v[2:3], v[188:189] op_sel_hi:[0,1,1]
v_pk_fma_f32 v[174:175], v[220:221], v[8:9], v[174:175] op_sel_hi:[0,1,1]
v_pk_fma_f32 v[190:191], v[220:221], v[4:5], v[190:191] op_sel_hi:[0,1,1]
s_waitcnt lgkmcnt(9)
v_pk_fma_f32 v[192:193], v[96:97], v[20:21], v[192:193] op_sel_hi:[1,0,1]
v_pk_fma_f32 v[194:195], v[98:99], v[20:21], v[194:195] op_sel_hi:[1,0,1]
s_waitcnt lgkmcnt(8)
v_pk_fma_f32 v[196:197], v[100:101], v[20:21], v[196:197] op_sel_hi:[1,0,1]
v_pk_fma_f32 v[198:199], v[102:103], v[20:21], v[198:199] op_sel_hi:[1,0,1]
s_waitcnt lgkmcnt(7)
v_pk_fma_f32 v[200:201], v[104:105], v[20:21], v[200:201] op_sel_hi:[1,0,1]
buffer_load_dwordx4 v42, s[24:27], s74 offen offset:2048 lds
v_pk_fma_f32 v[202:203], v[106:107], v[20:21], v[202:203] op_sel_hi:[1,0,1]
s_waitcnt lgkmcnt(6)
v_pk_fma_f32 v[204:205], v[108:109], v[20:21], v[204:205] op_sel_hi:[1,0,1]
v_pk_fma_f32 v[206:207], v[110:111], v[20:21], v[206:207] op_sel_hi:[1,0,1]
s_waitcnt lgkmcnt(5)
v_pk_fma_f32 v[208:209], v[112:113], v[20:21], v[208:209] op_sel_hi:[1,0,1]
v_pk_fma_f32 v[210:211], v[114:115], v[20:21], v[210:211] op_sel_hi:[1,0,1]
v_pk_mul_f32 v[156:157], v[212:213], v[156:157] op_sel:[1,0] op_sel_hi:[1,1]
v_pk_mul_f32 v[158:159], v[212:213], v[158:159] op_sel:[1,0] op_sel_hi:[1,1]
v_pk_mul_f32 v[160:161], v[214:215], v[160:161] op_sel:[1,0] op_sel_hi:[1,1]
v_pk_mul_f32 v[162:163], v[214:215], v[162:163] op_sel:[1,0] op_sel_hi:[1,1]
v_pk_mul_f32 v[164:165], v[216:217], v[164:165] op_sel:[1,0] op_sel_hi:[1,1]
v_pk_mul_f32 v[166:167], v[216:217], v[166:167] op_sel:[1,0] op_sel_hi:[1,1]
v_pk_mul_f32 v[168:169], v[218:219], v[168:169] op_sel:[1,0] op_sel_hi:[1,1]
buffer_load_dwordx4 v43, s[24:27], s74 offen offset:3072 lds
v_pk_mul_f32 v[170:171], v[218:219], v[170:171] op_sel:[1,0] op_sel_hi:[1,1]
v_pk_mul_f32 v[172:173], v[220:221], v[172:173] op_sel:[1,0] op_sel_hi:[1,1]
v_pk_mul_f32 v[174:175], v[220:221], v[174:175] op_sel:[1,0] op_sel_hi:[1,1]
s_waitcnt lgkmcnt(4)
v_pk_mul_f32 v[192:193], v[192:193], v[228:229]
v_pk_mul_f32 v[194:195], v[194:195], v[230:231]
s_waitcnt lgkmcnt(3)
v_pk_mul_f32 v[196:197], v[196:197], v[232:233]
v_pk_mul_f32 v[198:199], v[198:199], v[234:235]
s_waitcnt lgkmcnt(2)
v_pk_mul_f32 v[200:201], v[200:201], v[236:237]
v_pk_mul_f32 v[202:203], v[202:203], v[238:239]
s_waitcnt lgkmcnt(1)
v_pk_mul_f32 v[204:205], v[204:205], v[240:241]
v_pk_mul_f32 v[206:207], v[206:207], v[242:243]
s_waitcnt lgkmcnt(0)
v_pk_mul_f32 v[208:209], v[208:209], v[244:245]
v_lshrrev_b32 v18, 3, v0
v_min_u32 v18, 4, v18
v_mul_u32_u24 v18, 2048, v18
v_and_b32 v19, 7, v0
v_add_lshl_u32 v18, v18, v19, 7
s_mul_i32 s98, s43, 256
s_add_u32 s98, s98, s67
s_add_u32 s98, s98, 524288
s_bfm_b64 exec, 40, 0
buffer_load_dword v77, v18, s[28:31], s98 offen
s_mov_b64 exec, -1
v_pk_mul_f32 v[210:211], v[210:211], v[246:247]
v_cvt_pk_bf16_f32 v128, v156, v157
v_cvt_pk_bf16_f32 v129, v158, v159
v_cvt_pk_bf16_f32 v138, v176, v177
v_cvt_pk_bf16_f32 v139, v178, v179
v_cvt_pk_bf16_f32 v130, v160, v161
v_cvt_pk_bf16_f32 v131, v162, v163
v_cvt_pk_bf16_f32 v140, v180, v181
v_cvt_pk_bf16_f32 v141, v182, v183
v_mfma_f32_16x16x16_bf16 v[156:159], v[128:129], v[138:139], 0
v_cvt_pk_bf16_f32 v132, v164, v165
v_cvt_pk_bf16_f32 v133, v166, v167
v_mfma_f32_16x16x16_bf16 v[176:179], v[130:131], v[138:139], 0
v_cvt_pk_bf16_f32 v142, v184, v185
v_cvt_pk_bf16_f32 v143, v186, v187
v_mfma_f32_16x16x16_bf16 v[160:163], v[130:131], v[140:141], 0
v_cvt_pk_bf16_f32 v134, v168, v169
v_cvt_pk_bf16_f32 v135, v170, v171
v_mfma_f32_16x16x16_bf16 v[180:183], v[132:133], v[140:141], 0
v_cvt_pk_bf16_f32 v144, v188, v189
v_cvt_pk_bf16_f32 v145, v190, v191
v_mfma_f32_16x16x16_bf16 v[164:167], v[132:133], v[142:143], 0
v_cvt_pk_bf16_f32 v136, v172, v173
v_cvt_pk_bf16_f32 v137, v174, v175
v_mfma_f32_16x16x16_bf16 v[184:187], v[134:135], v[142:143], 0
v_cvt_pk_bf16_f32 v146, v192, v193
v_cvt_pk_bf16_f32 v147, v194, v195
v_mfma_f32_16x16x16_bf16 v[168:171], v[134:135], v[144:145], 0
v_cvt_pk_bf16_f32 v148, v196, v197
v_cvt_pk_bf16_f32 v149, v198, v199
v_mfma_f32_16x16x16_bf16 v[188:191], v[136:137], v[144:145], 0
v_cvt_pk_bf16_f32 v150, v200, v201
v_cvt_pk_bf16_f32 v151, v202, v203
v_cvt_pk_bf16_f32 v152, v204, v205
v_cvt_pk_bf16_f32 v153, v206, v207
v_cvt_pk_bf16_f32 v154, v208, v209
v_cvt_pk_bf16_f32 v155, v210, v211
v_mul_f32 v248, 0x3eb8aa3b, v215
v_mul_f32 v250, 0x3eb8aa3b, v217
v_mul_f32 v252, 0x3eb8aa3b, v219
v_mul_f32 v254, 0x3eb8aa3b, v221
v_cndmask_b32 v96, v156, v157, s[64:65]
v_cndmask_b32 v100, v160, v161, s[64:65]
v_cndmask_b32 v104, v164, v165, s[64:65]
v_cndmask_b32 v108, v168, v169, s[64:65]
v_cndmask_b32 v97, v158, v159, s[64:65]
v_cndmask_b32 v101, v162, v163, s[64:65]
v_cndmask_b32 v105, v166, v167, s[64:65]
v_cndmask_b32 v109, v170, v171, s[64:65]
v_cndmask_b32 v98, v176, v177, s[64:65]
v_cndmask_b32 v102, v180, v181, s[64:65]
v_cndmask_b32 v106, v184, v185, s[64:65]
v_cndmask_b32 v110, v188, v189, s[64:65]
v_cndmask_b32 v99, v178, v179, s[64:65]
v_cndmask_b32 v103, v182, v183, s[64:65]
v_cndmask_b32 v107, v186, v187, s[64:65]
v_cndmask_b32 v111, v190, v191, s[64:65]
v_pk_fma_f32 v[96:97], v[96:97], v[248:249], v[58:59] op_sel_hi:[1,0,1]
v_pk_fma_f32 v[100:101], v[100:101], v[250:251], v[54:55] op_sel_hi:[1,0,1]
v_pk_fma_f32 v[104:105], v[104:105], v[252:253], v[54:55] op_sel_hi:[1,0,1]
v_pk_fma_f32 v[108:109], v[108:109], v[254:255], v[54:55] op_sel_hi:[1,0,1]
v_pk_fma_f32 v[98:99], v[98:99], v[248:249], v[56:57] op_sel_hi:[1,0,1]
v_pk_fma_f32 v[102:103], v[102:103], v[250:251], v[56:57] op_sel_hi:[1,0,1]
v_pk_fma_f32 v[106:107], v[106:107], v[252:253], v[56:57] op_sel_hi:[1,0,1]
v_pk_fma_f32 v[110:111], v[110:111], v[254:255], v[56:57] op_sel_hi:[1,0,1]
v_max3_f32 v112, v96, v97, v98
v_max3_f32 v114, v100, v101, v102
v_max3_f32 v116, v104, v105, v106
v_max3_f32 v118, v108, v109, v110
v_max_f32 v112, v112, v99
v_max_f32 v114, v114, v103
v_max_f32 v116, v116, v107
v_max_f32 v118, v118, v111
v_mov_b32 v113, v112
v_mov_b32 v115, v114
v_mov_b32 v117, v116
v_mov_b32 v119, v118
v_permlane16_swap_b32 v112, v113
v_permlane16_swap_b32 v114, v115
v_permlane16_swap_b32 v116, v117
v_permlane16_swap_b32 v118, v119
v_max_f32 v112, v112, v113
v_max_f32 v114, v114, v115
v_max_f32 v116, v116, v117
v_max_f32 v118, v118, v119
v_mov_b32 v113, v112
v_mov_b32 v115, v114
v_mov_b32 v117, v116
v_mov_b32 v119, v118
v_permlane32_swap_b32 v112, v113
v_permlane32_swap_b32 v114, v115
v_permlane32_swap_b32 v116, v117
v_permlane32_swap_b32 v118, v119
v_max_f32 v112, v112, v113
v_max_f32 v114, v114, v115
v_max_f32 v116, v116, v117
v_max_f32 v118, v118, v119
v_pk_add_f32 v[96:97], v[96:97], v[112:113] op_sel_hi:[1,0] neg_lo:[0,1] neg_hi:[0,1]
v_pk_add_f32 v[100:101], v[100:101], v[114:115] op_sel_hi:[1,0] neg_lo:[0,1] neg_hi:[0,1]
v_pk_add_f32 v[104:105], v[104:105], v[116:117] op_sel_hi:[1,0] neg_lo:[0,1] neg_hi:[0,1]
v_pk_add_f32 v[108:109], v[108:109], v[118:119] op_sel_hi:[1,0] neg_lo:[0,1] neg_hi:[0,1]
v_pk_add_f32 v[98:99], v[98:99], v[112:113] op_sel_hi:[1,0] neg_lo:[0,1] neg_hi:[0,1]
v_pk_add_f32 v[102:103], v[102:103], v[114:115] op_sel_hi:[1,0] neg_lo:[0,1] neg_hi:[0,1]
v_pk_add_f32 v[106:107], v[106:107], v[116:117] op_sel_hi:[1,0] neg_lo:[0,1] neg_hi:[0,1]
v_pk_add_f32 v[110:111], v[110:111], v[118:119] op_sel_hi:[1,0] neg_lo:[0,1] neg_hi:[0,1]
v_exp_f32 v96, v96
v_exp_f32 v100, v100
v_exp_f32 v104, v104
v_exp_f32 v108, v108
v_exp_f32 v97, v97
v_exp_f32 v101, v101
v_exp_f32 v105, v105
v_exp_f32 v109, v109
v_exp_f32 v98, v98
v_exp_f32 v102, v102
v_exp_f32 v106, v106
v_exp_f32 v110, v110
v_exp_f32 v99, v99
v_exp_f32 v103, v103
v_exp_f32 v107, v107
v_exp_f32 v111, v111
v_cvt_pk_bf16_f32 v96, v96, v97
v_cvt_pk_bf16_f32 v100, v100, v101
v_cvt_pk_bf16_f32 v104, v104, v105
v_cvt_pk_bf16_f32 v108, v108, v109
v_cvt_pk_bf16_f32 v98, v98, v99
v_cvt_pk_bf16_f32 v102, v102, v103
v_cvt_pk_bf16_f32 v106, v106, v107
v_cvt_pk_bf16_f32 v110, v110, v111
v_perm_b32 v192, v96, v96, v60
v_perm_b32 v193, v96, v96, v61
v_perm_b32 v194, v98, v98, v60
v_perm_b32 v195, v98, v98, v61
v_perm_b32 v196, v100, v100, v60
v_perm_b32 v197, v100, v100, v61
v_mfma_f32_16x16x32_bf16 v[156:159], v[10:13], v[192:195], 0
v_perm_b32 v198, v102, v102, v60
v_perm_b32 v199, v102, v102, v61
v_mfma_f32_16x16x32_bf16 v[176:179], v[146:149], v[192:195], 0
v_perm_b32 v200, v104, v104, v60
v_perm_b32 v201, v104, v104, v61
v_mfma_f32_16x16x32_bf16 v[160:163], v[10:13], v[196:199], 0
v_perm_b32 v202, v106, v106, v60
v_perm_b32 v203, v106, v106, v61
v_mfma_f32_16x16x32_bf16 v[180:183], v[148:151], v[196:199], 0
v_perm_b32 v204, v108, v108, v60
v_perm_b32 v205, v108, v108, v61
v_mfma_f32_16x16x32_bf16 v[164:167], v[10:13], v[200:203], 0
v_perm_b32 v206, v110, v110, v60
v_perm_b32 v207, v110, v110, v61
v_mfma_f32_16x16x32_bf16 v[184:187], v[150:153], v[200:203], 0
v_rcp_f32 v156, v156
s_nop 0
v_mfma_f32_16x16x32_bf16 v[168:171], v[10:13], v[204:207], 0
v_rcp_f32 v160, v160
s_nop 0
v_mfma_f32_16x16x32_bf16 v[188:191], v[152:155], v[204:207], 0
v_pk_mul_f32 v[176:177], v[176:177], v[156:157] op_sel_hi:[1,0]
v_pk_mul_f32 v[180:181], v[180:181], v[160:161] op_sel_hi:[1,0]
v_pk_mul_f32 v[178:179], v[178:179], v[156:157] op_sel_hi:[1,0]
)ASM"
R"ASM(
v_pk_mul_f32 v[182:183], v[182:183], v[160:161] op_sel_hi:[1,0]
v_cvt_pk_bf16_f32 v120, v176, v177
v_cvt_pk_bf16_f32 v122, v180, v181
v_cvt_pk_bf16_f32 v121, v178, v179
v_cvt_pk_bf16_f32 v123, v182, v183
v_min_u32 v120, -2, v120
v_min_u32 v122, -2, v122
v_min_u32 v121, -2, v121
v_min_u32 v123, -2, v123
v_rcp_f32 v164, v164
v_rcp_f32 v168, v168
v_pk_mul_f32 v[184:185], v[184:185], v[164:165] op_sel_hi:[1,0]
v_pk_mul_f32 v[188:189], v[188:189], v[168:169] op_sel_hi:[1,0]
v_pk_mul_f32 v[186:187], v[186:187], v[164:165] op_sel_hi:[1,0]
v_pk_mul_f32 v[190:191], v[190:191], v[168:169] op_sel_hi:[1,0]
v_cvt_pk_bf16_f32 v124, v184, v185
v_cvt_pk_bf16_f32 v126, v188, v189
v_cvt_pk_bf16_f32 v125, v186, v187
v_cvt_pk_bf16_f32 v127, v190, v191
v_min_u32 v124, -2, v124
v_min_u32 v126, -2, v126
v_min_u32 v125, -2, v125
v_min_u32 v127, -2, v127
v_permlane16_swap_b32 v120, v122
v_permlane16_swap_b32 v121, v123
buffer_store_dwordx4 v[120:123], v65, s[4:7], s81 offen offset:0 sc1
v_permlane16_swap_b32 v124, v126
v_permlane16_swap_b32 v125, v127
buffer_store_dwordx4 v[124:127], v65, s[4:7], s72 offen offset:0 sc1
v_and_b32 v18, 7, v0
v_min_u32 v19, 6, v18
s_lshl_b32 s70, s55, 3
v_add_u32 v20, s70, v18
s_lshl_b32 s70, s54, 6
s_add_u32 s70, s70, s57
v_lshl_add_u32 v18, v18, 3, s70
s_mul_i32 s70, s54, 7
s_lshl_b32 s71, s56, 5
s_add_u32 s70, s70, s71
s_add_u32 s70, s70, 256
v_add_u32 v19, s70, v19
v_cmp_gt_u32 vcc, 8, v0
s_nop 1
v_cndmask_b32 v18, v18, v20, vcc
v_cmp_lt_u32 vcc, 15, v0
s_nop 1
v_cndmask_b32 v18, v18, v19, vcc
v_lshlrev_b32 v21, 7, v18
s_movk_i32 s72, 4096
buffer_load_dwordx2 v[252:253], v21, s[4:7], s72 offen sc1
s_add_u32 m0, s60, 16384
s_add_u32 s74, s79, 344064
buffer_load_dwordx4 v40, s[24:27], s74 offen offset:0 lds
s_waitcnt vmcnt(17)
ds_read_b64_tr_b16 a[192:193], v26 offset:4096
ds_read_b64_tr_b16 a[194:195], v26 offset:4608
ds_read_b64_tr_b16 a[196:197], v27 offset:4096
ds_read_b64_tr_b16 a[198:199], v27 offset:4608
ds_read_b64_tr_b16 a[200:201], v28 offset:4096
ds_read_b64_tr_b16 a[202:203], v28 offset:4608
ds_read_b64_tr_b16 a[204:205], v29 offset:4096
ds_read_b64_tr_b16 a[206:207], v29 offset:4608
buffer_load_dwordx4 v41, s[24:27], s74 offen offset:1024 lds
buffer_load_dwordx4 v42, s[24:27], s74 offen offset:2048 lds
buffer_load_dwordx4 v43, s[24:27], s74 offen offset:3072 lds
s_waitcnt lgkmcnt(0)
s_add_u32 m0, s60, 4096
s_add_u32 s74, s79, 458752
buffer_load_dwordx4 v40, s[24:27], s74 offen offset:0 lds
s_waitcnt vmcnt(13)
ds_read_b64_tr_b16 a[208:209], v26 offset:0
ds_read_b64_tr_b16 a[210:211], v26 offset:512
ds_read_b64_tr_b16 a[212:213], v27 offset:0
ds_read_b64_tr_b16 a[214:215], v27 offset:512
ds_read_b64_tr_b16 a[216:217], v28 offset:0
ds_read_b64_tr_b16 a[218:219], v28 offset:512
ds_read_b64_tr_b16 a[220:221], v29 offset:0
ds_read_b64_tr_b16 a[222:223], v29 offset:512
buffer_load_dwordx4 v41, s[24:27], s74 offen offset:1024 lds
buffer_load_dwordx4 v42, s[24:27], s74 offen offset:2048 lds
buffer_load_dwordx4 v43, s[24:27], s74 offen offset:3072 lds
s_waitcnt lgkmcnt(0)
s_add_u32 m0, s60, 0
s_add_u32 s74, s79, 573440
buffer_load_dwordx4 v40, s[24:27], s74 offen offset:0 lds
buffer_load_dwordx4 v41, s[24:27], s74 offen offset:1024 lds
buffer_load_dwordx4 v42, s[24:27], s74 offen offset:2048 lds
buffer_load_dwordx4 v43, s[24:27], s74 offen offset:3072 lds
s_barrier
ds_read_b64_tr_b16 a[224:225], v26 offset:8192
ds_read_b64_tr_b16 a[226:227], v26 offset:8704
ds_read_b64_tr_b16 a[228:229], v27 offset:8192
ds_read_b64_tr_b16 a[230:231], v27 offset:8704
ds_read_b64_tr_b16 a[232:233], v28 offset:8192
ds_read_b64_tr_b16 a[234:235], v28 offset:8704
ds_read_b64_tr_b16 a[236:237], v29 offset:8192
ds_read_b64_tr_b16 a[238:239], v29 offset:8704
s_waitcnt vmcnt(16)
ds_read_b64_tr_b16 a[240:241], v26 offset:12288
ds_read_b64_tr_b16 a[242:243], v26 offset:12800
s_waitcnt lgkmcnt(2)
s_add_u32 m0, s60, 8192
s_add_u32 s74, s79, 688128
buffer_load_dwordx4 v40, s[24:27], s74 offen offset:0 lds
ds_read_b64_tr_b16 a[244:245], v27 offset:12288
ds_read_b64_tr_b16 a[246:247], v27 offset:12800
ds_read_b64_tr_b16 a[248:249], v28 offset:12288
ds_read_b64_tr_b16 a[250:251], v28 offset:12800
ds_read_b64_tr_b16 a[252:253], v29 offset:12288
buffer_load_dwordx4 v41, s[24:27], s74 offen offset:1024 lds
ds_read_b64_tr_b16 a[254:255], v29 offset:12800
s_waitcnt vmcnt(10)
ds_read_b64_tr_b16 a[0:1], v26 offset:16384
ds_read_b64_tr_b16 a[2:3], v26 offset:16896
ds_read_b64_tr_b16 a[4:5], v27 offset:16384
ds_read_b64_tr_b16 a[6:7], v27 offset:16896
buffer_load_dwordx4 v42, s[24:27], s74 offen offset:2048 lds
ds_read_b64_tr_b16 a[8:9], v28 offset:16384
ds_read_b64_tr_b16 a[10:11], v28 offset:16896
ds_read_b64_tr_b16 a[12:13], v29 offset:16384
ds_read_b64_tr_b16 a[14:15], v29 offset:16896
s_lshl_b32 s70, s54, 5
v_xor_b32 v240, s70, v30
buffer_load_dwordx4 v43, s[24:27], s74 offen offset:3072 lds
v_add_u32 v240, 20480, v240
v_add_u32 v244, 73728, v240
v_xor_b32 v241, 32, v240
v_xor_b32 v242, 64, v240
v_xor_b32 v243, 96, v240
v_xor_b32 v245, 32, v244
v_xor_b32 v246, 64, v244
v_xor_b32 v247, 96, v244
s_waitcnt lgkmcnt(8)
s_add_u32 m0, s60, 12288
s_add_u32 s74, s79, 802816
buffer_load_dwordx4 v40, s[24:27], s74 offen offset:0 lds
v_xor_b32 v18, 0, v72
ds_read_b64_tr_b16 v[96:97], v18
v_xor_b32 v18, 32, v72
ds_read_b64_tr_b16 v[98:99], v18
v_xor_b32 v18, 64, v72
ds_read_b64_tr_b16 v[100:101], v18
v_xor_b32 v18, 96, v72
buffer_load_dwordx4 v41, s[24:27], s74 offen offset:1024 lds
ds_read_b64_tr_b16 v[102:103], v18
s_waitcnt lgkmcnt(3)
v_lshlrev_b32 v224, 16, v96
v_and_b32 v225, 0xffff0000, v96
v_lshlrev_b32 v226, 16, v97
v_and_b32 v227, 0xffff0000, v97
s_waitcnt lgkmcnt(2)
v_lshlrev_b32 v228, 16, v98
v_and_b32 v229, 0xffff0000, v98
v_lshlrev_b32 v230, 16, v99
buffer_load_dwordx4 v42, s[24:27], s74 offen offset:2048 lds
v_and_b32 v231, 0xffff0000, v99
s_waitcnt lgkmcnt(1)
v_lshlrev_b32 v232, 16, v100
v_and_b32 v233, 0xffff0000, v100
v_lshlrev_b32 v234, 16, v101
v_and_b32 v235, 0xffff0000, v101
s_waitcnt lgkmcnt(0)
v_lshlrev_b32 v236, 16, v102
v_and_b32 v237, 0xffff0000, v102
v_lshlrev_b32 v238, 16, v103
buffer_load_dwordx4 v43, s[24:27], s74 offen offset:3072 lds
v_and_b32 v239, 0xffff0000, v103
s_add_u32 m0, s60, 16384
s_add_u32 s74, s79, 917504
buffer_load_dwordx4 v40, s[24:27], s74 offen offset:0 lds
buffer_load_dwordx4 v41, s[24:27], s74 offen offset:1024 lds
buffer_load_dwordx4 v42, s[24:27], s74 offen offset:2048 lds
buffer_load_dwordx4 v43, s[24:27], s74 offen offset:3072 lds
v_cmp_ne_u64 vcc, s[62:63], v[252:253]
s_cbranch_vccnz .LS_3_%=
.LATT_%=:
s_add_u32 s75, s82, 0
buffer_load_dwordx4 v[96:99], v1, s[4:7], s75 offen sc1
ds_read_b64_tr_b16 v[160:161], v240 offset:0
ds_read_b64_tr_b16 v[162:163], v240 offset:512
ds_read_b64_tr_b16 v[164:165], v241 offset:0
ds_read_b64_tr_b16 v[166:167], v241 offset:512
ds_read_b64_tr_b16 v[168:169], v242 offset:0
ds_read_b64_tr_b16 v[170:171], v242 offset:512
s_add_u32 s75, s82, 4096
buffer_load_dwordx4 v[100:103], v1, s[4:7], s75 offen sc1
ds_read_b64_tr_b16 v[172:173], v243 offset:0
ds_read_b64_tr_b16 v[174:175], v243 offset:512
ds_read_b64_tr_b16 v[176:177], v240 offset:36864
ds_read_b64_tr_b16 v[178:179], v240 offset:37376
ds_read_b64_tr_b16 v[180:181], v241 offset:36864
ds_read_b64_tr_b16 v[182:183], v241 offset:37376
s_add_u32 s75, s82, 8192
buffer_load_dwordx4 v[104:107], v1, s[4:7], s75 offen sc1
ds_read_b64_tr_b16 v[184:185], v242 offset:36864
ds_read_b64_tr_b16 v[186:187], v242 offset:37376
ds_read_b64_tr_b16 v[188:189], v243 offset:36864
ds_read_b64_tr_b16 v[190:191], v243 offset:37376
ds_read_b64_tr_b16 v[192:193], v244 offset:0
ds_read_b64_tr_b16 v[194:195], v244 offset:512
s_add_u32 s75, s82, 12288
buffer_load_dwordx4 v[108:111], v1, s[4:7], s75 offen sc1
ds_read_b64_tr_b16 v[196:197], v245 offset:0
ds_read_b64_tr_b16 v[198:199], v245 offset:512
ds_read_b64_tr_b16 v[200:201], v246 offset:0
ds_read_b64_tr_b16 v[202:203], v246 offset:512
ds_read_b64_tr_b16 v[204:205], v247 offset:0
ds_read_b64_tr_b16 v[206:207], v247 offset:512
s_add_u32 s75, s82, 1024
buffer_load_dwordx4 v[112:115], v1, s[4:7], s75 offen sc1
ds_read_b64_tr_b16 v[208:209], v244 offset:36864
ds_read_b64_tr_b16 v[210:211], v244 offset:37376
ds_read_b64_tr_b16 v[212:213], v245 offset:36864
ds_read_b64_tr_b16 v[214:215], v245 offset:37376
ds_read_b64_tr_b16 v[216:217], v246 offset:36864
ds_read_b64_tr_b16 v[218:219], v246 offset:37376
s_add_u32 s75, s82, 5120
buffer_load_dwordx4 v[116:119], v1, s[4:7], s75 offen sc1
ds_read_b64_tr_b16 v[220:221], v247 offset:36864
ds_read_b64_tr_b16 v[222:223], v247 offset:37376
ds_read_b64_tr_b16 a[16:17], v240 offset:4096
ds_read_b64_tr_b16 a[18:19], v240 offset:4608
ds_read_b64_tr_b16 a[20:21], v241 offset:4096
ds_read_b64_tr_b16 a[22:23], v241 offset:4608
s_add_u32 s75, s82, 9216
buffer_load_dwordx4 v[120:123], v1, s[4:7], s75 offen sc1
ds_read_b64_tr_b16 a[24:25], v242 offset:4096
ds_read_b64_tr_b16 a[26:27], v242 offset:4608
ds_read_b64_tr_b16 a[28:29], v243 offset:4096
ds_read_b64_tr_b16 a[30:31], v243 offset:4608
ds_read_b64_tr_b16 a[32:33], v240 offset:40960
ds_read_b64_tr_b16 a[34:35], v240 offset:41472
s_add_u32 s75, s82, 13312
buffer_load_dwordx4 v[124:127], v1, s[4:7], s75 offen sc1
ds_read_b64_tr_b16 a[36:37], v241 offset:40960
ds_read_b64_tr_b16 a[38:39], v241 offset:41472
ds_read_b64_tr_b16 a[40:41], v242 offset:40960
ds_read_b64_tr_b16 a[42:43], v242 offset:41472
ds_read_b64_tr_b16 a[44:45], v243 offset:40960
ds_read_b64_tr_b16 a[46:47], v243 offset:41472
s_add_u32 s75, s82, 2048
buffer_load_dwordx4 v[128:131], v1, s[4:7], s75 offen sc1
ds_read_b64_tr_b16 a[48:49], v244 offset:4096
ds_read_b64_tr_b16 a[50:51], v244 offset:4608
ds_read_b64_tr_b16 a[52:53], v245 offset:4096
ds_read_b64_tr_b16 a[54:55], v245 offset:4608
ds_read_b64_tr_b16 a[56:57], v246 offset:4096
ds_read_b64_tr_b16 a[58:59], v246 offset:4608
s_add_u32 s75, s82, 6144
buffer_load_dwordx4 v[132:135], v1, s[4:7], s75 offen sc1
ds_read_b64_tr_b16 a[60:61], v247 offset:4096
ds_read_b64_tr_b16 a[62:63], v247 offset:4608
ds_read_b64_tr_b16 a[64:65], v244 offset:40960
ds_read_b64_tr_b16 a[66:67], v244 offset:41472
ds_read_b64_tr_b16 a[68:69], v245 offset:40960
ds_read_b64_tr_b16 a[70:71], v245 offset:41472
s_add_u32 s75, s82, 10240
buffer_load_dwordx4 v[136:139], v1, s[4:7], s75 offen sc1
ds_read_b64_tr_b16 a[72:73], v246 offset:40960
ds_read_b64_tr_b16 a[74:75], v246 offset:41472
ds_read_b64_tr_b16 a[76:77], v247 offset:40960
ds_read_b64_tr_b16 a[78:79], v247 offset:41472
ds_read_b64_tr_b16 a[80:81], v240 offset:8192
ds_read_b64_tr_b16 a[82:83], v240 offset:8704
s_add_u32 s75, s82, 14336
buffer_load_dwordx4 v[140:143], v1, s[4:7], s75 offen sc1
ds_read_b64_tr_b16 a[84:85], v241 offset:8192
ds_read_b64_tr_b16 a[86:87], v241 offset:8704
ds_read_b64_tr_b16 a[88:89], v242 offset:8192
ds_read_b64_tr_b16 a[90:91], v242 offset:8704
ds_read_b64_tr_b16 a[92:93], v243 offset:8192
ds_read_b64_tr_b16 a[94:95], v243 offset:8704
s_add_u32 s75, s82, 3072
buffer_load_dwordx4 v[144:147], v1, s[4:7], s75 offen sc1
ds_read_b64_tr_b16 a[96:97], v240 offset:45056
ds_read_b64_tr_b16 a[98:99], v240 offset:45568
ds_read_b64_tr_b16 a[100:101], v241 offset:45056
ds_read_b64_tr_b16 a[102:103], v241 offset:45568
ds_read_b64_tr_b16 a[104:105], v242 offset:45056
ds_read_b64_tr_b16 a[106:107], v242 offset:45568
s_add_u32 s75, s82, 7168
buffer_load_dwordx4 v[148:151], v1, s[4:7], s75 offen sc1
ds_read_b64_tr_b16 a[108:109], v243 offset:45056
ds_read_b64_tr_b16 a[110:111], v243 offset:45568
ds_read_b64_tr_b16 a[112:113], v244 offset:8192
ds_read_b64_tr_b16 a[114:115], v244 offset:8704
ds_read_b64_tr_b16 a[116:117], v245 offset:8192
ds_read_b64_tr_b16 a[118:119], v245 offset:8704
s_add_u32 s75, s82, 11264
buffer_load_dwordx4 v[152:155], v1, s[4:7], s75 offen sc1
ds_read_b64_tr_b16 a[120:121], v246 offset:8192
ds_read_b64_tr_b16 a[122:123], v246 offset:8704
ds_read_b64_tr_b16 a[124:125], v247 offset:8192
ds_read_b64_tr_b16 a[126:127], v247 offset:8704
ds_read_b64_tr_b16 a[128:129], v244 offset:45056
ds_read_b64_tr_b16 a[130:131], v244 offset:45568
)ASM"
R"ASM(
s_add_u32 s75, s82, 15360
buffer_load_dwordx4 v[156:159], v1, s[4:7], s75 offen sc1
ds_read_b64_tr_b16 a[132:133], v245 offset:45056
ds_read_b64_tr_b16 a[134:135], v245 offset:45568
ds_read_b64_tr_b16 a[136:137], v246 offset:45056
ds_read_b64_tr_b16 a[138:139], v246 offset:45568
ds_read_b64_tr_b16 a[140:141], v247 offset:45056
ds_read_b64_tr_b16 a[142:143], v247 offset:45568
s_waitcnt lgkmcnt(0)
s_barrier
s_waitcnt vmcnt(14)
v_max3_u32 v18, v97, v99, v101
v_max_u32 v18, v18, v103
v_cmp_eq_u32 vcc, -1, v18
s_cbranch_vccnz .LS_4_%=
.LB_5_%=:
v_mfma_f32_16x16x32_bf16 v[224:227], v[96:99], v[160:163], v[224:227]
s_add_u32 m0, s60, 20480
s_add_u32 s74, s79, 1032192
v_mfma_f32_16x16x32_bf16 v[228:231], v[96:99], v[164:167], v[228:231]
buffer_load_dwordx4 v40, s[24:27], s74 offen offset:0 lds
v_mfma_f32_16x16x32_bf16 v[232:235], v[96:99], v[168:171], v[232:235]
s_waitcnt vmcnt(13)
v_max3_u32 v18, v105, v107, v109
v_max_u32 v18, v18, v111
v_mfma_f32_16x16x32_bf16 v[236:239], v[96:99], v[172:175], v[236:239]
v_cmp_eq_u32 vcc, -1, v18
v_mfma_f32_16x16x32_bf16 v[224:227], v[100:103], v[176:179], v[224:227]
ds_read_b64_tr_b16 v[160:161], v240 offset:12288
ds_read_b64_tr_b16 v[162:163], v240 offset:12800
v_mfma_f32_16x16x32_bf16 v[228:231], v[100:103], v[180:183], v[228:231]
s_cbranch_vccnz .LS_7_%=
.LB_8_%=:
v_mfma_f32_16x16x32_bf16 v[232:235], v[100:103], v[184:187], v[232:235]
buffer_load_dwordx4 v41, s[24:27], s74 offen offset:1024 lds
v_mfma_f32_16x16x32_bf16 v[236:239], v[100:103], v[188:191], v[236:239]
ds_read_b64_tr_b16 v[164:165], v241 offset:12288
ds_read_b64_tr_b16 v[166:167], v241 offset:12800
v_mfma_f32_16x16x32_bf16 v[224:227], v[104:107], v[192:195], v[224:227]
ds_read_b64_tr_b16 v[168:169], v242 offset:12288
ds_read_b64_tr_b16 v[170:171], v242 offset:12800
v_mfma_f32_16x16x32_bf16 v[228:231], v[104:107], v[196:199], v[228:231]
ds_read_b64_tr_b16 v[172:173], v243 offset:12288
ds_read_b64_tr_b16 v[174:175], v243 offset:12800
v_mfma_f32_16x16x32_bf16 v[232:235], v[104:107], v[200:203], v[232:235]
s_waitcnt vmcnt(12)
v_max3_u32 v18, v113, v115, v117
v_max_u32 v18, v18, v119
v_mfma_f32_16x16x32_bf16 v[236:239], v[104:107], v[204:207], v[236:239]
ds_read_b64_tr_b16 v[176:177], v240 offset:49152
v_cmp_eq_u32 vcc, -1, v18
v_mfma_f32_16x16x32_bf16 v[224:227], v[108:111], v[208:211], v[224:227]
buffer_load_dwordx4 v42, s[24:27], s74 offen offset:2048 lds
v_mfma_f32_16x16x32_bf16 v[228:231], v[108:111], v[212:215], v[228:231]
s_cbranch_vccnz .LS_10_%=
.LB_11_%=:
v_mfma_f32_16x16x32_bf16 v[232:235], v[108:111], v[216:219], v[232:235]
ds_read_b64_tr_b16 v[178:179], v240 offset:49664
ds_read_b64_tr_b16 v[180:181], v241 offset:49152
v_mfma_f32_16x16x32_bf16 v[236:239], v[108:111], v[220:223], v[236:239]
ds_read_b64_tr_b16 v[182:183], v241 offset:49664
ds_read_b64_tr_b16 v[184:185], v242 offset:49152
v_mfma_f32_16x16x32_bf16 v[224:227], v[112:115], a[16:19], v[224:227]
ds_read_b64_tr_b16 v[186:187], v242 offset:49664
ds_read_b64_tr_b16 v[188:189], v243 offset:49152
v_mfma_f32_16x16x32_bf16 v[228:231], v[112:115], a[20:23], v[228:231]
buffer_load_dwordx4 v43, s[24:27], s74 offen offset:3072 lds
v_mfma_f32_16x16x32_bf16 v[232:235], v[112:115], a[24:27], v[232:235]
s_waitcnt vmcnt(12)
v_max3_u32 v18, v121, v123, v125
v_max_u32 v18, v18, v127
v_mfma_f32_16x16x32_bf16 v[236:239], v[112:115], a[28:31], v[236:239]
s_add_u32 m0, s60, 24576
s_add_u32 s74, s79, 1146880
v_cmp_eq_u32 vcc, -1, v18
v_mfma_f32_16x16x32_bf16 v[224:227], v[116:119], a[32:35], v[224:227]
ds_read_b64_tr_b16 v[190:191], v243 offset:49664
ds_read_b64_tr_b16 v[192:193], v244 offset:12288
v_mfma_f32_16x16x32_bf16 v[228:231], v[116:119], a[36:39], v[228:231]
s_cbranch_vccnz .LS_13_%=
.LB_14_%=:
v_mfma_f32_16x16x32_bf16 v[232:235], v[116:119], a[40:43], v[232:235]
buffer_load_dwordx4 v40, s[24:27], s74 offen offset:0 lds
v_mfma_f32_16x16x32_bf16 v[236:239], v[116:119], a[44:47], v[236:239]
ds_read_b64_tr_b16 v[194:195], v244 offset:12800
ds_read_b64_tr_b16 v[196:197], v245 offset:12288
v_mfma_f32_16x16x32_bf16 v[224:227], v[120:123], a[48:51], v[224:227]
ds_read_b64_tr_b16 v[198:199], v245 offset:12800
ds_read_b64_tr_b16 v[200:201], v246 offset:12288
v_mfma_f32_16x16x32_bf16 v[228:231], v[120:123], a[52:55], v[228:231]
ds_read_b64_tr_b16 v[202:203], v246 offset:12800
ds_read_b64_tr_b16 v[204:205], v247 offset:12288
v_mfma_f32_16x16x32_bf16 v[232:235], v[120:123], a[56:59], v[232:235]
s_waitcnt vmcnt(11)
v_max3_u32 v18, v129, v131, v133
v_max_u32 v18, v18, v135
v_mfma_f32_16x16x32_bf16 v[236:239], v[120:123], a[60:63], v[236:239]
ds_read_b64_tr_b16 v[206:207], v247 offset:12800
v_cmp_eq_u32 vcc, -1, v18
v_mfma_f32_16x16x32_bf16 v[224:227], v[124:127], a[64:67], v[224:227]
buffer_load_dwordx4 v41, s[24:27], s74 offen offset:1024 lds
v_mfma_f32_16x16x32_bf16 v[228:231], v[124:127], a[68:71], v[228:231]
s_cbranch_vccnz .LS_16_%=
.LB_17_%=:
v_mfma_f32_16x16x32_bf16 v[232:235], v[124:127], a[72:75], v[232:235]
ds_read_b64_tr_b16 v[208:209], v244 offset:49152
ds_read_b64_tr_b16 v[210:211], v244 offset:49664
v_mfma_f32_16x16x32_bf16 v[236:239], v[124:127], a[76:79], v[236:239]
ds_read_b64_tr_b16 v[212:213], v245 offset:49152
ds_read_b64_tr_b16 v[214:215], v245 offset:49664
v_mfma_f32_16x16x32_bf16 v[224:227], v[128:131], a[80:83], v[224:227]
ds_read_b64_tr_b16 v[216:217], v246 offset:49152
ds_read_b64_tr_b16 v[218:219], v246 offset:49664
v_mfma_f32_16x16x32_bf16 v[228:231], v[128:131], a[84:87], v[228:231]
buffer_load_dwordx4 v42, s[24:27], s74 offen offset:2048 lds
v_mfma_f32_16x16x32_bf16 v[232:235], v[128:131], a[88:91], v[232:235]
s_waitcnt vmcnt(11)
v_max3_u32 v18, v137, v139, v141
v_max_u32 v18, v18, v143
v_mfma_f32_16x16x32_bf16 v[236:239], v[128:131], a[92:95], v[236:239]
ds_read_b64_tr_b16 v[220:221], v247 offset:49152
v_cmp_eq_u32 vcc, -1, v18
v_mfma_f32_16x16x32_bf16 v[224:227], v[132:135], a[96:99], v[224:227]
ds_read_b64_tr_b16 v[222:223], v247 offset:49664
v_mfma_f32_16x16x32_bf16 v[228:231], v[132:135], a[100:103], v[228:231]
s_cbranch_vccnz .LS_19_%=
.LB_20_%=:
v_mfma_f32_16x16x32_bf16 v[232:235], v[132:135], a[104:107], v[232:235]
buffer_load_dwordx4 v43, s[24:27], s74 offen offset:3072 lds
v_mfma_f32_16x16x32_bf16 v[236:239], v[132:135], a[108:111], v[236:239]
s_add_u32 m0, s60, 28672
s_add_u32 s74, s79, 1261568
v_mfma_f32_16x16x32_bf16 v[224:227], v[136:139], a[112:115], v[224:227]
v_mfma_f32_16x16x32_bf16 v[228:231], v[136:139], a[116:119], v[228:231]
v_mfma_f32_16x16x32_bf16 v[232:235], v[136:139], a[120:123], v[232:235]
s_waitcnt vmcnt(10)
v_max3_u32 v18, v145, v147, v149
v_max_u32 v18, v18, v151
v_mfma_f32_16x16x32_bf16 v[236:239], v[136:139], a[124:127], v[236:239]
v_cmp_eq_u32 vcc, -1, v18
v_mfma_f32_16x16x32_bf16 v[224:227], v[140:143], a[128:131], v[224:227]
buffer_load_dwordx4 v40, s[24:27], s74 offen offset:0 lds
v_mfma_f32_16x16x32_bf16 v[228:231], v[140:143], a[132:135], v[228:231]
s_cbranch_vccnz .LS_22_%=
.LB_23_%=:
v_mfma_f32_16x16x32_bf16 v[232:235], v[140:143], a[136:139], v[232:235]
v_mfma_f32_16x16x32_bf16 v[236:239], v[140:143], a[140:143], v[236:239]
s_waitcnt lgkmcnt(15)
v_mfma_f32_16x16x32_bf16 v[224:227], v[144:147], v[160:163], v[224:227]
v_mfma_f32_16x16x32_bf16 v[228:231], v[144:147], v[164:167], v[228:231]
buffer_load_dwordx4 v41, s[24:27], s74 offen offset:1024 lds
v_mfma_f32_16x16x32_bf16 v[232:235], v[144:147], v[168:171], v[232:235]
s_waitcnt vmcnt(10)
v_max3_u32 v18, v153, v155, v157
v_max_u32 v18, v18, v159
v_mfma_f32_16x16x32_bf16 v[236:239], v[144:147], v[172:175], v[236:239]
v_cmp_eq_u32 vcc, -1, v18
v_mfma_f32_16x16x32_bf16 v[224:227], v[148:151], v[176:179], v[224:227]
v_mfma_f32_16x16x32_bf16 v[228:231], v[148:151], v[180:183], v[228:231]
s_cbranch_vccnz .LS_25_%=
.LB_26_%=:
v_mfma_f32_16x16x32_bf16 v[232:235], v[148:151], v[184:187], v[232:235]
buffer_load_dwordx4 v42, s[24:27], s74 offen offset:2048 lds
v_mfma_f32_16x16x32_bf16 v[236:239], v[148:151], v[188:191], v[236:239]
s_waitcnt lgkmcnt(14)
v_mfma_f32_16x16x32_bf16 v[224:227], v[152:155], v[192:195], v[224:227]
s_waitcnt lgkmcnt(12)
v_mfma_f32_16x16x32_bf16 v[228:231], v[152:155], v[196:199], v[228:231]
s_waitcnt lgkmcnt(10)
v_mfma_f32_16x16x32_bf16 v[232:235], v[152:155], v[200:203], v[232:235]
s_waitcnt lgkmcnt(8)
v_mfma_f32_16x16x32_bf16 v[236:239], v[152:155], v[204:207], v[236:239]
buffer_load_dwordx4 v43, s[24:27], s74 offen offset:3072 lds
s_waitcnt lgkmcnt(6)
v_mfma_f32_16x16x32_bf16 v[224:227], v[156:159], v[208:211], v[224:227]
s_waitcnt lgkmcnt(4)
v_mfma_f32_16x16x32_bf16 v[228:231], v[156:159], v[212:215], v[228:231]
s_waitcnt lgkmcnt(2)
v_mfma_f32_16x16x32_bf16 v[232:235], v[156:159], v[216:219], v[232:235]
s_waitcnt lgkmcnt(0)
v_mfma_f32_16x16x32_bf16 v[236:239], v[156:159], v[220:223], v[236:239]
v_bfe_u32 v19, v0, 4, 2
v_and_b32 v20, 15, v0
v_lshlrev_b32 v19, 3, v19
v_lshl_or_b32 v19, v20, 5, v19
s_lshl_b32 s70, s54, 11
s_add_u32 s70, s70, 147456
v_add_u32 v19, s70, v19
s_lshl_b32 s70, s56, 7
s_add_u32 s70, s70, s87
v_cvt_pk_bf16_f32 v96, v224, v225
v_cvt_pk_bf16_f32 v97, v226, v227
v_cvt_pk_bf16_f32 v98, v228, v229
v_cvt_pk_bf16_f32 v99, v230, v231
v_cvt_pk_bf16_f32 v100, v232, v233
v_cvt_pk_bf16_f32 v101, v234, v235
v_cvt_pk_bf16_f32 v102, v236, v237
v_cvt_pk_bf16_f32 v103, v238, v239
v_lshlrev_b32 v104, 16, v96
v_and_b32 v105, 0xffff0000, v96
v_lshlrev_b32 v106, 16, v97
v_and_b32 v107, 0xffff0000, v97
v_lshlrev_b32 v108, 16, v98
v_and_b32 v109, 0xffff0000, v98
v_lshlrev_b32 v110, 16, v99
v_and_b32 v111, 0xffff0000, v99
v_lshlrev_b32 v112, 16, v100
v_and_b32 v113, 0xffff0000, v100
v_lshlrev_b32 v114, 16, v101
v_and_b32 v115, 0xffff0000, v101
v_lshlrev_b32 v116, 16, v102
v_and_b32 v117, 0xffff0000, v102
v_lshlrev_b32 v118, 16, v103
v_and_b32 v119, 0xffff0000, v103
v_pk_add_f32 v[120:121], v[104:105], v[108:109]
v_pk_mul_f32 v[124:125], v[104:105], v[104:105]
v_pk_add_f32 v[122:123], v[106:107], v[110:111]
v_pk_mul_f32 v[126:127], v[106:107], v[106:107]
v_pk_fma_f32 v[124:125], v[108:109], v[108:109], v[124:125]
v_pk_fma_f32 v[126:127], v[110:111], v[110:111], v[126:127]
v_pk_add_f32 v[120:121], v[120:121], v[112:113]
v_pk_fma_f32 v[124:125], v[112:113], v[112:113], v[124:125]
v_pk_add_f32 v[122:123], v[122:123], v[114:115]
v_pk_fma_f32 v[126:127], v[114:115], v[114:115], v[126:127]
v_pk_add_f32 v[120:121], v[120:121], v[116:117]
v_pk_fma_f32 v[124:125], v[116:117], v[116:117], v[124:125]
v_pk_add_f32 v[122:123], v[122:123], v[118:119]
v_pk_fma_f32 v[126:127], v[118:119], v[118:119], v[126:127]
v_cndmask_b32 v128, v120, v122, s[64:65]
v_cndmask_b32 v132, v122, v120, s[64:65]
v_cndmask_b32 v129, v124, v126, s[64:65]
v_cndmask_b32 v133, v126, v124, s[64:65]
v_cndmask_b32 v130, v121, v123, s[64:65]
v_cndmask_b32 v134, v123, v121, s[64:65]
v_cndmask_b32 v131, v125, v127, s[64:65]
v_cndmask_b32 v135, v127, v125, s[64:65]
v_add_f32_dpp v128, v132, v128 quad_perm:[1,0,3,2] row_mask:0xf bank_mask:0xf
v_add_f32_dpp v129, v133, v129 quad_perm:[1,0,3,2] row_mask:0xf bank_mask:0xf
v_add_f32_dpp v130, v134, v130 quad_perm:[1,0,3,2] row_mask:0xf bank_mask:0xf
v_add_f32_dpp v131, v135, v131 quad_perm:[1,0,3,2] row_mask:0xf bank_mask:0xf
v_add_f32_dpp v128, v128, v128 row_ror:2 row_mask:0xf bank_mask:0xf
v_add_f32_dpp v129, v129, v129 row_ror:2 row_mask:0xf bank_mask:0xf
v_add_f32_dpp v130, v130, v130 row_ror:2 row_mask:0xf bank_mask:0xf
v_add_f32_dpp v131, v131, v131 row_ror:2 row_mask:0xf bank_mask:0xf
v_add_f32_dpp v128, v128, v128 row_ror:4 row_mask:0xf bank_mask:0xf
v_add_f32_dpp v129, v129, v129 row_ror:4 row_mask:0xf bank_mask:0xf
v_add_f32_dpp v130, v130, v130 row_ror:4 row_mask:0xf bank_mask:0xf
v_add_f32_dpp v131, v131, v131 row_ror:4 row_mask:0xf bank_mask:0xf
v_add_f32_dpp v128, v128, v128 row_ror:8 row_mask:0xf bank_mask:0xf
v_add_f32_dpp v129, v129, v129 row_ror:8 row_mask:0xf bank_mask:0xf
v_add_f32_dpp v130, v130, v130 row_ror:8 row_mask:0xf bank_mask:0xf
v_add_f32_dpp v131, v131, v131 row_ror:8 row_mask:0xf bank_mask:0xf
v_min_u32 v128, -2, v128
v_min_u32 v129, -2, v129
v_min_u32 v130, -2, v130
v_min_u32 v131, -2, v131
)ASM"
R"ASM(
v_cmp_gt_u32 vcc, 2, v20
s_and_saveexec_b64 s[68:69], vcc
buffer_store_dwordx4 v[128:131], v71, s[4:7], s70 offen sc1
s_mov_b64 exec, s[68:69]
ds_write_b64 v19, v[96:97] offset:0
ds_write_b64 v19, v[98:99] offset:512
ds_write_b64 v19, v[100:101] offset:1024
ds_write_b64 v19, v[102:103] offset:1536
ds_read_b64_tr_b16 a[16:17], v26 offset:4096
ds_read_b64_tr_b16 a[18:19], v26 offset:4608
ds_read_b64_tr_b16 a[20:21], v27 offset:4096
ds_read_b64_tr_b16 a[22:23], v27 offset:4608
ds_read_b64_tr_b16 a[24:25], v28 offset:4096
ds_read_b64_tr_b16 a[26:27], v28 offset:4608
ds_read_b64_tr_b16 a[28:29], v29 offset:4096
ds_read_b64_tr_b16 a[30:31], v29 offset:4608
ds_read_b64_tr_b16 a[32:33], v26 offset:0
s_waitcnt lgkmcnt(1)
s_add_u32 m0, s60, 4096
s_add_u32 s74, s80, 0
buffer_load_dwordx4 v44, s[28:31], s74 offen offset:0 lds
ds_read_b64_tr_b16 a[34:35], v26 offset:512
ds_read_b64_tr_b16 a[36:37], v27 offset:0
ds_read_b64_tr_b16 a[38:39], v27 offset:512
ds_read_b64_tr_b16 a[40:41], v28 offset:0
ds_read_b64_tr_b16 a[42:43], v28 offset:512
buffer_load_dwordx4 v45, s[28:31], s74 offen offset:1024 lds
ds_read_b64_tr_b16 a[44:45], v29 offset:0
ds_read_b64_tr_b16 a[46:47], v29 offset:512
ds_read_b64_tr_b16 a[48:49], v26 offset:8192
ds_read_b64_tr_b16 a[50:51], v26 offset:8704
ds_read_b64_tr_b16 a[52:53], v27 offset:8192
buffer_load_dwordx4 v46, s[28:31], s74 offen offset:2048 lds
ds_read_b64_tr_b16 a[54:55], v27 offset:8704
ds_read_b64_tr_b16 a[56:57], v28 offset:8192
ds_read_b64_tr_b16 a[58:59], v28 offset:8704
ds_read_b64_tr_b16 a[60:61], v29 offset:8192
ds_read_b64_tr_b16 a[62:63], v29 offset:8704
buffer_load_dwordx4 v47, s[28:31], s74 offen offset:3072 lds
s_waitcnt lgkmcnt(8)
s_add_u32 m0, s60, 0
s_add_u32 s74, s80, 131072
buffer_load_dwordx4 v44, s[28:31], s74 offen offset:0 lds
buffer_load_dwordx4 v45, s[28:31], s74 offen offset:1024 lds
buffer_load_dwordx4 v46, s[28:31], s74 offen offset:2048 lds
buffer_load_dwordx4 v47, s[28:31], s74 offen offset:3072 lds
buffer_load_dwordx4 v[128:131], v70, s[4:7], s87 offen sc1
ds_read_b64_tr_b16 a[64:65], v26 offset:12288
ds_read_b64_tr_b16 a[66:67], v26 offset:12800
ds_read_b64_tr_b16 a[68:69], v27 offset:12288
ds_read_b64_tr_b16 a[70:71], v27 offset:12800
s_waitcnt lgkmcnt(4)
s_add_u32 m0, s60, 8192
s_add_u32 s74, s80, 262144
buffer_load_dwordx4 v44, s[28:31], s74 offen offset:0 lds
ds_read_b64_tr_b16 a[72:73], v28 offset:12288
ds_read_b64_tr_b16 a[74:75], v28 offset:12800
ds_read_b64_tr_b16 a[76:77], v29 offset:12288
ds_read_b64_tr_b16 a[78:79], v29 offset:12800
buffer_load_dwordx4 v45, s[28:31], s74 offen offset:1024 lds
ds_read_b64_tr_b16 a[80:81], v26 offset:16384
ds_read_b64_tr_b16 a[82:83], v26 offset:16896
ds_read_b64_tr_b16 a[84:85], v27 offset:16384
ds_read_b64_tr_b16 a[86:87], v27 offset:16896
buffer_load_dwordx4 v46, s[28:31], s74 offen offset:2048 lds
ds_read_b64_tr_b16 a[88:89], v28 offset:16384
ds_read_b64_tr_b16 a[90:91], v28 offset:16896
ds_read_b64_tr_b16 a[92:93], v29 offset:16384
ds_read_b64_tr_b16 a[94:95], v29 offset:16896
buffer_load_dwordx4 v47, s[28:31], s74 offen offset:3072 lds
s_waitcnt lgkmcnt(8)
s_add_u32 m0, s60, 12288
s_add_u32 s74, s80, 393216
buffer_load_dwordx4 v44, s[28:31], s74 offen offset:0 lds
buffer_load_dwordx4 v45, s[28:31], s74 offen offset:1024 lds
buffer_load_dwordx4 v46, s[28:31], s74 offen offset:2048 lds
buffer_load_dwordx4 v47, s[28:31], s74 offen offset:3072 lds
s_waitcnt lgkmcnt(0)
s_add_u32 m0, s60, 16384
s_add_u32 s74, s80, 524288
buffer_load_dwordx4 v44, s[28:31], s74 offen offset:0 lds
buffer_load_dwordx4 v45, s[28:31], s74 offen offset:1024 lds
s_waitcnt vmcnt(10)
v_max3_u32 v18, v128, v129, v130
v_max_u32 v18, v18, v131
v_cmp_eq_u32 vcc, -1, v18
s_cbranch_vccnz .LS_28_%=
.LB_29_%=:
v_add_f32_dpp v128, v128, v128 row_ror:2 row_mask:0xf bank_mask:0xf
v_add_f32_dpp v129, v129, v129 row_ror:2 row_mask:0xf bank_mask:0xf
v_add_f32_dpp v130, v130, v130 row_ror:2 row_mask:0xf bank_mask:0xf
v_add_f32_dpp v131, v131, v131 row_ror:2 row_mask:0xf bank_mask:0xf
v_add_f32_dpp v128, v128, v128 row_ror:4 row_mask:0xf bank_mask:0xf
v_add_f32_dpp v129, v129, v129 row_ror:4 row_mask:0xf bank_mask:0xf
v_add_f32_dpp v130, v130, v130 row_ror:4 row_mask:0xf bank_mask:0xf
v_add_f32_dpp v131, v131, v131 row_ror:4 row_mask:0xf bank_mask:0xf
v_add_f32_dpp v128, v128, v128 row_ror:8 row_mask:0xf bank_mask:0xf
v_add_f32_dpp v129, v129, v129 row_ror:8 row_mask:0xf bank_mask:0xf
v_add_f32_dpp v130, v130, v130 row_ror:8 row_mask:0xf bank_mask:0xf
v_add_f32_dpp v131, v131, v131 row_ror:8 row_mask:0xf bank_mask:0xf
v_mul_f32 v128, 0xbb000000, v128
v_mul_f32 v130, 0xbb000000, v130
v_mul_f32 v129, 0x3b000000, v129
v_mul_f32 v131, 0x3b000000, v131
v_fma_f32 v129, -v128, v128, v129
v_fma_f32 v131, -v130, v130, v131
v_max_f32 v129, 0, v129
v_max_f32 v131, 0, v131
v_add_f32 v18, 0x3727c5ac, v129
v_add_f32 v19, 0x3727c5ac, v131
v_rsq_f32 v18, v18
v_rsq_f32 v19, v19
v_mul_f32 v129, v129, v18
v_mul_f32 v131, v131, v19
v_mul_f32 v129, v129, v18
v_mul_f32 v131, v131, v19
v_add_f32 v129, 0x3727c5ac, v129
v_add_f32 v131, 0x3727c5ac, v131
v_rsq_f32 v129, v129
v_rsq_f32 v131, v131
v_mul_f32 v129, v129, v18
v_mul_f32 v131, v131, v19
v_mul_f32 v128, v128, v129
v_mul_f32 v130, v130, v131
v_mov_b32_dpp v132, v129 quad_perm:[0,0,2,2] row_mask:0xf bank_mask:0xf bound_ctrl:1
v_mov_b32_dpp v136, v128 quad_perm:[0,0,2,2] row_mask:0xf bank_mask:0xf bound_ctrl:1
v_mov_b32_dpp v133, v131 quad_perm:[0,0,2,2] row_mask:0xf bank_mask:0xf bound_ctrl:1
v_mov_b32_dpp v137, v130 quad_perm:[0,0,2,2] row_mask:0xf bank_mask:0xf bound_ctrl:1
v_mov_b32_dpp v134, v129 quad_perm:[1,1,3,3] row_mask:0xf bank_mask:0xf bound_ctrl:1
v_mov_b32_dpp v138, v128 quad_perm:[1,1,3,3] row_mask:0xf bank_mask:0xf bound_ctrl:1
v_mov_b32_dpp v135, v131 quad_perm:[1,1,3,3] row_mask:0xf bank_mask:0xf bound_ctrl:1
v_mov_b32_dpp v139, v130 quad_perm:[1,1,3,3] row_mask:0xf bank_mask:0xf bound_ctrl:1
v_pk_fma_f32 v[104:105], v[104:105], v[132:133], v[136:137]
v_pk_fma_f32 v[106:107], v[106:107], v[134:135], v[138:139]
v_pk_fma_f32 v[108:109], v[108:109], v[132:133], v[136:137]
v_pk_fma_f32 v[110:111], v[110:111], v[134:135], v[138:139]
v_pk_fma_f32 v[112:113], v[112:113], v[132:133], v[136:137]
v_pk_fma_f32 v[114:115], v[114:115], v[134:135], v[138:139]
v_pk_fma_f32 v[116:117], v[116:117], v[132:133], v[136:137]
v_pk_fma_f32 v[118:119], v[118:119], v[134:135], v[138:139]
v_cvt_pk_bf16_f32 v96, v104, v105
v_cvt_pk_bf16_f32 v97, v106, v107
v_cvt_pk_bf16_f32 v98, v108, v109
v_cvt_pk_bf16_f32 v99, v110, v111
v_cvt_pk_bf16_f32 v100, v112, v113
v_cvt_pk_bf16_f32 v101, v114, v115
v_cvt_pk_bf16_f32 v102, v116, v117
v_cvt_pk_bf16_f32 v103, v118, v119
v_min_u32 v96, -2, v96
v_min_u32 v97, -2, v97
v_min_u32 v98, -2, v98
v_min_u32 v99, -2, v99
v_min_u32 v100, -2, v100
v_min_u32 v101, -2, v101
v_min_u32 v102, -2, v102
v_min_u32 v103, -2, v103
s_add_u32 s72, s83, 2048
v_permlane16_swap_b32 v96, v98
v_permlane16_swap_b32 v97, v99
buffer_store_dwordx4 v[96:99], v66, s[4:7], s83 offen offset:0 sc1
v_permlane16_swap_b32 v100, v102
v_permlane16_swap_b32 v101, v103
buffer_store_dwordx4 v[100:103], v66, s[4:7], s72 offen offset:0 sc1
s_barrier
s_getpc_b64 s[68:69]
.LPC_31_%=:
s_lshl_b32 s98, s97, 7
s_mul_i32 s99, s59, .LLAST_%=-.LJOIN_%=
s_add_u32 s98, s98, s99
s_add_u32 s98, s98, .LJOIN_%=-.LPC_31_%=
s_min_u32 s98, s98, .LDONE_%=-.LPC_31_%=-4
v_mov_b32 v22, s98
s_mov_b64 exec, 1
global_load_dword v77, v22, s[68:69]
s_mov_b64 exec, -1
s_cmp_eq_u32 s59, 1
s_cbranch_scc1 .LK_32_%=
v_mov_b32 v252, -1
v_mov_b32 v253, -1
v_mov_b32 v254, -1
v_mov_b32 v255, -1
s_add_u32 s70, s84, 2359296
buffer_store_dwordx4 v[252:255], v66, s[8:11], s70 offen sc1
s_add_u32 s70, s84, 2361344
buffer_store_dwordx4 v[252:255], v66, s[8:11], s70 offen sc1
.LK_32_%=:
ds_read_b64_tr_b16 a[112:113], v26 offset:24576
buffer_load_dwordx4 v46, s[28:31], s74 offen offset:2048 lds
ds_read_b64_tr_b16 a[114:115], v26 offset:25088
ds_read_b64_tr_b16 a[116:117], v27 offset:24576
ds_read_b64_tr_b16 a[118:119], v27 offset:25088
ds_read_b64_tr_b16 a[120:121], v28 offset:24576
ds_read_b64_tr_b16 a[122:123], v28 offset:25088
buffer_load_dwordx4 v47, s[28:31], s74 offen offset:3072 lds
ds_read_b64_tr_b16 a[124:125], v29 offset:24576
ds_read_b64_tr_b16 a[126:127], v29 offset:25088
s_mul_i32 s70, s54, 288
s_add_u32 s70, s70, 158720
v_lshrrev_b32 v19, 4, v0
v_lshl_add_u32 v19, v19, 3, s70
ds_read_b64 v[234:235], v19
s_add_u32 m0, s60, 32768
s_add_u32 s74, s80, 655360
buffer_load_dwordx4 v44, s[28:31], s74 offen offset:0 lds
ds_read_b64_tr_b16 a[128:129], v26 offset:28672
ds_read_b64_tr_b16 a[130:131], v26 offset:29184
ds_read_b64_tr_b16 a[132:133], v27 offset:28672
ds_read_b64_tr_b16 a[134:135], v27 offset:29184
ds_read_b64_tr_b16 a[136:137], v28 offset:28672
buffer_load_dwordx4 v45, s[28:31], s74 offen offset:1024 lds
ds_read_b64_tr_b16 a[138:139], v28 offset:29184
ds_read_b64_tr_b16 a[140:141], v29 offset:28672
ds_read_b64_tr_b16 a[142:143], v29 offset:29184
ds_read_b64_tr_b16 a[96:97], v26 offset:20480
ds_read_b64_tr_b16 a[98:99], v26 offset:20992
buffer_load_dwordx4 v46, s[28:31], s74 offen offset:2048 lds
ds_read_b64_tr_b16 a[100:101], v27 offset:20480
ds_read_b64_tr_b16 a[102:103], v27 offset:20992
ds_read_b64_tr_b16 a[104:105], v28 offset:20480
ds_read_b64_tr_b16 a[106:107], v28 offset:20992
ds_read_b64_tr_b16 a[108:109], v29 offset:20480
buffer_load_dwordx4 v47, s[28:31], s74 offen offset:3072 lds
ds_read_b64_tr_b16 a[110:111], v29 offset:20992
s_waitcnt lgkmcnt(15)
v_lshlrev_b32 v96, 16, v234
v_and_b32 v97, 0xffff0000, v234
v_lshlrev_b32 v98, 16, v235
v_and_b32 v99, 0xffff0000, v235
v_lshlrev_b32 v100, 16, v234
v_and_b32 v101, 0xffff0000, v234
v_lshlrev_b32 v102, 16, v235
s_add_u32 m0, s60, 24576
s_add_u32 s74, s80, 786432
buffer_load_dwordx4 v44, s[28:31], s74 offen offset:0 lds
v_and_b32 v103, 0xffff0000, v235
v_lshlrev_b32 v104, 16, v234
v_and_b32 v105, 0xffff0000, v234
v_lshlrev_b32 v106, 16, v235
v_and_b32 v107, 0xffff0000, v235
v_lshlrev_b32 v108, 16, v234
v_and_b32 v109, 0xffff0000, v234
v_lshlrev_b32 v110, 16, v235
buffer_load_dwordx4 v45, s[28:31], s74 offen offset:1024 lds
v_and_b32 v111, 0xffff0000, v235
buffer_load_dwordx4 v46, s[28:31], s74 offen offset:2048 lds
buffer_load_dwordx4 v47, s[28:31], s74 offen offset:3072 lds
s_waitcnt lgkmcnt(8)
s_add_u32 m0, s60, 28672
s_add_u32 s74, s80, 917504
buffer_load_dwordx4 v44, s[28:31], s74 offen offset:0 lds
buffer_load_dwordx4 v45, s[28:31], s74 offen offset:1024 lds
buffer_load_dwordx4 v46, s[28:31], s74 offen offset:2048 lds
buffer_load_dwordx4 v47, s[28:31], s74 offen offset:3072 lds
s_waitcnt lgkmcnt(0)
s_add_u32 m0, s60, 20480
s_add_u32 s74, s80, 1048576
buffer_load_dwordx4 v44, s[28:31], s74 offen offset:0 lds
buffer_load_dwordx4 v45, s[28:31], s74 offen offset:1024 lds
buffer_load_dwordx4 v46, s[28:31], s74 offen offset:2048 lds
buffer_load_dwordx4 v47, s[28:31], s74 offen offset:3072 lds
s_cmp_eq_u32 s10, 0
s_cbranch_scc1 .LIDLE_%=
s_add_u32 s75, s92, 12288
buffer_load_dwordx4 v[160:163], v1, s[8:11], s75 offen offset:0 sc1
s_add_u32 s75, s92, 12288
buffer_load_dwordx4 v[164:167], v1, s[8:11], s75 offen offset:1024 sc1
s_add_u32 s75, s92, 12288
buffer_load_dwordx4 v[168:171], v1, s[8:11], s75 offen offset:2048 sc1
s_add_u32 s75, s92, 12288
buffer_load_dwordx4 v[172:175], v1, s[8:11], s75 offen offset:3072 sc1
s_add_u32 s75, s92, 16384
buffer_load_dwordx4 v[176:179], v1, s[8:11], s75 offen offset:0 sc1
s_add_u32 s75, s92, 16384
buffer_load_dwordx4 v[180:183], v1, s[8:11], s75 offen offset:1024 sc1
s_add_u32 s75, s92, 16384
buffer_load_dwordx4 v[184:187], v1, s[8:11], s75 offen offset:2048 sc1
s_add_u32 s75, s92, 16384
buffer_load_dwordx4 v[188:191], v1, s[8:11], s75 offen offset:3072 sc1
s_add_u32 s75, s92, 20480
buffer_load_dwordx4 v[192:195], v1, s[8:11], s75 offen offset:0 sc1
s_add_u32 s75, s92, 20480
buffer_load_dwordx4 v[196:199], v1, s[8:11], s75 offen offset:1024 sc1
s_add_u32 s75, s92, 20480
buffer_load_dwordx4 v[200:203], v1, s[8:11], s75 offen offset:2048 sc1
s_add_u32 s75, s92, 20480
)ASM"
R"ASM(
buffer_load_dwordx4 v[204:207], v1, s[8:11], s75 offen offset:3072 sc1
s_add_u32 s75, s92, 24576
buffer_load_dwordx4 v[208:211], v1, s[8:11], s75 offen offset:0 sc1
s_add_u32 s75, s92, 24576
buffer_load_dwordx4 v[212:215], v1, s[8:11], s75 offen offset:1024 sc1
s_waitcnt vmcnt(12)
v_max3_u32 v18, v161, v163, v165
v_max_u32 v18, v18, v167
v_cmp_eq_u32 vcc, -1, v18
s_cbranch_vccnz .LS_33_%=
.LB_34_%=:
v_mfma_f32_16x16x32_bf16 v[96:99], a[0:3], v[160:163], v[96:99]
s_add_u32 s75, s92, 24576
v_mfma_f32_16x16x32_bf16 v[112:115], a[4:7], v[160:163], 0
v_mfma_f32_16x16x32_bf16 v[128:131], a[8:11], v[160:163], 0
s_waitcnt vmcnt(10)
v_max3_u32 v18, v169, v171, v173
v_max_u32 v18, v18, v175
v_mfma_f32_16x16x32_bf16 v[144:147], a[12:15], v[160:163], 0
v_cmp_eq_u32 vcc, -1, v18
v_mfma_f32_16x16x32_bf16 v[100:103], a[0:3], v[164:167], v[100:103]
buffer_load_dwordx4 v[160:163], v1, s[8:11], s75 offen offset:2048 sc1
v_mfma_f32_16x16x32_bf16 v[116:119], a[4:7], v[164:167], 0
s_cbranch_vccnz .LS_36_%=
.LB_37_%=:
v_mfma_f32_16x16x32_bf16 v[132:135], a[8:11], v[164:167], 0
s_add_u32 s75, s92, 24576
v_mfma_f32_16x16x32_bf16 v[148:151], a[12:15], v[164:167], 0
buffer_load_dwordx4 v[164:167], v1, s[8:11], s75 offen offset:3072 sc1
v_mfma_f32_16x16x32_bf16 v[104:107], a[0:3], v[168:171], v[104:107]
s_add_u32 s75, s92, 28672
v_mfma_f32_16x16x32_bf16 v[120:123], a[4:7], v[168:171], 0
v_mfma_f32_16x16x32_bf16 v[136:139], a[8:11], v[168:171], 0
s_waitcnt vmcnt(10)
v_max3_u32 v18, v177, v179, v181
v_max_u32 v18, v18, v183
v_mfma_f32_16x16x32_bf16 v[152:155], a[12:15], v[168:171], 0
v_cmp_eq_u32 vcc, -1, v18
v_mfma_f32_16x16x32_bf16 v[108:111], a[0:3], v[172:175], v[108:111]
buffer_load_dwordx4 v[168:171], v1, s[8:11], s75 offen offset:0 sc1
v_mfma_f32_16x16x32_bf16 v[124:127], a[4:7], v[172:175], 0
s_cbranch_vccnz .LS_39_%=
.LB_40_%=:
v_mfma_f32_16x16x32_bf16 v[140:143], a[8:11], v[172:175], 0
s_add_u32 s75, s92, 28672
v_mfma_f32_16x16x32_bf16 v[156:159], a[12:15], v[172:175], 0
buffer_load_dwordx4 v[172:175], v1, s[8:11], s75 offen offset:1024 sc1
v_mfma_f32_16x16x32_bf16 v[96:99], a[16:19], v[176:179], v[96:99]
s_add_u32 s75, s92, 28672
ds_read_b64_tr_b16 a[0:1], v26 offset:4096
ds_read_b64_tr_b16 a[2:3], v26 offset:4608
v_mfma_f32_16x16x32_bf16 v[112:115], a[20:23], v[176:179], v[112:115]
ds_read_b64_tr_b16 a[4:5], v27 offset:4096
ds_read_b64_tr_b16 a[6:7], v27 offset:4608
v_mfma_f32_16x16x32_bf16 v[128:131], a[24:27], v[176:179], v[128:131]
s_waitcnt vmcnt(10)
v_max3_u32 v18, v185, v187, v189
v_max_u32 v18, v18, v191
v_mfma_f32_16x16x32_bf16 v[144:147], a[28:31], v[176:179], v[144:147]
ds_read_b64_tr_b16 a[8:9], v28 offset:4096
v_cmp_eq_u32 vcc, -1, v18
v_mfma_f32_16x16x32_bf16 v[100:103], a[16:19], v[180:183], v[100:103]
buffer_load_dwordx4 v[176:179], v1, s[8:11], s75 offen offset:2048 sc1
v_mfma_f32_16x16x32_bf16 v[116:119], a[20:23], v[180:183], v[116:119]
s_cbranch_vccnz .LS_42_%=
.LB_43_%=:
v_mfma_f32_16x16x32_bf16 v[132:135], a[24:27], v[180:183], v[132:135]
s_add_u32 s75, s92, 28672
ds_read_b64_tr_b16 a[10:11], v28 offset:4608
ds_read_b64_tr_b16 a[12:13], v29 offset:4096
v_mfma_f32_16x16x32_bf16 v[148:151], a[28:31], v[180:183], v[148:151]
buffer_load_dwordx4 v[180:183], v1, s[8:11], s75 offen offset:3072 sc1
v_mfma_f32_16x16x32_bf16 v[104:107], a[16:19], v[184:187], v[104:107]
s_add_u32 s75, s92, 32768
ds_read_b64_tr_b16 a[14:15], v29 offset:4608
v_mfma_f32_16x16x32_bf16 v[120:123], a[20:23], v[184:187], v[120:123]
v_mfma_f32_16x16x32_bf16 v[136:139], a[24:27], v[184:187], v[136:139]
s_waitcnt vmcnt(10)
v_max3_u32 v18, v193, v195, v197
v_max_u32 v18, v18, v199
v_mfma_f32_16x16x32_bf16 v[152:155], a[28:31], v[184:187], v[152:155]
v_cmp_eq_u32 vcc, -1, v18
v_mfma_f32_16x16x32_bf16 v[108:111], a[16:19], v[188:191], v[108:111]
buffer_load_dwordx4 v[184:187], v1, s[8:11], s75 offen offset:0 sc1
v_mfma_f32_16x16x32_bf16 v[124:127], a[20:23], v[188:191], v[124:127]
s_cbranch_vccnz .LS_45_%=
.LB_46_%=:
v_mfma_f32_16x16x32_bf16 v[140:143], a[24:27], v[188:191], v[140:143]
s_add_u32 s75, s92, 32768
v_mfma_f32_16x16x32_bf16 v[156:159], a[28:31], v[188:191], v[156:159]
buffer_load_dwordx4 v[188:191], v1, s[8:11], s75 offen offset:1024 sc1
v_mfma_f32_16x16x32_bf16 v[96:99], a[32:35], v[192:195], v[96:99]
s_add_u32 s75, s92, 32768
ds_read_b64_tr_b16 a[16:17], v26 offset:0
ds_read_b64_tr_b16 a[18:19], v26 offset:512
v_mfma_f32_16x16x32_bf16 v[112:115], a[36:39], v[192:195], v[112:115]
ds_read_b64_tr_b16 a[20:21], v27 offset:0
ds_read_b64_tr_b16 a[22:23], v27 offset:512
v_mfma_f32_16x16x32_bf16 v[128:131], a[40:43], v[192:195], v[128:131]
s_waitcnt vmcnt(10)
v_max3_u32 v18, v201, v203, v205
v_max_u32 v18, v18, v207
v_mfma_f32_16x16x32_bf16 v[144:147], a[44:47], v[192:195], v[144:147]
ds_read_b64_tr_b16 a[24:25], v28 offset:0
v_cmp_eq_u32 vcc, -1, v18
v_mfma_f32_16x16x32_bf16 v[100:103], a[32:35], v[196:199], v[100:103]
buffer_load_dwordx4 v[192:195], v1, s[8:11], s75 offen offset:2048 sc1
v_mfma_f32_16x16x32_bf16 v[116:119], a[36:39], v[196:199], v[116:119]
s_cbranch_vccnz .LS_48_%=
.LB_49_%=:
v_mfma_f32_16x16x32_bf16 v[132:135], a[40:43], v[196:199], v[132:135]
s_add_u32 s75, s92, 32768
ds_read_b64_tr_b16 a[26:27], v28 offset:512
ds_read_b64_tr_b16 a[28:29], v29 offset:0
v_mfma_f32_16x16x32_bf16 v[148:151], a[44:47], v[196:199], v[148:151]
buffer_load_dwordx4 v[196:199], v1, s[8:11], s75 offen offset:3072 sc1
v_mfma_f32_16x16x32_bf16 v[104:107], a[32:35], v[200:203], v[104:107]
s_add_u32 s75, s92, 36864
ds_read_b64_tr_b16 a[30:31], v29 offset:512
v_mfma_f32_16x16x32_bf16 v[120:123], a[36:39], v[200:203], v[120:123]
v_mfma_f32_16x16x32_bf16 v[136:139], a[40:43], v[200:203], v[136:139]
s_waitcnt vmcnt(10)
v_max3_u32 v18, v209, v211, v213
v_max_u32 v18, v18, v215
v_mfma_f32_16x16x32_bf16 v[152:155], a[44:47], v[200:203], v[152:155]
v_cmp_eq_u32 vcc, -1, v18
v_mfma_f32_16x16x32_bf16 v[108:111], a[32:35], v[204:207], v[108:111]
buffer_load_dwordx4 v[200:203], v1, s[8:11], s75 offen offset:0 sc1
v_mfma_f32_16x16x32_bf16 v[124:127], a[36:39], v[204:207], v[124:127]
s_cbranch_vccnz .LS_51_%=
.LB_52_%=:
v_mfma_f32_16x16x32_bf16 v[140:143], a[40:43], v[204:207], v[140:143]
s_add_u32 s75, s92, 36864
v_mfma_f32_16x16x32_bf16 v[156:159], a[44:47], v[204:207], v[156:159]
buffer_load_dwordx4 v[204:207], v1, s[8:11], s75 offen offset:1024 sc1
v_mfma_f32_16x16x32_bf16 v[96:99], a[48:51], v[208:211], v[96:99]
s_add_u32 s75, s92, 36864
ds_read_b64_tr_b16 a[32:33], v26 offset:8192
ds_read_b64_tr_b16 a[34:35], v26 offset:8704
v_mfma_f32_16x16x32_bf16 v[112:115], a[52:55], v[208:211], v[112:115]
ds_read_b64_tr_b16 a[36:37], v27 offset:8192
ds_read_b64_tr_b16 a[38:39], v27 offset:8704
v_mfma_f32_16x16x32_bf16 v[128:131], a[56:59], v[208:211], v[128:131]
s_waitcnt vmcnt(10)
v_max3_u32 v18, v161, v163, v165
v_max_u32 v18, v18, v167
v_mfma_f32_16x16x32_bf16 v[144:147], a[60:63], v[208:211], v[144:147]
ds_read_b64_tr_b16 a[40:41], v28 offset:8192
v_cmp_eq_u32 vcc, -1, v18
v_mfma_f32_16x16x32_bf16 v[100:103], a[48:51], v[212:215], v[100:103]
buffer_load_dwordx4 v[208:211], v1, s[8:11], s75 offen offset:2048 sc1
v_mfma_f32_16x16x32_bf16 v[116:119], a[52:55], v[212:215], v[116:119]
s_cbranch_vccnz .LS_54_%=
.LB_55_%=:
v_mfma_f32_16x16x32_bf16 v[132:135], a[56:59], v[212:215], v[132:135]
s_add_u32 s75, s92, 36864
ds_read_b64_tr_b16 a[42:43], v28 offset:8704
ds_read_b64_tr_b16 a[44:45], v29 offset:8192
v_mfma_f32_16x16x32_bf16 v[148:151], a[60:63], v[212:215], v[148:151]
buffer_load_dwordx4 v[212:215], v1, s[8:11], s75 offen offset:3072 sc1
v_mfma_f32_16x16x32_bf16 v[104:107], a[48:51], v[160:163], v[104:107]
s_add_u32 s75, s92, 40960
ds_read_b64_tr_b16 a[46:47], v29 offset:8704
v_mfma_f32_16x16x32_bf16 v[120:123], a[52:55], v[160:163], v[120:123]
v_mfma_f32_16x16x32_bf16 v[136:139], a[56:59], v[160:163], v[136:139]
s_waitcnt vmcnt(10)
v_max3_u32 v18, v169, v171, v173
v_max_u32 v18, v18, v175
v_mfma_f32_16x16x32_bf16 v[152:155], a[60:63], v[160:163], v[152:155]
v_cmp_eq_u32 vcc, -1, v18
v_mfma_f32_16x16x32_bf16 v[108:111], a[48:51], v[164:167], v[108:111]
buffer_load_dwordx4 v[160:163], v1, s[8:11], s75 offen offset:0 sc1
v_mfma_f32_16x16x32_bf16 v[124:127], a[52:55], v[164:167], v[124:127]
s_cbranch_vccnz .LS_57_%=
.LB_58_%=:
v_mfma_f32_16x16x32_bf16 v[140:143], a[56:59], v[164:167], v[140:143]
s_add_u32 s75, s92, 40960
v_mfma_f32_16x16x32_bf16 v[156:159], a[60:63], v[164:167], v[156:159]
buffer_load_dwordx4 v[164:167], v1, s[8:11], s75 offen offset:1024 sc1
v_mfma_f32_16x16x32_bf16 v[96:99], a[64:67], v[168:171], v[96:99]
s_add_u32 s75, s92, 40960
ds_read_b64_tr_b16 a[48:49], v26 offset:12288
ds_read_b64_tr_b16 a[50:51], v26 offset:12800
v_mfma_f32_16x16x32_bf16 v[112:115], a[68:71], v[168:171], v[112:115]
ds_read_b64_tr_b16 a[52:53], v27 offset:12288
ds_read_b64_tr_b16 a[54:55], v27 offset:12800
v_mfma_f32_16x16x32_bf16 v[128:131], a[72:75], v[168:171], v[128:131]
s_waitcnt vmcnt(10)
v_max3_u32 v18, v177, v179, v181
v_max_u32 v18, v18, v183
v_mfma_f32_16x16x32_bf16 v[144:147], a[76:79], v[168:171], v[144:147]
ds_read_b64_tr_b16 a[56:57], v28 offset:12288
v_cmp_eq_u32 vcc, -1, v18
v_mfma_f32_16x16x32_bf16 v[100:103], a[64:67], v[172:175], v[100:103]
buffer_load_dwordx4 v[168:171], v1, s[8:11], s75 offen offset:2048 sc1
v_mfma_f32_16x16x32_bf16 v[116:119], a[68:71], v[172:175], v[116:119]
s_cbranch_vccnz .LS_60_%=
.LB_61_%=:
v_mfma_f32_16x16x32_bf16 v[132:135], a[72:75], v[172:175], v[132:135]
s_add_u32 s75, s92, 40960
ds_read_b64_tr_b16 a[58:59], v28 offset:12800
ds_read_b64_tr_b16 a[60:61], v29 offset:12288
v_mfma_f32_16x16x32_bf16 v[148:151], a[76:79], v[172:175], v[148:151]
buffer_load_dwordx4 v[172:175], v1, s[8:11], s75 offen offset:3072 sc1
v_mfma_f32_16x16x32_bf16 v[104:107], a[64:67], v[176:179], v[104:107]
s_add_u32 s75, s92, 45056
ds_read_b64_tr_b16 a[62:63], v29 offset:12800
v_mfma_f32_16x16x32_bf16 v[120:123], a[68:71], v[176:179], v[120:123]
v_mfma_f32_16x16x32_bf16 v[136:139], a[72:75], v[176:179], v[136:139]
s_waitcnt vmcnt(10)
v_max3_u32 v18, v185, v187, v189
v_max_u32 v18, v18, v191
v_mfma_f32_16x16x32_bf16 v[152:155], a[76:79], v[176:179], v[152:155]
v_cmp_eq_u32 vcc, -1, v18
v_mfma_f32_16x16x32_bf16 v[108:111], a[64:67], v[180:183], v[108:111]
buffer_load_dwordx4 v[176:179], v1, s[8:11], s75 offen offset:0 sc1
v_mfma_f32_16x16x32_bf16 v[124:127], a[68:71], v[180:183], v[124:127]
s_cbranch_vccnz .LS_63_%=
.LB_64_%=:
v_mfma_f32_16x16x32_bf16 v[140:143], a[72:75], v[180:183], v[140:143]
s_add_u32 s75, s92, 49152
v_mfma_f32_16x16x32_bf16 v[156:159], a[76:79], v[180:183], v[156:159]
buffer_load_dwordx4 v[180:183], v1, s[8:11], s75 offen offset:0 sc1
v_mfma_f32_16x16x32_bf16 v[96:99], a[80:83], v[184:187], v[96:99]
s_add_u32 s75, s92, 53248
ds_read_b64_tr_b16 a[64:65], v26 offset:16384
ds_read_b64_tr_b16 a[66:67], v26 offset:16896
v_mfma_f32_16x16x32_bf16 v[112:115], a[84:87], v[184:187], v[112:115]
ds_read_b64_tr_b16 a[68:69], v27 offset:16384
ds_read_b64_tr_b16 a[70:71], v27 offset:16896
v_mfma_f32_16x16x32_bf16 v[128:131], a[88:91], v[184:187], v[128:131]
s_waitcnt vmcnt(10)
v_max3_u32 v18, v193, v195, v197
v_max_u32 v18, v18, v199
v_mfma_f32_16x16x32_bf16 v[144:147], a[92:95], v[184:187], v[144:147]
ds_read_b64_tr_b16 a[72:73], v28 offset:16384
v_cmp_eq_u32 vcc, -1, v18
v_mfma_f32_16x16x32_bf16 v[100:103], a[80:83], v[188:191], v[100:103]
buffer_load_dwordx4 v[184:187], v1, s[8:11], s75 offen offset:0 sc1
v_mfma_f32_16x16x32_bf16 v[116:119], a[84:87], v[188:191], v[116:119]
s_cbranch_vccnz .LS_66_%=
.LB_67_%=:
v_mfma_f32_16x16x32_bf16 v[132:135], a[88:91], v[188:191], v[132:135]
s_add_u32 s75, s92, 57344
ds_read_b64_tr_b16 a[74:75], v28 offset:16896
ds_read_b64_tr_b16 a[76:77], v29 offset:16384
v_mfma_f32_16x16x32_bf16 v[148:151], a[92:95], v[188:191], v[148:151]
buffer_load_dwordx4 v[188:191], v1, s[8:11], s75 offen offset:0 sc1
v_mfma_f32_16x16x32_bf16 v[104:107], a[80:83], v[192:195], v[104:107]
s_add_u32 s75, s92, 61440
ds_read_b64_tr_b16 a[78:79], v29 offset:16896
v_mfma_f32_16x16x32_bf16 v[120:123], a[84:87], v[192:195], v[120:123]
)ASM"
R"ASM(
v_mfma_f32_16x16x32_bf16 v[136:139], a[88:91], v[192:195], v[136:139]
s_waitcnt vmcnt(10)
v_max3_u32 v18, v201, v203, v205
v_max_u32 v18, v18, v207
v_mfma_f32_16x16x32_bf16 v[152:155], a[92:95], v[192:195], v[152:155]
v_cmp_eq_u32 vcc, -1, v18
v_mfma_f32_16x16x32_bf16 v[108:111], a[80:83], v[196:199], v[108:111]
buffer_load_dwordx4 v[192:195], v1, s[8:11], s75 offen offset:0 sc1
v_mfma_f32_16x16x32_bf16 v[124:127], a[84:87], v[196:199], v[124:127]
s_cbranch_vccnz .LS_69_%=
.LB_70_%=:
v_mfma_f32_16x16x32_bf16 v[140:143], a[88:91], v[196:199], v[140:143]
s_add_u32 s75, s92, 0
v_mfma_f32_16x16x32_bf16 v[156:159], a[92:95], v[196:199], v[156:159]
buffer_load_dwordx4 v[196:199], v1, s[8:11], s75 offen offset:0 sc1
v_mfma_f32_16x16x32_bf16 v[96:99], a[96:99], v[200:203], v[96:99]
s_add_u32 s75, s92, 4096
ds_read_b64_tr_b16 a[80:81], v26 offset:32768
ds_read_b64_tr_b16 a[82:83], v26 offset:33280
v_mfma_f32_16x16x32_bf16 v[112:115], a[100:103], v[200:203], v[112:115]
ds_read_b64_tr_b16 a[84:85], v27 offset:32768
ds_read_b64_tr_b16 a[86:87], v27 offset:33280
v_mfma_f32_16x16x32_bf16 v[128:131], a[104:107], v[200:203], v[128:131]
s_waitcnt vmcnt(10)
v_max3_u32 v18, v209, v211, v213
v_max_u32 v18, v18, v215
v_mfma_f32_16x16x32_bf16 v[144:147], a[108:111], v[200:203], v[144:147]
ds_read_b64_tr_b16 a[88:89], v28 offset:32768
v_cmp_eq_u32 vcc, -1, v18
v_mfma_f32_16x16x32_bf16 v[100:103], a[96:99], v[204:207], v[100:103]
buffer_load_dwordx4 v[200:203], v1, s[8:11], s75 offen offset:0 sc1
v_mfma_f32_16x16x32_bf16 v[116:119], a[100:103], v[204:207], v[116:119]
s_cbranch_vccnz .LS_72_%=
.LB_73_%=:
v_mfma_f32_16x16x32_bf16 v[132:135], a[104:107], v[204:207], v[132:135]
s_add_u32 s75, s92, 8192
ds_read_b64_tr_b16 a[90:91], v28 offset:33280
ds_read_b64_tr_b16 a[92:93], v29 offset:32768
v_mfma_f32_16x16x32_bf16 v[148:151], a[108:111], v[204:207], v[148:151]
buffer_load_dwordx4 v[204:207], v1, s[8:11], s75 offen offset:0 sc1
v_mfma_f32_16x16x32_bf16 v[104:107], a[96:99], v[208:211], v[104:107]
s_add_u32 s75, s92, 45056
ds_read_b64_tr_b16 a[94:95], v29 offset:33280
v_mfma_f32_16x16x32_bf16 v[120:123], a[100:103], v[208:211], v[120:123]
v_mfma_f32_16x16x32_bf16 v[136:139], a[104:107], v[208:211], v[136:139]
s_waitcnt vmcnt(10)
v_max3_u32 v18, v161, v163, v165
v_max_u32 v18, v18, v167
v_mfma_f32_16x16x32_bf16 v[152:155], a[108:111], v[208:211], v[152:155]
v_cmp_eq_u32 vcc, -1, v18
v_mfma_f32_16x16x32_bf16 v[108:111], a[96:99], v[212:215], v[108:111]
buffer_load_dwordx4 v[208:211], v1, s[8:11], s75 offen offset:1024 sc1
v_mfma_f32_16x16x32_bf16 v[124:127], a[100:103], v[212:215], v[124:127]
s_cbranch_vccnz .LS_75_%=
.LB_76_%=:
v_mfma_f32_16x16x32_bf16 v[140:143], a[104:107], v[212:215], v[140:143]
s_add_u32 s75, s92, 49152
v_mfma_f32_16x16x32_bf16 v[156:159], a[108:111], v[212:215], v[156:159]
buffer_load_dwordx4 v[212:215], v1, s[8:11], s75 offen offset:1024 sc1
v_mfma_f32_16x16x32_bf16 v[96:99], a[112:115], v[160:163], v[96:99]
s_add_u32 s75, s92, 53248
ds_read_b64_tr_b16 a[96:97], v26 offset:24576
ds_read_b64_tr_b16 a[98:99], v26 offset:25088
v_mfma_f32_16x16x32_bf16 v[112:115], a[116:119], v[160:163], v[112:115]
ds_read_b64_tr_b16 a[100:101], v27 offset:24576
ds_read_b64_tr_b16 a[102:103], v27 offset:25088
v_mfma_f32_16x16x32_bf16 v[128:131], a[120:123], v[160:163], v[128:131]
s_waitcnt vmcnt(10)
v_max3_u32 v18, v169, v171, v173
v_max_u32 v18, v18, v175
v_mfma_f32_16x16x32_bf16 v[144:147], a[124:127], v[160:163], v[144:147]
ds_read_b64_tr_b16 a[104:105], v28 offset:24576
v_cmp_eq_u32 vcc, -1, v18
v_mfma_f32_16x16x32_bf16 v[100:103], a[112:115], v[164:167], v[100:103]
buffer_load_dwordx4 v[160:163], v1, s[8:11], s75 offen offset:1024 sc1
v_mfma_f32_16x16x32_bf16 v[116:119], a[116:119], v[164:167], v[116:119]
s_cbranch_vccnz .LS_78_%=
.LB_79_%=:
v_mfma_f32_16x16x32_bf16 v[132:135], a[120:123], v[164:167], v[132:135]
s_add_u32 s75, s92, 57344
ds_read_b64_tr_b16 a[106:107], v28 offset:25088
ds_read_b64_tr_b16 a[108:109], v29 offset:24576
v_mfma_f32_16x16x32_bf16 v[148:151], a[124:127], v[164:167], v[148:151]
buffer_load_dwordx4 v[164:167], v1, s[8:11], s75 offen offset:1024 sc1
v_mfma_f32_16x16x32_bf16 v[104:107], a[112:115], v[168:171], v[104:107]
s_add_u32 s75, s92, 61440
ds_read_b64_tr_b16 a[110:111], v29 offset:25088
v_mfma_f32_16x16x32_bf16 v[120:123], a[116:119], v[168:171], v[120:123]
v_mfma_f32_16x16x32_bf16 v[136:139], a[120:123], v[168:171], v[136:139]
s_waitcnt vmcnt(10)
v_max3_u32 v18, v177, v179, v181
v_max_u32 v18, v18, v183
v_mfma_f32_16x16x32_bf16 v[152:155], a[124:127], v[168:171], v[152:155]
v_cmp_eq_u32 vcc, -1, v18
v_mfma_f32_16x16x32_bf16 v[108:111], a[112:115], v[172:175], v[108:111]
buffer_load_dwordx4 v[168:171], v1, s[8:11], s75 offen offset:1024 sc1
v_mfma_f32_16x16x32_bf16 v[124:127], a[116:119], v[172:175], v[124:127]
s_cbranch_vccnz .LS_81_%=
.LB_82_%=:
v_mfma_f32_16x16x32_bf16 v[140:143], a[120:123], v[172:175], v[140:143]
s_add_u32 s75, s92, 0
v_mfma_f32_16x16x32_bf16 v[156:159], a[124:127], v[172:175], v[156:159]
buffer_load_dwordx4 v[172:175], v1, s[8:11], s75 offen offset:1024 sc1
v_mfma_f32_16x16x32_bf16 v[96:99], a[128:131], v[176:179], v[96:99]
s_add_u32 s75, s92, 4096
ds_read_b64_tr_b16 a[112:113], v26 offset:28672
ds_read_b64_tr_b16 a[114:115], v26 offset:29184
v_mfma_f32_16x16x32_bf16 v[112:115], a[132:135], v[176:179], v[112:115]
ds_read_b64_tr_b16 a[116:117], v27 offset:28672
ds_read_b64_tr_b16 a[118:119], v27 offset:29184
v_mfma_f32_16x16x32_bf16 v[128:131], a[136:139], v[176:179], v[128:131]
s_waitcnt vmcnt(10)
v_max3_u32 v18, v185, v187, v189
v_max_u32 v18, v18, v191
v_mfma_f32_16x16x32_bf16 v[144:147], a[140:143], v[176:179], v[144:147]
s_lshr_b32 s98, s16, 7
ds_read_b64_tr_b16 a[120:121], v28 offset:28672
v_cmp_eq_u32 vcc, -1, v18
v_mfma_f32_16x16x32_bf16 v[96:99], a[144:147], v[180:183], v[96:99]
s_sub_u32 s98, 3, s98
buffer_load_dwordx4 v[176:179], v1, s[8:11], s75 offen offset:1024 sc1
v_mfma_f32_16x16x32_bf16 v[112:115], a[148:151], v[180:183], v[112:115]
s_cbranch_vccnz .LS_84_%=
.LB_85_%=:
v_mfma_f32_16x16x32_bf16 v[128:131], a[152:155], v[180:183], v[128:131]
s_add_u32 s75, s92, 8192
ds_read_b64_tr_b16 a[122:123], v28 offset:29184
ds_read_b64_tr_b16 a[124:125], v29 offset:28672
v_mfma_f32_16x16x32_bf16 v[144:147], a[156:159], v[180:183], v[144:147]
s_and_b32 s98, s98, 7
buffer_load_dwordx4 v[180:183], v1, s[8:11], s75 offen offset:1024 sc1
v_mfma_f32_16x16x32_bf16 v[96:99], a[160:163], v[184:187], v[96:99]
s_add_u32 s75, s92, 45056
s_xor_b32 s99, s98, s56
ds_read_b64_tr_b16 a[126:127], v29 offset:29184
v_mfma_f32_16x16x32_bf16 v[112:115], a[164:167], v[184:187], v[112:115]
s_and_b32 s99, s99, 4
s_lshl_b32 s99, s99, 28
s_lshl_b32 s98, s98, 7
v_mfma_f32_16x16x32_bf16 v[128:131], a[168:171], v[184:187], v[128:131]
s_waitcnt vmcnt(10)
v_max3_u32 v18, v193, v195, v197
v_max_u32 v18, v18, v199
v_mfma_f32_16x16x32_bf16 v[144:147], a[172:175], v[184:187], v[144:147]
s_or_b32 s99, s99, s98
s_mul_i32 s98, s97, 3072
v_cmp_eq_u32 vcc, -1, v18
v_mfma_f32_16x16x32_bf16 v[96:99], a[176:179], v[188:191], v[96:99]
buffer_load_dwordx4 v[184:187], v1, s[8:11], s75 offen offset:2048 sc1
v_mfma_f32_16x16x32_bf16 v[112:115], a[180:183], v[188:191], v[112:115]
s_cbranch_vccnz .LS_87_%=
.LB_88_%=:
v_mfma_f32_16x16x32_bf16 v[128:131], a[184:187], v[188:191], v[128:131]
s_add_u32 s75, s92, 49152
s_waitcnt lgkmcnt(0)
s_barrier
v_mfma_f32_16x16x32_bf16 v[144:147], a[188:191], v[188:191], v[144:147]
buffer_load_dwordx4 v[188:191], v1, s[8:11], s75 offen offset:2048 sc1
v_mfma_f32_16x16x32_bf16 v[96:99], a[192:195], v[192:195], v[96:99]
s_add_u32 s75, s92, 53248
v_bfe_u32 v20, v0, 2, 2
v_mul_u32_u24 v20, 384, v20
v_mfma_f32_16x16x32_bf16 v[112:115], a[196:199], v[192:195], v[112:115]
v_and_b32 v21, 3, v0
v_min_u32 v21, 2, v21
v_mfma_f32_16x16x32_bf16 v[128:131], a[200:203], v[192:195], v[128:131]
s_waitcnt vmcnt(10)
v_max3_u32 v18, v201, v203, v205
v_max_u32 v18, v18, v207
v_mfma_f32_16x16x32_bf16 v[144:147], a[204:207], v[192:195], v[144:147]
v_cmp_eq_u32 vcc, -1, v18
v_add_lshl_u32 v20, v20, v21, 10
v_mfma_f32_16x16x32_bf16 v[96:99], a[208:211], v[196:199], v[96:99]
buffer_load_dwordx4 v[192:195], v1, s[8:11], s75 offen offset:2048 sc1
v_mfma_f32_16x16x32_bf16 v[112:115], a[212:215], v[196:199], v[112:115]
s_cbranch_vccnz .LS_90_%=
.LB_91_%=:
v_mfma_f32_16x16x32_bf16 v[128:131], a[216:219], v[196:199], v[128:131]
s_add_u32 s75, s92, 57344
v_or_b32 v20, s99, v20
v_mfma_f32_16x16x32_bf16 v[144:147], a[220:223], v[196:199], v[144:147]
buffer_load_dwordx4 v[196:199], v1, s[8:11], s75 offen offset:2048 sc1
v_mfma_f32_16x16x32_bf16 v[96:99], a[224:227], v[200:203], v[96:99]
s_add_u32 s75, s92, 61440
s_bfm_b64 exec, 16, 0
buffer_load_dword v77, v20, s[16:19], s98 offen
s_mov_b64 exec, -1
v_mfma_f32_16x16x32_bf16 v[112:115], a[228:231], v[200:203], v[112:115]
v_mfma_f32_16x16x32_bf16 v[128:131], a[232:235], v[200:203], v[128:131]
s_waitcnt vmcnt(11)
v_max3_u32 v18, v209, v211, v213
v_max_u32 v18, v18, v215
v_mfma_f32_16x16x32_bf16 v[144:147], a[236:239], v[200:203], v[144:147]
v_cmp_eq_u32 vcc, -1, v18
v_mfma_f32_16x16x32_bf16 v[96:99], a[240:243], v[204:207], v[96:99]
buffer_load_dwordx4 v[200:203], v1, s[8:11], s75 offen offset:2048 sc1
v_mfma_f32_16x16x32_bf16 v[112:115], a[244:247], v[204:207], v[112:115]
s_cbranch_vccnz .LS_93_%=
.LB_94_%=:
v_mfma_f32_16x16x32_bf16 v[128:131], a[248:251], v[204:207], v[128:131]
s_add_u32 s75, s92, 0
v_mfma_f32_16x16x32_bf16 v[144:147], a[252:255], v[204:207], v[144:147]
buffer_load_dwordx4 v[204:207], v1, s[8:11], s75 offen offset:2048 sc1
v_mfma_f32_16x16x32_bf16 v[100:103], a[128:131], v[208:211], v[100:103]
s_add_u32 s75, s92, 4096
v_mfma_f32_16x16x32_bf16 v[116:119], a[132:135], v[208:211], v[116:119]
v_mfma_f32_16x16x32_bf16 v[132:135], a[136:139], v[208:211], v[132:135]
s_waitcnt vmcnt(11)
v_max3_u32 v18, v161, v163, v165
v_max_u32 v18, v18, v167
v_cmp_eq_u32 vcc, -1, v18
v_mfma_f32_16x16x32_bf16 v[148:151], a[140:143], v[208:211], v[148:151]
buffer_load_dwordx4 v[208:211], v1, s[8:11], s75 offen offset:2048 sc1
ds_write_b128 v74, v[112:115] offset:24576
v_mfma_f32_16x16x32_bf16 v[100:103], a[144:147], v[212:215], v[100:103]
s_cbranch_vccnz .LS_96_%=
.LB_97_%=:
s_add_u32 s75, s92, 8192
v_mfma_f32_16x16x32_bf16 v[116:119], a[148:151], v[212:215], v[116:119]
v_mfma_f32_16x16x32_bf16 v[132:135], a[152:155], v[212:215], v[132:135]
ds_write_b128 v75, v[128:131] offset:28672
v_mfma_f32_16x16x32_bf16 v[148:151], a[156:159], v[212:215], v[148:151]
buffer_load_dwordx4 v[212:215], v1, s[8:11], s75 offen offset:2048 sc1
v_mfma_f32_16x16x32_bf16 v[100:103], a[160:163], v[160:163], v[100:103]
s_add_u32 s75, s92, 45056
v_mfma_f32_16x16x32_bf16 v[116:119], a[164:167], v[160:163], v[116:119]
ds_write_b128 v76, v[144:147] offset:32768
v_mfma_f32_16x16x32_bf16 v[132:135], a[168:171], v[160:163], v[132:135]
s_waitcnt vmcnt(11)
v_max3_u32 v18, v169, v171, v173
v_max_u32 v18, v18, v175
v_cmp_eq_u32 vcc, -1, v18
v_mfma_f32_16x16x32_bf16 v[148:151], a[172:175], v[160:163], v[148:151]
buffer_load_dwordx4 v[160:163], v1, s[8:11], s75 offen offset:3072 sc1
v_mfma_f32_16x16x32_bf16 v[100:103], a[176:179], v[164:167], v[100:103]
s_cbranch_vccnz .LS_99_%=
.LB_100_%=:
s_add_u32 s75, s92, 49152
s_waitcnt lgkmcnt(0)
v_mfma_f32_16x16x32_bf16 v[116:119], a[180:183], v[164:167], v[116:119]
s_barrier
ds_read_b128 v[112:115], v73 offset:24576
ds_read_b128 v[128:131], v73 offset:28672
ds_read_b128 v[144:147], v73 offset:32768
v_mfma_f32_16x16x32_bf16 v[132:135], a[184:187], v[164:167], v[132:135]
v_mfma_f32_16x16x32_bf16 v[148:151], a[188:191], v[164:167], v[148:151]
buffer_load_dwordx4 v[164:167], v1, s[8:11], s75 offen offset:3072 sc1
v_mfma_f32_16x16x32_bf16 v[100:103], a[192:195], v[168:171], v[100:103]
s_add_u32 s75, s92, 53248
v_mfma_f32_16x16x32_bf16 v[116:119], a[196:199], v[168:171], v[116:119]
v_mfma_f32_16x16x32_bf16 v[132:135], a[200:203], v[168:171], v[132:135]
s_waitcnt vmcnt(11)
v_max3_u32 v18, v177, v179, v181
v_max_u32 v18, v18, v183
v_cmp_eq_u32 vcc, -1, v18
)ASM"
R"ASM(
s_waitcnt lgkmcnt(2)
v_add_f32 v96, v96, v112
v_mfma_f32_16x16x32_bf16 v[148:151], a[204:207], v[168:171], v[148:151]
buffer_load_dwordx4 v[168:171], v1, s[8:11], s75 offen offset:3072 sc1
v_add_f32 v97, v97, v113
v_add_f32 v98, v98, v114
v_mfma_f32_16x16x32_bf16 v[100:103], a[208:211], v[172:175], v[100:103]
s_cbranch_vccnz .LS_102_%=
.LB_103_%=:
s_add_u32 s75, s92, 57344
v_add_f32 v99, v99, v115
v_mfma_f32_16x16x32_bf16 v[116:119], a[212:215], v[172:175], v[116:119]
s_waitcnt lgkmcnt(1)
v_add_f32 v96, v96, v128
v_add_f32 v97, v97, v129
v_add_f32 v98, v98, v130
v_add_f32 v99, v99, v131
v_mfma_f32_16x16x32_bf16 v[132:135], a[216:219], v[172:175], v[132:135]
s_waitcnt lgkmcnt(0)
v_add_f32 v96, v96, v144
v_add_f32 v97, v97, v145
v_add_f32 v98, v98, v146
v_add_f32 v99, v99, v147
v_mfma_f32_16x16x32_bf16 v[148:151], a[220:223], v[172:175], v[148:151]
buffer_load_dwordx4 v[172:175], v1, s[8:11], s75 offen offset:3072 sc1
v_mul_f32 v112, v96, v96
v_mul_f32 v113, v97, v97
v_mfma_f32_16x16x32_bf16 v[100:103], a[224:227], v[176:179], v[100:103]
s_add_u32 s75, s92, 61440
v_mul_f32 v114, v98, v98
v_mul_f32 v115, v99, v99
v_fma_f32 v112, v112, s40, v34
v_fma_f32 v113, v113, s40, v34
v_mfma_f32_16x16x32_bf16 v[116:119], a[228:231], v[176:179], v[116:119]
v_fma_f32 v114, v114, s40, v34
v_fma_f32 v115, v115, s40, v34
v_mul_f32 v112, v112, v96
v_mul_f32 v113, v113, v97
v_mfma_f32_16x16x32_bf16 v[132:135], a[232:235], v[176:179], v[132:135]
s_waitcnt vmcnt(11)
v_max3_u32 v18, v185, v187, v189
v_max_u32 v18, v18, v191
v_cmp_eq_u32 vcc, -1, v18
v_mul_f32 v114, v114, v98
v_mfma_f32_16x16x32_bf16 v[148:151], a[236:239], v[176:179], v[148:151]
buffer_load_dwordx4 v[176:179], v1, s[8:11], s75 offen offset:3072 sc1
v_mul_f32 v115, v115, v99
v_mfma_f32_16x16x32_bf16 v[100:103], a[240:243], v[180:183], v[100:103]
s_cbranch_vccnz .LS_105_%=
.LB_106_%=:
s_add_u32 s75, s92, 0
v_mfma_f32_16x16x32_bf16 v[116:119], a[244:247], v[180:183], v[116:119]
v_exp_f32 v112, v112
v_exp_f32 v113, v113
v_mfma_f32_16x16x32_bf16 v[132:135], a[248:251], v[180:183], v[132:135]
v_exp_f32 v114, v114
v_exp_f32 v115, v115
v_mfma_f32_16x16x32_bf16 v[148:151], a[252:255], v[180:183], v[148:151]
buffer_load_dwordx4 v[180:183], v1, s[8:11], s75 offen offset:3072 sc1
v_add_f32 v112, 1.0, v112
v_add_f32 v113, 1.0, v113
v_mfma_f32_16x16x32_bf16 v[104:107], a[128:131], v[184:187], v[104:107]
s_add_u32 s75, s92, 4096
v_add_f32 v114, 1.0, v114
v_add_f32 v115, 1.0, v115
v_rcp_f32 v112, v112
v_mfma_f32_16x16x32_bf16 v[120:123], a[132:135], v[184:187], v[120:123]
v_rcp_f32 v113, v113
v_rcp_f32 v114, v114
v_mfma_f32_16x16x32_bf16 v[136:139], a[136:139], v[184:187], v[136:139]
s_waitcnt vmcnt(11)
v_max3_u32 v18, v193, v195, v197
v_max_u32 v18, v18, v199
v_cmp_eq_u32 vcc, -1, v18
v_mfma_f32_16x16x32_bf16 v[152:155], a[140:143], v[184:187], v[152:155]
buffer_load_dwordx4 v[184:187], v1, s[8:11], s75 offen offset:3072 sc1
ds_write_b128 v74, v[116:119] offset:25600
v_mfma_f32_16x16x32_bf16 v[104:107], a[144:147], v[188:191], v[104:107]
s_cbranch_vccnz .LS_108_%=
.LB_109_%=:
s_add_u32 s75, s92, 8192
v_mfma_f32_16x16x32_bf16 v[120:123], a[148:151], v[188:191], v[120:123]
v_rcp_f32 v115, v115
v_mul_f32 v112, v112, v96
v_mul_f32 v113, v113, v97
v_mfma_f32_16x16x32_bf16 v[136:139], a[152:155], v[188:191], v[136:139]
ds_write_b128 v75, v[132:135] offset:29696
v_mul_f32 v114, v114, v98
v_mul_f32 v115, v115, v99
v_mfma_f32_16x16x32_bf16 v[152:155], a[156:159], v[188:191], v[152:155]
buffer_load_dwordx4 v[188:191], v1, s[8:11], s75 offen offset:3072 sc1
v_cvt_pk_bf16_f32 v224, v112, v113
v_cvt_pk_bf16_f32 v225, v114, v115
v_mfma_f32_16x16x32_bf16 v[104:107], a[160:163], v[192:195], v[104:107]
s_add_u32 m0, s60, 16384
s_add_u32 s74, s80, 1179648
buffer_load_dwordx4 v44, s[28:31], s74 offen offset:0 lds
v_min_u32 v224, -2, v224
v_mfma_f32_16x16x32_bf16 v[120:123], a[164:167], v[192:195], v[120:123]
ds_write_b128 v76, v[148:151] offset:33792
v_min_u32 v225, -2, v225
v_mfma_f32_16x16x32_bf16 v[136:139], a[168:171], v[192:195], v[136:139]
s_waitcnt vmcnt(11)
v_max3_u32 v18, v201, v203, v205
v_max_u32 v18, v18, v207
v_cmp_eq_u32 vcc, -1, v18
v_mfma_f32_16x16x32_bf16 v[152:155], a[172:175], v[192:195], v[152:155]
v_mfma_f32_16x16x32_bf16 v[104:107], a[176:179], v[196:199], v[104:107]
s_cbranch_vccnz .LS_111_%=
.LB_112_%=:
s_waitcnt vmcnt(9)
v_max3_u32 v18, v209, v211, v213
v_mfma_f32_16x16x32_bf16 v[120:123], a[180:183], v[196:199], v[120:123]
s_waitcnt lgkmcnt(0)
s_barrier
ds_read_b128 v[116:119], v73 offset:25600
v_max_u32 v18, v18, v215
v_cmp_eq_u32 vcc, -1, v18
v_mfma_f32_16x16x32_bf16 v[136:139], a[184:187], v[196:199], v[136:139]
ds_read_b128 v[132:135], v73 offset:29696
ds_read_b128 v[148:151], v73 offset:33792
buffer_load_dwordx4 v45, s[28:31], s74 offen offset:1024 lds
v_mfma_f32_16x16x32_bf16 v[152:155], a[188:191], v[196:199], v[152:155]
s_cbranch_vccnz .LS_114_%=
.LB_115_%=:
s_waitcnt vmcnt(8)
v_max3_u32 v18, v161, v163, v165
v_mfma_f32_16x16x32_bf16 v[104:107], a[192:195], v[200:203], v[104:107]
v_max_u32 v18, v18, v167
v_cmp_eq_u32 vcc, -1, v18
v_mfma_f32_16x16x32_bf16 v[120:123], a[196:199], v[200:203], v[120:123]
v_mfma_f32_16x16x32_bf16 v[136:139], a[200:203], v[200:203], v[136:139]
s_cbranch_vccnz .LS_117_%=
.LB_118_%=:
s_waitcnt vmcnt(6)
v_max3_u32 v18, v169, v171, v173
v_mfma_f32_16x16x32_bf16 v[152:155], a[204:207], v[200:203], v[152:155]
buffer_load_dwordx4 v46, s[28:31], s74 offen offset:2048 lds
v_max_u32 v18, v18, v175
v_cmp_eq_u32 vcc, -1, v18
v_mfma_f32_16x16x32_bf16 v[104:107], a[208:211], v[204:207], v[104:107]
s_waitcnt lgkmcnt(2)
v_add_f32 v100, v100, v116
v_add_f32 v101, v101, v117
v_add_f32 v102, v102, v118
v_add_f32 v103, v103, v119
v_mfma_f32_16x16x32_bf16 v[120:123], a[212:215], v[204:207], v[120:123]
s_cbranch_vccnz .LS_120_%=
.LB_121_%=:
s_waitcnt vmcnt(5)
v_max3_u32 v18, v177, v179, v181
v_mfma_f32_16x16x32_bf16 v[136:139], a[216:219], v[204:207], v[136:139]
v_max_u32 v18, v18, v183
v_cmp_eq_u32 vcc, -1, v18
s_waitcnt lgkmcnt(1)
v_add_f32 v100, v100, v132
v_add_f32 v101, v101, v133
v_mfma_f32_16x16x32_bf16 v[152:155], a[220:223], v[204:207], v[152:155]
buffer_load_dwordx4 v47, s[28:31], s74 offen offset:3072 lds
s_add_u32 m0, s60, 0
s_add_u32 s74, s80, 1310720
v_mfma_f32_16x16x32_bf16 v[104:107], a[224:227], v[208:211], v[104:107]
s_cbranch_vccnz .LS_123_%=
.LB_124_%=:
s_waitcnt vmcnt(4)
v_max3_u32 v18, v185, v187, v189
v_mfma_f32_16x16x32_bf16 v[120:123], a[228:231], v[208:211], v[120:123]
v_max_u32 v18, v18, v191
v_cmp_eq_u32 vcc, -1, v18
v_add_f32 v102, v102, v134
v_add_f32 v103, v103, v135
v_mfma_f32_16x16x32_bf16 v[136:139], a[232:235], v[208:211], v[136:139]
s_waitcnt lgkmcnt(0)
v_add_f32 v100, v100, v148
v_add_f32 v101, v101, v149
v_add_f32 v102, v102, v150
v_add_f32 v103, v103, v151
v_mfma_f32_16x16x32_bf16 v[152:155], a[236:239], v[208:211], v[152:155]
s_cbranch_vccnz .LS_126_%=
.LB_127_%=:
buffer_load_dwordx4 v44, s[28:31], s74 offen offset:0 lds
v_mfma_f32_16x16x32_bf16 v[104:107], a[240:243], v[212:215], v[104:107]
v_mul_f32 v116, v100, v100
v_mul_f32 v117, v101, v101
v_mul_f32 v118, v102, v102
v_mul_f32 v119, v103, v103
v_mfma_f32_16x16x32_bf16 v[120:123], a[244:247], v[212:215], v[120:123]
v_fma_f32 v116, v116, s40, v34
v_fma_f32 v117, v117, s40, v34
v_fma_f32 v118, v118, s40, v34
v_fma_f32 v119, v119, s40, v34
v_mfma_f32_16x16x32_bf16 v[136:139], a[248:251], v[212:215], v[136:139]
v_mul_f32 v116, v116, v100
v_mul_f32 v117, v117, v101
v_mul_f32 v118, v118, v102
v_mul_f32 v119, v119, v103
v_mfma_f32_16x16x32_bf16 v[152:155], a[252:255], v[212:215], v[152:155]
buffer_load_dwordx4 v45, s[28:31], s74 offen offset:1024 lds
v_exp_f32 v116, v116
v_mfma_f32_16x16x32_bf16 v[124:127], a[132:135], v[160:163], v[124:127]
v_exp_f32 v117, v117
v_exp_f32 v118, v118
v_mfma_f32_16x16x32_bf16 v[124:127], a[148:151], v[164:167], v[124:127]
v_exp_f32 v119, v119
v_add_f32 v116, 1.0, v116
v_add_f32 v117, 1.0, v117
v_mfma_f32_16x16x32_bf16 v[124:127], a[164:167], v[168:171], v[124:127]
ds_write_b128 v74, v[120:123] offset:26624
v_add_f32 v118, 1.0, v118
v_add_f32 v119, 1.0, v119
v_mfma_f32_16x16x32_bf16 v[124:127], a[180:183], v[172:175], v[124:127]
buffer_load_dwordx4 v46, s[28:31], s74 offen offset:2048 lds
v_rcp_f32 v116, v116
v_mfma_f32_16x16x32_bf16 v[124:127], a[196:199], v[176:179], v[124:127]
v_rcp_f32 v117, v117
v_rcp_f32 v118, v118
v_mfma_f32_16x16x32_bf16 v[124:127], a[212:215], v[180:183], v[124:127]
ds_write_b128 v75, v[136:139] offset:30720
v_rcp_f32 v119, v119
v_mfma_f32_16x16x32_bf16 v[124:127], a[228:231], v[184:187], v[124:127]
v_mul_f32 v116, v116, v100
v_mul_f32 v117, v117, v101
v_mul_f32 v118, v118, v102
v_mul_f32 v119, v119, v103
v_mfma_f32_16x16x32_bf16 v[124:127], a[244:247], v[188:191], v[124:127]
buffer_load_dwordx4 v47, s[28:31], s74 offen offset:3072 lds
s_add_u32 m0, s60, 4096
s_add_u32 s74, s80, 1441792
v_mfma_f32_16x16x32_bf16 v[140:143], a[136:139], v[160:163], v[140:143]
ds_write_b128 v76, v[152:155] offset:34816
v_cvt_pk_bf16_f32 v226, v116, v117
v_cvt_pk_bf16_f32 v227, v118, v119
v_mfma_f32_16x16x32_bf16 v[140:143], a[152:155], v[164:167], v[140:143]
v_min_u32 v226, -2, v226
v_min_u32 v227, -2, v227
s_nop 0
v_permlane16_swap_b32 v224, v226
v_mfma_f32_16x16x32_bf16 v[140:143], a[168:171], v[168:171], v[140:143]
v_permlane16_swap_b32 v225, v227
v_mfma_f32_16x16x32_bf16 v[140:143], a[184:187], v[172:175], v[140:143]
ds_write_b128 v74, v[124:127] offset:27648
buffer_load_dwordx4 v44, s[28:31], s74 offen offset:0 lds
v_mfma_f32_16x16x32_bf16 v[140:143], a[200:203], v[176:179], v[140:143]
buffer_store_dwordx4 v[224:227], v66, s[8:11], s84 offen offset:0 sc1
v_mfma_f32_16x16x32_bf16 v[140:143], a[216:219], v[180:183], v[140:143]
v_mfma_f32_16x16x32_bf16 v[140:143], a[232:235], v[184:187], v[140:143]
v_mfma_f32_16x16x32_bf16 v[140:143], a[248:251], v[188:191], v[140:143]
buffer_load_dwordx4 v45, s[28:31], s74 offen offset:1024 lds
v_mfma_f32_16x16x32_bf16 v[156:159], a[140:143], v[160:163], v[156:159]
v_mfma_f32_16x16x32_bf16 v[156:159], a[156:159], v[164:167], v[156:159]
v_mfma_f32_16x16x32_bf16 v[156:159], a[172:175], v[168:171], v[156:159]
s_nop 3
ds_write_b128 v75, v[140:143] offset:31744
v_mfma_f32_16x16x32_bf16 v[156:159], a[188:191], v[172:175], v[156:159]
buffer_load_dwordx4 v46, s[28:31], s74 offen offset:2048 lds
v_mfma_f32_16x16x32_bf16 v[156:159], a[204:207], v[176:179], v[156:159]
v_mfma_f32_16x16x32_bf16 v[156:159], a[220:223], v[180:183], v[156:159]
v_mfma_f32_16x16x32_bf16 v[156:159], a[236:239], v[184:187], v[156:159]
v_mfma_f32_16x16x32_bf16 v[156:159], a[252:255], v[188:191], v[156:159]
buffer_load_dwordx4 v47, s[28:31], s74 offen offset:3072 lds
s_add_u32 m0, s60, 8192
s_add_u32 s74, s80, 1572864
v_mfma_f32_16x16x32_bf16 v[108:111], a[128:131], v[160:163], v[108:111]
v_mfma_f32_16x16x32_bf16 v[108:111], a[144:147], v[164:167], v[108:111]
v_mfma_f32_16x16x32_bf16 v[108:111], a[160:163], v[168:171], v[108:111]
s_nop 1
ds_write_b128 v76, v[156:159] offset:35840
v_mfma_f32_16x16x32_bf16 v[108:111], a[176:179], v[172:175], v[108:111]
buffer_load_dwordx4 v44, s[28:31], s74 offen offset:0 lds
v_mfma_f32_16x16x32_bf16 v[108:111], a[192:195], v[176:179], v[108:111]
v_mfma_f32_16x16x32_bf16 v[108:111], a[208:211], v[180:183], v[108:111]
s_waitcnt lgkmcnt(0)
s_barrier
ds_read_b128 v[120:123], v73 offset:26624
ds_read_b128 v[136:139], v73 offset:30720
ds_read_b128 v[152:155], v73 offset:34816
v_mfma_f32_16x16x32_bf16 v[108:111], a[224:227], v[184:187], v[108:111]
ds_read_b128 v[124:127], v73 offset:27648
ds_read_b128 v[140:143], v73 offset:31744
ds_read_b128 v[156:159], v73 offset:35840
v_mfma_f32_16x16x32_bf16 v[108:111], a[240:243], v[188:191], v[108:111]
buffer_load_dwordx4 v45, s[28:31], s74 offen offset:1024 lds
s_waitcnt lgkmcnt(3)
v_pk_add_f32 v[136:137], v[136:137], v[152:153]
s_waitcnt lgkmcnt(0)
v_pk_add_f32 v[140:141], v[140:141], v[156:157]
v_pk_add_f32 v[104:105], v[104:105], v[120:121]
s_nop 1
v_pk_add_f32 v[108:109], v[108:109], v[124:125]
v_pk_add_f32 v[138:139], v[138:139], v[154:155]
)ASM"
R"ASM(
v_pk_add_f32 v[142:143], v[142:143], v[158:159]
v_pk_add_f32 v[106:107], v[106:107], v[122:123]
v_pk_add_f32 v[110:111], v[110:111], v[126:127]
v_pk_add_f32 v[104:105], v[104:105], v[136:137]
v_pk_add_f32 v[108:109], v[108:109], v[140:141]
v_pk_add_f32 v[106:107], v[106:107], v[138:139]
v_pk_add_f32 v[110:111], v[110:111], v[142:143]
v_pk_mul_f32 v[120:121], v[104:105], v[104:105]
v_pk_mul_f32 v[124:125], v[108:109], v[108:109]
v_pk_mul_f32 v[122:123], v[106:107], v[106:107]
v_pk_mul_f32 v[126:127], v[110:111], v[110:111]
v_pk_fma_f32 v[120:121], v[120:121], s[40:41], v[34:35]
v_pk_fma_f32 v[124:125], v[124:125], s[40:41], v[34:35]
v_pk_fma_f32 v[122:123], v[122:123], s[40:41], v[34:35]
v_pk_fma_f32 v[126:127], v[126:127], s[40:41], v[34:35]
v_pk_mul_f32 v[120:121], v[120:121], v[104:105]
v_pk_mul_f32 v[124:125], v[124:125], v[108:109]
v_pk_mul_f32 v[122:123], v[122:123], v[106:107]
v_pk_mul_f32 v[126:127], v[126:127], v[110:111]
v_exp_f32 v120, v120
v_exp_f32 v124, v124
v_exp_f32 v121, v121
v_exp_f32 v125, v125
v_exp_f32 v122, v122
v_exp_f32 v126, v126
v_exp_f32 v123, v123
v_exp_f32 v127, v127
v_pk_add_f32 v[120:121], v[120:121], 1.0 op_sel_hi:[1,0]
v_pk_add_f32 v[124:125], v[124:125], 1.0 op_sel_hi:[1,0]
v_pk_add_f32 v[122:123], v[122:123], 1.0 op_sel_hi:[1,0]
v_pk_add_f32 v[126:127], v[126:127], 1.0 op_sel_hi:[1,0]
v_rcp_f32 v120, v120
v_rcp_f32 v124, v124
v_rcp_f32 v121, v121
v_rcp_f32 v125, v125
v_rcp_f32 v122, v122
v_rcp_f32 v126, v126
v_rcp_f32 v123, v123
v_rcp_f32 v127, v127
v_pk_mul_f32 v[120:121], v[120:121], v[104:105]
v_pk_mul_f32 v[124:125], v[124:125], v[108:109]
v_pk_mul_f32 v[122:123], v[122:123], v[106:107]
v_pk_mul_f32 v[126:127], v[126:127], v[110:111]
v_cvt_pk_bf16_f32 v228, v120, v121
v_cvt_pk_bf16_f32 v230, v124, v125
v_cvt_pk_bf16_f32 v229, v122, v123
v_cvt_pk_bf16_f32 v231, v126, v127
v_min_u32 v228, -2, v228
v_min_u32 v230, -2, v230
v_min_u32 v229, -2, v229
v_min_u32 v231, -2, v231
v_permlane16_swap_b32 v228, v230
s_nop 0
v_permlane16_swap_b32 v229, v231
buffer_store_dwordx4 v[228:231], v66, s[8:11], s84 offen offset:2048 sc1
.LJOIN_%=:
buffer_load_dwordx4 v46, s[28:31], s74 offen offset:2048 lds
buffer_load_dwordx4 v47, s[28:31], s74 offen offset:3072 lds
s_cmp_eq_u32 s59, 1
s_cbranch_scc1 .LQ_129_%=
s_add_u32 m0, s60, 12288
s_add_u32 s74, s80, 1703936
buffer_load_dwordx4 v44, s[28:31], s74 offen offset:0 lds
buffer_load_dwordx4 v45, s[28:31], s74 offen offset:1024 lds
buffer_load_dwordx4 v46, s[28:31], s74 offen offset:2048 lds
buffer_load_dwordx4 v47, s[28:31], s74 offen offset:3072 lds
s_add_u32 m0, s60, 24576
s_add_u32 s74, s77, 0
buffer_load_dwordx4 v36, s[16:19], s74 offen offset:0 lds
buffer_load_dwordx4 v37, s[16:19], s74 offen offset:1024 lds
buffer_load_dwordx4 v38, s[16:19], s74 offen offset:2048 lds
buffer_load_dwordx4 v39, s[16:19], s74 offen offset:3072 lds
s_add_u32 m0, s60, 28672
s_add_u32 s74, s77, 1024
buffer_load_dwordx4 v36, s[16:19], s74 offen offset:0 lds
buffer_load_dwordx4 v37, s[16:19], s74 offen offset:1024 lds
buffer_load_dwordx4 v38, s[16:19], s74 offen offset:2048 lds
buffer_load_dwordx4 v39, s[16:19], s74 offen offset:3072 lds
s_mov_b64 s[12:13], s[32:33]
s_add_u32 s75, s85, 0
buffer_load_dwordx4 v[160:163], v1, s[4:7], s75 offen offset:0 sc1
s_lshl_b32 s70, s54, 11
s_add_u32 s70, s70, 147456
v_lshl_add_u32 v20, v0, 3, s70
ds_read_b64_tr_b16 v[234:235], v20 offset:0
ds_read_b64_tr_b16 v[236:237], v20 offset:512
s_add_u32 s75, s85, 0
buffer_load_dwordx4 v[164:167], v1, s[4:7], s75 offen offset:1024 sc1
ds_read_b64_tr_b16 v[238:239], v20 offset:1024
ds_read_b64_tr_b16 v[240:241], v20 offset:1536
v_and_b32 v20, 15, v0
v_bfe_u32 v232, v0, 1, 2
v_lshlrev_b32 v232, 5, v232
s_add_u32 s75, s85, 4096
buffer_load_dwordx4 v[168:171], v1, s[4:7], s75 offen offset:0 sc1
v_lshl_or_b32 v232, v20, 7, v232
v_bfe_u32 v19, v0, 4, 2
v_lshl_or_b32 v232, v19, 3, v232
v_add_u32 v232, s70, v232
s_mul_i32 s70, s54, 288
s_add_u32 s70, s70, 158848
s_add_u32 s75, s85, 4096
buffer_load_dwordx4 v[172:175], v1, s[4:7], s75 offen offset:1024 sc1
v_lshl_add_u32 v20, v20, 1, s70
ds_read_u16 v242, v20
s_waitcnt lgkmcnt(4)
v_lshlrev_b32 v96, 16, v234
v_and_b32 v97, 0xffff0000, v234
v_lshlrev_b32 v98, 16, v235
v_and_b32 v99, 0xffff0000, v235
s_add_u32 s75, s85, 8192
buffer_load_dwordx4 v[176:179], v1, s[4:7], s75 offen offset:0 sc1
s_waitcnt lgkmcnt(3)
v_lshlrev_b32 v100, 16, v236
v_and_b32 v101, 0xffff0000, v236
v_lshlrev_b32 v102, 16, v237
v_and_b32 v103, 0xffff0000, v237
s_waitcnt lgkmcnt(2)
v_lshlrev_b32 v104, 16, v238
v_and_b32 v105, 0xffff0000, v238
s_add_u32 s75, s85, 8192
buffer_load_dwordx4 v[180:183], v1, s[4:7], s75 offen offset:1024 sc1
v_lshlrev_b32 v106, 16, v239
v_and_b32 v107, 0xffff0000, v239
s_waitcnt lgkmcnt(1)
v_lshlrev_b32 v108, 16, v240
v_and_b32 v109, 0xffff0000, v240
v_lshlrev_b32 v110, 16, v241
v_and_b32 v111, 0xffff0000, v241
s_add_u32 s75, s85, 12288
buffer_load_dwordx4 v[184:187], v1, s[4:7], s75 offen offset:0 sc1
s_waitcnt lgkmcnt(0)
v_lshlrev_b32 v242, 16, v242
v_pk_add_f32 v[96:97], v[96:97], v[242:243] op_sel_hi:[1,0]
v_pk_add_f32 v[98:99], v[98:99], v[242:243] op_sel_hi:[1,0]
v_pk_add_f32 v[100:101], v[100:101], v[242:243] op_sel_hi:[1,0]
v_pk_add_f32 v[102:103], v[102:103], v[242:243] op_sel_hi:[1,0]
v_pk_add_f32 v[104:105], v[104:105], v[242:243] op_sel_hi:[1,0]
s_add_u32 s75, s85, 12288
buffer_load_dwordx4 v[188:191], v1, s[4:7], s75 offen offset:1024 sc1
v_pk_add_f32 v[106:107], v[106:107], v[242:243] op_sel_hi:[1,0]
v_pk_add_f32 v[108:109], v[108:109], v[242:243] op_sel_hi:[1,0]
v_pk_add_f32 v[110:111], v[110:111], v[242:243] op_sel_hi:[1,0]
v_and_b32 v24, 31, v0
v_lshlrev_b32 v24, 3, v24
s_and_b32 s72, s42, 0
s_add_u32 s75, s85, 16384
buffer_load_dwordx4 v[192:195], v1, s[4:7], s75 offen offset:0 sc1
s_lshl_b32 s72, s72, 8
s_add_u32 s72, s72, 69632
s_add_u32 s75, s85, 16384
buffer_load_dwordx4 v[196:199], v1, s[4:7], s75 offen offset:1024 sc1
s_add_u32 s75, s85, 20480
buffer_load_dwordx4 v[200:203], v1, s[4:7], s75 offen offset:0 sc1
s_add_u32 s75, s85, 20480
buffer_load_dwordx4 v[204:207], v1, s[4:7], s75 offen offset:1024 sc1
s_add_u32 s75, s85, 24576
buffer_load_dwordx4 v[208:211], v1, s[4:7], s75 offen offset:0 sc1
s_add_u32 s75, s85, 24576
buffer_load_dwordx4 v[212:215], v1, s[4:7], s75 offen offset:1024 sc1
s_waitcnt vmcnt(12)
v_max3_u32 v18, v161, v163, v165
v_max_u32 v18, v18, v167
v_cmp_eq_u32 vcc, -1, v18
s_cbranch_vccnz .LS_132_%=
.LB_133_%=:
v_mfma_f32_16x16x32_bf16 v[96:99], v[160:163], a[0:3], v[96:99]
s_add_u32 s75, s85, 28672
s_add_u32 m0, s60, 32768
s_add_u32 s74, s77, 2048
v_mfma_f32_16x16x32_bf16 v[112:115], v[160:163], a[4:7], 0
buffer_load_dwordx4 v36, s[16:19], s74 offen offset:0 lds
v_mfma_f32_16x16x32_bf16 v[128:131], v[160:163], a[8:11], 0
s_waitcnt vmcnt(11)
v_max3_u32 v18, v169, v171, v173
v_max_u32 v18, v18, v175
v_mfma_f32_16x16x32_bf16 v[144:147], v[160:163], a[12:15], 0
ds_read_b64_tr_b16 a[128:129], v26 offset:20480
v_cmp_eq_u32 vcc, -1, v18
v_mfma_f32_16x16x32_bf16 v[100:103], v[164:167], a[0:3], v[100:103]
buffer_load_dwordx4 v[160:163], v1, s[4:7], s75 offen offset:0 sc1
v_mfma_f32_16x16x32_bf16 v[116:119], v[164:167], a[4:7], 0
s_cbranch_vccnz .LS_135_%=
.LB_136_%=:
v_mfma_f32_16x16x32_bf16 v[132:135], v[164:167], a[8:11], 0
s_add_u32 s75, s85, 32768
ds_read_b64_tr_b16 a[130:131], v26 offset:20992
ds_read_b64_tr_b16 a[132:133], v27 offset:20480
v_mfma_f32_16x16x32_bf16 v[148:151], v[164:167], a[12:15], 0
buffer_load_dwordx4 v[164:167], v1, s[4:7], s75 offen offset:0 sc1
v_mfma_f32_16x16x32_bf16 v[96:99], v[168:171], a[16:19], v[96:99]
s_add_u32 s75, s85, 0
ds_read_b64_tr_b16 a[134:135], v27 offset:20992
ds_read_b64_tr_b16 a[136:137], v28 offset:20480
v_mfma_f32_16x16x32_bf16 v[112:115], v[168:171], a[20:23], v[112:115]
ds_read_b64_tr_b16 a[138:139], v28 offset:20992
ds_read_b64_tr_b16 a[140:141], v29 offset:20480
v_mfma_f32_16x16x32_bf16 v[128:131], v[168:171], a[24:27], v[128:131]
s_waitcnt vmcnt(11)
v_max3_u32 v18, v177, v179, v181
v_max_u32 v18, v18, v183
v_mfma_f32_16x16x32_bf16 v[144:147], v[168:171], a[28:31], v[144:147]
ds_read_b64_tr_b16 a[142:143], v29 offset:20992
v_cmp_eq_u32 vcc, -1, v18
v_mfma_f32_16x16x32_bf16 v[100:103], v[172:175], a[16:19], v[100:103]
buffer_load_dwordx4 v[168:171], v1, s[4:7], s75 offen offset:2048 sc1
v_mfma_f32_16x16x32_bf16 v[116:119], v[172:175], a[20:23], v[116:119]
s_cbranch_vccnz .LS_138_%=
.LB_139_%=:
v_mfma_f32_16x16x32_bf16 v[132:135], v[172:175], a[24:27], v[132:135]
s_add_u32 s75, s85, 0
buffer_load_dwordx4 v37, s[16:19], s74 offen offset:1024 lds
v_mfma_f32_16x16x32_bf16 v[148:151], v[172:175], a[28:31], v[148:151]
buffer_load_dwordx4 v[172:175], v1, s[4:7], s75 offen offset:3072 sc1
v_mfma_f32_16x16x32_bf16 v[96:99], v[176:179], a[32:35], v[96:99]
s_add_u32 s75, s85, 4096
ds_read_b64_tr_b16 v[80:81], v26 offset:12288
ds_read_b64_tr_b16 v[82:83], v26 offset:12800
v_mfma_f32_16x16x32_bf16 v[112:115], v[176:179], a[36:39], v[112:115]
ds_read_b64_tr_b16 v[84:85], v27 offset:12288
ds_read_b64_tr_b16 v[86:87], v27 offset:12800
v_mfma_f32_16x16x32_bf16 v[128:131], v[176:179], a[40:43], v[128:131]
s_waitcnt vmcnt(12)
v_max3_u32 v18, v185, v187, v189
v_max_u32 v18, v18, v191
v_mfma_f32_16x16x32_bf16 v[144:147], v[176:179], a[44:47], v[144:147]
ds_read_b64_tr_b16 v[88:89], v28 offset:12288
v_cmp_eq_u32 vcc, -1, v18
v_mfma_f32_16x16x32_bf16 v[100:103], v[180:183], a[32:35], v[100:103]
buffer_load_dwordx4 v[176:179], v1, s[4:7], s75 offen offset:2048 sc1
v_mfma_f32_16x16x32_bf16 v[116:119], v[180:183], a[36:39], v[116:119]
s_cbranch_vccnz .LS_141_%=
.LB_142_%=:
v_mfma_f32_16x16x32_bf16 v[132:135], v[180:183], a[40:43], v[132:135]
s_add_u32 s75, s85, 4096
ds_read_b64_tr_b16 v[90:91], v28 offset:12800
ds_read_b64_tr_b16 v[92:93], v29 offset:12288
v_mfma_f32_16x16x32_bf16 v[148:151], v[180:183], a[44:47], v[148:151]
buffer_load_dwordx4 v[180:183], v1, s[4:7], s75 offen offset:3072 sc1
v_mfma_f32_16x16x32_bf16 v[96:99], v[184:187], a[48:51], v[96:99]
s_add_u32 s75, s85, 8192
buffer_load_dwordx4 v38, s[16:19], s74 offen offset:2048 lds
v_mfma_f32_16x16x32_bf16 v[112:115], v[184:187], a[52:55], v[112:115]
ds_read_b64_tr_b16 v[94:95], v29 offset:12800
v_mfma_f32_16x16x32_bf16 v[128:131], v[184:187], a[56:59], v[128:131]
s_waitcnt vmcnt(13)
v_max3_u32 v18, v193, v195, v197
v_max_u32 v18, v18, v199
v_mfma_f32_16x16x32_bf16 v[144:147], v[184:187], a[60:63], v[144:147]
v_cmp_eq_u32 vcc, -1, v18
v_mfma_f32_16x16x32_bf16 v[100:103], v[188:191], a[48:51], v[100:103]
buffer_load_dwordx4 v[184:187], v1, s[4:7], s75 offen offset:2048 sc1
v_mfma_f32_16x16x32_bf16 v[116:119], v[188:191], a[52:55], v[116:119]
s_cbranch_vccnz .LS_144_%=
.LB_145_%=:
v_mfma_f32_16x16x32_bf16 v[132:135], v[188:191], a[56:59], v[132:135]
s_add_u32 s75, s85, 8192
v_mfma_f32_16x16x32_bf16 v[148:151], v[188:191], a[60:63], v[148:151]
buffer_load_dwordx4 v[188:191], v1, s[4:7], s75 offen offset:3072 sc1
v_mfma_f32_16x16x32_bf16 v[96:99], v[192:195], a[64:67], v[96:99]
s_add_u32 s75, s85, 12288
v_mfma_f32_16x16x32_bf16 v[112:115], v[192:195], a[68:71], v[112:115]
v_mfma_f32_16x16x32_bf16 v[128:131], v[192:195], a[72:75], v[128:131]
s_waitcnt vmcnt(13)
v_max3_u32 v18, v201, v203, v205
v_max_u32 v18, v18, v207
v_mfma_f32_16x16x32_bf16 v[144:147], v[192:195], a[76:79], v[144:147]
v_cmp_eq_u32 vcc, -1, v18
v_mfma_f32_16x16x32_bf16 v[100:103], v[196:199], a[64:67], v[100:103]
buffer_load_dwordx4 v[192:195], v1, s[4:7], s75 offen offset:2048 sc1
v_mfma_f32_16x16x32_bf16 v[116:119], v[196:199], a[68:71], v[116:119]
s_cbranch_vccnz .LS_147_%=
.LB_148_%=:
v_mfma_f32_16x16x32_bf16 v[132:135], v[196:199], a[72:75], v[132:135]
s_add_u32 s75, s85, 12288
buffer_load_dwordx4 v39, s[16:19], s74 offen offset:3072 lds
v_mfma_f32_16x16x32_bf16 v[148:151], v[196:199], a[76:79], v[148:151]
buffer_load_dwordx4 v[196:199], v1, s[4:7], s75 offen offset:3072 sc1
v_mfma_f32_16x16x32_bf16 v[96:99], v[200:203], a[80:83], v[96:99]
s_add_u32 s75, s85, 16384
v_mfma_f32_16x16x32_bf16 v[112:115], v[200:203], a[84:87], v[112:115]
)ASM"
R"ASM(
v_mfma_f32_16x16x32_bf16 v[128:131], v[200:203], a[88:91], v[128:131]
s_waitcnt vmcnt(14)
v_max3_u32 v18, v209, v211, v213
v_max_u32 v18, v18, v215
v_mfma_f32_16x16x32_bf16 v[144:147], v[200:203], a[92:95], v[144:147]
v_cmp_eq_u32 vcc, -1, v18
v_mfma_f32_16x16x32_bf16 v[100:103], v[204:207], a[80:83], v[100:103]
buffer_load_dwordx4 v[200:203], v1, s[4:7], s75 offen offset:2048 sc1
v_mfma_f32_16x16x32_bf16 v[116:119], v[204:207], a[84:87], v[116:119]
s_cbranch_vccnz .LS_150_%=
.LB_151_%=:
v_mfma_f32_16x16x32_bf16 v[132:135], v[204:207], a[88:91], v[132:135]
s_add_u32 s75, s85, 16384
v_mfma_f32_16x16x32_bf16 v[148:151], v[204:207], a[92:95], v[148:151]
buffer_load_dwordx4 v[204:207], v1, s[4:7], s75 offen offset:3072 sc1
v_mfma_f32_16x16x32_bf16 v[96:99], v[208:211], a[96:99], v[96:99]
s_add_u32 s75, s85, 20480
v_mfma_f32_16x16x32_bf16 v[112:115], v[208:211], a[100:103], v[112:115]
v_mfma_f32_16x16x32_bf16 v[128:131], v[208:211], a[104:107], v[128:131]
s_waitcnt vmcnt(13)
v_max3_u32 v18, v161, v163, v165
v_max_u32 v18, v18, v167
v_mfma_f32_16x16x32_bf16 v[144:147], v[208:211], a[108:111], v[144:147]
v_cmp_eq_u32 vcc, -1, v18
v_mfma_f32_16x16x32_bf16 v[100:103], v[212:215], a[96:99], v[100:103]
buffer_load_dwordx4 v[208:211], v1, s[4:7], s75 offen offset:2048 sc1
v_mfma_f32_16x16x32_bf16 v[116:119], v[212:215], a[100:103], v[116:119]
s_cbranch_vccnz .LS_153_%=
.LB_154_%=:
v_mfma_f32_16x16x32_bf16 v[132:135], v[212:215], a[104:107], v[132:135]
s_add_u32 s75, s85, 20480
v_mfma_f32_16x16x32_bf16 v[148:151], v[212:215], a[108:111], v[148:151]
buffer_load_dwordx4 v[212:215], v1, s[4:7], s75 offen offset:3072 sc1
v_mfma_f32_16x16x32_bf16 v[96:99], v[160:163], a[112:115], v[96:99]
s_add_u32 s75, s85, 24576
v_mfma_f32_16x16x32_bf16 v[112:115], v[160:163], a[116:119], v[112:115]
v_mfma_f32_16x16x32_bf16 v[128:131], v[160:163], a[120:123], v[128:131]
s_waitcnt vmcnt(12)
v_max3_u32 v18, v169, v171, v173
v_max_u32 v18, v18, v175
v_mfma_f32_16x16x32_bf16 v[144:147], v[160:163], a[124:127], v[144:147]
v_cmp_eq_u32 vcc, -1, v18
s_waitcnt lgkmcnt(14)
v_mfma_f32_16x16x32_bf16 v[96:99], v[164:167], a[128:131], v[96:99]
buffer_load_dwordx4 v[160:163], v1, s[4:7], s75 offen offset:2048 sc1
s_waitcnt lgkmcnt(12)
v_mfma_f32_16x16x32_bf16 v[112:115], v[164:167], a[132:135], v[112:115]
s_cbranch_vccnz .LS_156_%=
.LB_157_%=:
s_waitcnt lgkmcnt(10)
v_mfma_f32_16x16x32_bf16 v[128:131], v[164:167], a[136:139], v[128:131]
s_add_u32 s75, s85, 24576
buffer_load_dwordx2 v[22:23], v24, s[4:7], s72 offen sc1
s_waitcnt lgkmcnt(8)
v_mfma_f32_16x16x32_bf16 v[144:147], v[164:167], a[140:143], v[144:147]
buffer_load_dwordx4 v[164:167], v1, s[4:7], s75 offen offset:3072 sc1
v_mfma_f32_16x16x32_bf16 v[104:107], v[168:171], a[0:3], v[104:107]
s_add_u32 s75, s85, 36864
v_mfma_f32_16x16x32_bf16 v[120:123], v[168:171], a[4:7], 0
v_mfma_f32_16x16x32_bf16 v[136:139], v[168:171], a[8:11], 0
s_waitcnt vmcnt(13)
v_max3_u32 v18, v177, v179, v181
v_max_u32 v18, v18, v183
v_mfma_f32_16x16x32_bf16 v[152:155], v[168:171], a[12:15], 0
v_cmp_eq_u32 vcc, -1, v18
v_mfma_f32_16x16x32_bf16 v[108:111], v[172:175], a[0:3], v[108:111]
buffer_load_dwordx4 v[168:171], v1, s[4:7], s75 offen offset:0 sc1
v_mfma_f32_16x16x32_bf16 v[124:127], v[172:175], a[4:7], 0
s_cbranch_vccnz .LS_159_%=
.LB_160_%=:
v_mfma_f32_16x16x32_bf16 v[140:143], v[172:175], a[8:11], 0
s_add_u32 s75, s85, 40960
v_mfma_f32_16x16x32_bf16 v[156:159], v[172:175], a[12:15], 0
buffer_load_dwordx4 v[172:175], v1, s[4:7], s75 offen offset:0 sc1
v_mfma_f32_16x16x32_bf16 v[104:107], v[176:179], a[16:19], v[104:107]
s_add_u32 s75, s85, 45056
ds_read_b64_tr_b16 a[0:1], v26 offset:16384
ds_read_b64_tr_b16 a[2:3], v26 offset:16896
v_mfma_f32_16x16x32_bf16 v[120:123], v[176:179], a[20:23], v[120:123]
ds_read_b64_tr_b16 a[4:5], v27 offset:16384
ds_read_b64_tr_b16 a[6:7], v27 offset:16896
v_mfma_f32_16x16x32_bf16 v[136:139], v[176:179], a[24:27], v[136:139]
s_waitcnt vmcnt(12)
v_max3_u32 v18, v185, v187, v189
v_max_u32 v18, v18, v191
v_mfma_f32_16x16x32_bf16 v[152:155], v[176:179], a[28:31], v[152:155]
ds_read_b64_tr_b16 a[8:9], v28 offset:16384
v_cmp_eq_u32 vcc, -1, v18
v_mfma_f32_16x16x32_bf16 v[108:111], v[180:183], a[16:19], v[108:111]
buffer_load_dwordx4 v[176:179], v1, s[4:7], s75 offen offset:0 sc1
v_mfma_f32_16x16x32_bf16 v[124:127], v[180:183], a[20:23], v[124:127]
s_cbranch_vccnz .LS_162_%=
.LB_163_%=:
v_mfma_f32_16x16x32_bf16 v[140:143], v[180:183], a[24:27], v[140:143]
s_add_u32 s75, s85, 49152
ds_read_b64_tr_b16 a[10:11], v28 offset:16896
ds_read_b64_tr_b16 a[12:13], v29 offset:16384
v_mfma_f32_16x16x32_bf16 v[156:159], v[180:183], a[28:31], v[156:159]
buffer_load_dwordx4 v[180:183], v1, s[4:7], s75 offen offset:0 sc1
v_mfma_f32_16x16x32_bf16 v[104:107], v[184:187], a[32:35], v[104:107]
s_add_u32 s75, s85, 53248
ds_read_b64_tr_b16 a[14:15], v29 offset:16896
ds_read_b64_tr_b16 a[16:17], v26 offset:0
v_mfma_f32_16x16x32_bf16 v[120:123], v[184:187], a[36:39], v[120:123]
ds_read_b64_tr_b16 a[18:19], v26 offset:512
ds_read_b64_tr_b16 a[20:21], v27 offset:0
v_mfma_f32_16x16x32_bf16 v[136:139], v[184:187], a[40:43], v[136:139]
s_waitcnt vmcnt(11)
v_max3_u32 v18, v193, v195, v197
v_max_u32 v18, v18, v199
v_mfma_f32_16x16x32_bf16 v[152:155], v[184:187], a[44:47], v[152:155]
ds_read_b64_tr_b16 a[22:23], v27 offset:512
v_cmp_eq_u32 vcc, -1, v18
v_mfma_f32_16x16x32_bf16 v[108:111], v[188:191], a[32:35], v[108:111]
buffer_load_dwordx4 v[184:187], v1, s[4:7], s75 offen offset:0 sc1
v_mfma_f32_16x16x32_bf16 v[124:127], v[188:191], a[36:39], v[124:127]
s_cbranch_vccnz .LS_165_%=
.LB_166_%=:
v_mfma_f32_16x16x32_bf16 v[140:143], v[188:191], a[40:43], v[140:143]
s_add_u32 s75, s85, 28672
ds_read_b64_tr_b16 a[24:25], v28 offset:0
ds_read_b64_tr_b16 a[26:27], v28 offset:512
v_mfma_f32_16x16x32_bf16 v[156:159], v[188:191], a[44:47], v[156:159]
buffer_load_dwordx4 v[188:191], v1, s[4:7], s75 offen offset:1024 sc1
v_mfma_f32_16x16x32_bf16 v[104:107], v[192:195], a[48:51], v[104:107]
s_add_u32 s75, s85, 32768
ds_read_b64_tr_b16 a[28:29], v29 offset:0
ds_read_b64_tr_b16 a[30:31], v29 offset:512
v_mfma_f32_16x16x32_bf16 v[120:123], v[192:195], a[52:55], v[120:123]
ds_read_b64_tr_b16 a[32:33], v26 offset:4096
ds_read_b64_tr_b16 a[34:35], v26 offset:4608
v_mfma_f32_16x16x32_bf16 v[136:139], v[192:195], a[56:59], v[136:139]
s_waitcnt vmcnt(11)
v_max3_u32 v18, v201, v203, v205
v_max_u32 v18, v18, v207
v_mfma_f32_16x16x32_bf16 v[152:155], v[192:195], a[60:63], v[152:155]
ds_read_b64_tr_b16 a[36:37], v27 offset:4096
v_cmp_eq_u32 vcc, -1, v18
v_mfma_f32_16x16x32_bf16 v[108:111], v[196:199], a[48:51], v[108:111]
s_waitcnt lgkmcnt(3)
s_add_u32 m0, s60, 0
buffer_load_dwordx4 v[192:195], v1, s[4:7], s75 offen offset:1024 sc1
v_mfma_f32_16x16x32_bf16 v[124:127], v[196:199], a[52:55], v[124:127]
s_cbranch_vccnz .LS_168_%=
.LB_169_%=:
v_mfma_f32_16x16x32_bf16 v[140:143], v[196:199], a[56:59], v[140:143]
s_add_u32 s75, s85, 36864
s_add_u32 s74, s77, 786432
ds_read_b64_tr_b16 a[38:39], v27 offset:4608
v_mfma_f32_16x16x32_bf16 v[156:159], v[196:199], a[60:63], v[156:159]
buffer_load_dwordx4 v[196:199], v1, s[4:7], s75 offen offset:1024 sc1
v_mfma_f32_16x16x32_bf16 v[104:107], v[200:203], a[64:67], v[104:107]
s_add_u32 s75, s85, 40960
buffer_load_dwordx4 v36, s[16:19], s74 offen offset:0 lds
v_mfma_f32_16x16x32_bf16 v[120:123], v[200:203], a[68:71], v[120:123]
ds_read_b64_tr_b16 a[40:41], v28 offset:4096
ds_read_b64_tr_b16 a[42:43], v28 offset:4608
v_mfma_f32_16x16x32_bf16 v[136:139], v[200:203], a[72:75], v[136:139]
s_waitcnt vmcnt(12)
v_max3_u32 v18, v209, v211, v213
v_max_u32 v18, v18, v215
v_mfma_f32_16x16x32_bf16 v[152:155], v[200:203], a[76:79], v[152:155]
ds_read_b64_tr_b16 a[44:45], v29 offset:4096
v_cmp_eq_u32 vcc, -1, v18
v_mfma_f32_16x16x32_bf16 v[108:111], v[204:207], a[64:67], v[108:111]
buffer_load_dwordx4 v[200:203], v1, s[4:7], s75 offen offset:1024 sc1
v_mfma_f32_16x16x32_bf16 v[124:127], v[204:207], a[68:71], v[124:127]
s_cbranch_vccnz .LS_171_%=
.LB_172_%=:
v_mfma_f32_16x16x32_bf16 v[140:143], v[204:207], a[72:75], v[140:143]
s_add_u32 s75, s85, 45056
ds_read_b64_tr_b16 a[46:47], v29 offset:4608
ds_read_b64_tr_b16 a[48:49], v26 offset:8192
v_mfma_f32_16x16x32_bf16 v[156:159], v[204:207], a[76:79], v[156:159]
buffer_load_dwordx4 v[204:207], v1, s[4:7], s75 offen offset:1024 sc1
v_mfma_f32_16x16x32_bf16 v[104:107], v[208:211], a[80:83], v[104:107]
s_add_u32 s75, s85, 49152
ds_read_b64_tr_b16 a[50:51], v26 offset:8704
ds_read_b64_tr_b16 a[52:53], v27 offset:8192
v_mfma_f32_16x16x32_bf16 v[120:123], v[208:211], a[84:87], v[120:123]
ds_read_b64_tr_b16 a[54:55], v27 offset:8704
ds_read_b64_tr_b16 a[56:57], v28 offset:8192
v_mfma_f32_16x16x32_bf16 v[136:139], v[208:211], a[88:91], v[136:139]
s_waitcnt vmcnt(11)
v_max3_u32 v18, v161, v163, v165
v_max_u32 v18, v18, v167
v_mfma_f32_16x16x32_bf16 v[152:155], v[208:211], a[92:95], v[152:155]
ds_read_b64_tr_b16 a[58:59], v28 offset:8704
v_cmp_eq_u32 vcc, -1, v18
v_mfma_f32_16x16x32_bf16 v[108:111], v[212:215], a[80:83], v[108:111]
buffer_load_dwordx4 v[208:211], v1, s[4:7], s75 offen offset:1024 sc1
v_mfma_f32_16x16x32_bf16 v[124:127], v[212:215], a[84:87], v[124:127]
s_cbranch_vccnz .LS_174_%=
.LB_175_%=:
v_mfma_f32_16x16x32_bf16 v[140:143], v[212:215], a[88:91], v[140:143]
s_add_u32 s75, s85, 53248
buffer_load_dwordx4 v37, s[16:19], s74 offen offset:1024 lds
v_mfma_f32_16x16x32_bf16 v[156:159], v[212:215], a[92:95], v[156:159]
buffer_load_dwordx4 v[212:215], v1, s[4:7], s75 offen offset:1024 sc1
v_mfma_f32_16x16x32_bf16 v[104:107], v[160:163], a[96:99], v[104:107]
s_add_u32 s75, s85, 28672
ds_read_b64_tr_b16 a[60:61], v29 offset:8192
ds_read_b64_tr_b16 a[62:63], v29 offset:8704
v_mfma_f32_16x16x32_bf16 v[120:123], v[160:163], a[100:103], v[120:123]
s_barrier
v_mfma_f32_16x16x32_bf16 v[136:139], v[160:163], a[104:107], v[136:139]
s_waitcnt vmcnt(12)
v_max3_u32 v18, v169, v171, v173
v_max_u32 v18, v18, v175
v_mfma_f32_16x16x32_bf16 v[152:155], v[160:163], a[108:111], v[152:155]
v_cmp_eq_u32 vcc, -1, v18
v_mfma_f32_16x16x32_bf16 v[108:111], v[164:167], a[96:99], v[108:111]
buffer_load_dwordx4 v[160:163], v1, s[4:7], s75 offen offset:2048 sc1
v_mfma_f32_16x16x32_bf16 v[124:127], v[164:167], a[100:103], v[124:127]
s_cbranch_vccnz .LS_177_%=
.LB_178_%=:
v_mfma_f32_16x16x32_bf16 v[140:143], v[164:167], a[104:107], v[140:143]
s_add_u32 s75, s85, 32768
v_mfma_f32_16x16x32_bf16 v[156:159], v[164:167], a[108:111], v[156:159]
buffer_load_dwordx4 v[164:167], v1, s[4:7], s75 offen offset:2048 sc1
v_mfma_f32_16x16x32_bf16 v[96:99], v[168:171], a[0:3], v[96:99]
s_add_u32 s75, s85, 36864
buffer_load_dwordx4 v38, s[16:19], s74 offen offset:2048 lds
v_mfma_f32_16x16x32_bf16 v[112:115], v[168:171], a[4:7], v[112:115]
ds_read_b64_tr_b16 a[64:65], v30 offset:24576
ds_read_b64_tr_b16 a[66:67], v30 offset:25088
v_mfma_f32_16x16x32_bf16 v[128:131], v[168:171], a[8:11], v[128:131]
s_waitcnt vmcnt(13)
v_max3_u32 v18, v177, v179, v181
v_max_u32 v18, v18, v183
v_mfma_f32_16x16x32_bf16 v[144:147], v[168:171], a[12:15], v[144:147]
ds_read_b64_tr_b16 a[68:69], v30 offset:28672
v_cmp_eq_u32 vcc, -1, v18
v_mfma_f32_16x16x32_bf16 v[96:99], v[172:175], a[16:19], v[96:99]
buffer_load_dwordx4 v[168:171], v1, s[4:7], s75 offen offset:2048 sc1
v_mfma_f32_16x16x32_bf16 v[112:115], v[172:175], a[20:23], v[112:115]
s_cbranch_vccnz .LS_180_%=
.LB_181_%=:
v_mfma_f32_16x16x32_bf16 v[128:131], v[172:175], a[24:27], v[128:131]
s_add_u32 s75, s85, 40960
ds_read_b64_tr_b16 a[70:71], v30 offset:29184
ds_read_b64_tr_b16 a[72:73], v30 offset:32768
v_mfma_f32_16x16x32_bf16 v[144:147], v[172:175], a[28:31], v[144:147]
buffer_load_dwordx4 v[172:175], v1, s[4:7], s75 offen offset:2048 sc1
s_waitcnt lgkmcnt(15)
v_mfma_f32_16x16x32_bf16 v[96:99], v[176:179], a[32:35], v[96:99]
s_add_u32 s75, s85, 45056
ds_read_b64_tr_b16 a[74:75], v30 offset:33280
ds_read_b64_tr_b16 a[76:77], v31 offset:24576
v_mfma_f32_16x16x32_bf16 v[112:115], v[176:179], a[36:39], v[112:115]
ds_read_b64_tr_b16 a[78:79], v31 offset:25088
ds_read_b64_tr_b16 a[80:81], v31 offset:28672
)ASM"
R"ASM(
v_mfma_f32_16x16x32_bf16 v[128:131], v[176:179], a[40:43], v[128:131]
s_waitcnt vmcnt(13)
v_max3_u32 v18, v185, v187, v189
v_max_u32 v18, v18, v191
s_waitcnt lgkmcnt(15)
v_mfma_f32_16x16x32_bf16 v[144:147], v[176:179], a[44:47], v[144:147]
ds_read_b64_tr_b16 a[82:83], v31 offset:29184
v_cmp_eq_u32 vcc, -1, v18
v_mfma_f32_16x16x32_bf16 v[96:99], v[180:183], a[48:51], v[96:99]
buffer_load_dwordx4 v[176:179], v1, s[4:7], s75 offen offset:2048 sc1
s_waitcnt lgkmcnt(14)
v_mfma_f32_16x16x32_bf16 v[112:115], v[180:183], a[52:55], v[112:115]
s_cbranch_vccnz .LS_183_%=
.LB_184_%=:
ds_read_b64_tr_b16 a[84:85], v31 offset:32768
ds_read_b64_tr_b16 a[86:87], v31 offset:33280
ds_read_b64_tr_b16 a[88:89], v32 offset:24576
ds_read_b64_tr_b16 a[90:91], v32 offset:25088
ds_read_b64_tr_b16 a[92:93], v32 offset:28672
ds_read_b64_tr_b16 a[94:95], v32 offset:29184
ds_read_b64_tr_b16 a[96:97], v32 offset:32768
ds_read_b64_tr_b16 a[98:99], v32 offset:33280
ds_read_b64_tr_b16 a[100:101], v33 offset:24576
ds_read_b64_tr_b16 a[102:103], v33 offset:25088
ds_read_b64_tr_b16 a[104:105], v33 offset:28672
ds_read_b64_tr_b16 a[106:107], v33 offset:29184
ds_read_b64_tr_b16 a[108:109], v33 offset:32768
ds_read_b64_tr_b16 a[110:111], v33 offset:33280
s_waitcnt lgkmcnt(15)
v_mfma_f32_16x16x32_bf16 v[128:131], v[180:183], a[56:59], v[128:131]
s_add_u32 s75, s85, 49152
buffer_load_dwordx4 v39, s[16:19], s74 offen offset:3072 lds
v_mfma_f32_16x16x32_bf16 v[144:147], v[180:183], a[60:63], v[144:147]
s_add_u32 m0, s60, 4096
buffer_load_dwordx4 v[180:183], v1, s[4:7], s75 offen offset:2048 sc1
v_mfma_f32_16x16x32_bf16 v[96:99], v[184:187], v[80:83], v[96:99]
s_add_u32 s75, s85, 53248
s_add_u32 s74, s77, 787456
v_cmp_ne_u64 s[68:69], s[62:63], v[22:23]
v_mfma_f32_16x16x32_bf16 v[112:115], v[184:187], v[84:87], v[112:115]
s_cmp_lg_u64 s[68:69], 0
s_cbranch_scc1 .LS_130_%=
.LD_131_%=:
v_mfma_f32_16x16x32_bf16 v[128:131], v[184:187], v[88:91], v[128:131]
s_waitcnt vmcnt(14)
v_max3_u32 v18, v193, v195, v197
v_max_u32 v18, v18, v199
v_mfma_f32_16x16x32_bf16 v[144:147], v[184:187], v[92:95], v[144:147]
s_waitcnt lgkmcnt(0)
s_barrier
v_cmp_eq_u32 vcc, -1, v18
v_mfma_f32_16x16x32_bf16 v[100:103], v[188:191], a[112:115], v[100:103]
buffer_load_dwordx4 v[184:187], v1, s[4:7], s75 offen offset:2048 sc1
s_add_u32 s75, s85, 28672
v_mfma_f32_16x16x32_bf16 v[116:119], v[188:191], a[116:119], v[116:119]
s_cbranch_vccnz .LS_186_%=
.LB_187_%=:
v_mfma_f32_16x16x32_bf16 v[132:135], v[188:191], a[120:123], v[132:135]
ds_write_b128 v74, v[112:115] offset:24576
v_mfma_f32_16x16x32_bf16 v[148:151], v[188:191], a[124:127], v[148:151]
buffer_load_dwordx4 v[188:191], v1, s[4:7], s75 offen offset:3072 sc1
v_mfma_f32_16x16x32_bf16 v[100:103], v[192:195], a[128:131], v[100:103]
s_add_u32 s75, s85, 32768
buffer_load_dwordx4 v36, s[16:19], s74 offen offset:0 lds
v_mfma_f32_16x16x32_bf16 v[116:119], v[192:195], a[132:135], v[116:119]
ds_write_b128 v75, v[128:131] offset:28672
v_mfma_f32_16x16x32_bf16 v[132:135], v[192:195], a[136:139], v[132:135]
s_waitcnt vmcnt(14)
v_max3_u32 v18, v201, v203, v205
v_max_u32 v18, v18, v207
v_cmp_eq_u32 vcc, -1, v18
v_mfma_f32_16x16x32_bf16 v[148:151], v[192:195], a[140:143], v[148:151]
buffer_load_dwordx4 v[192:195], v1, s[4:7], s75 offen offset:3072 sc1
v_mfma_f32_16x16x32_bf16 v[100:103], v[196:199], a[0:3], v[100:103]
s_cbranch_vccnz .LS_189_%=
.LB_190_%=:
s_add_u32 s75, s85, 36864
v_mfma_f32_16x16x32_bf16 v[116:119], v[196:199], a[4:7], v[116:119]
ds_write_b128 v76, v[144:147] offset:32768
v_mfma_f32_16x16x32_bf16 v[132:135], v[196:199], a[8:11], v[132:135]
v_mfma_f32_16x16x32_bf16 v[148:151], v[196:199], a[12:15], v[148:151]
buffer_load_dwordx4 v[196:199], v1, s[4:7], s75 offen offset:3072 sc1
v_mfma_f32_16x16x32_bf16 v[100:103], v[200:203], a[16:19], v[100:103]
s_add_u32 s75, s85, 40960
s_waitcnt lgkmcnt(0)
s_barrier
ds_read_b128 v[112:115], v73 offset:24576
ds_read_b128 v[128:131], v73 offset:28672
v_mfma_f32_16x16x32_bf16 v[116:119], v[200:203], a[20:23], v[116:119]
ds_read_b128 v[144:147], v73 offset:32768
v_mfma_f32_16x16x32_bf16 v[132:135], v[200:203], a[24:27], v[132:135]
s_waitcnt vmcnt(13)
v_max3_u32 v18, v209, v211, v213
v_max_u32 v18, v18, v215
v_cmp_eq_u32 vcc, -1, v18
v_mfma_f32_16x16x32_bf16 v[148:151], v[200:203], a[28:31], v[148:151]
buffer_load_dwordx4 v[200:203], v1, s[4:7], s75 offen offset:3072 sc1
v_mfma_f32_16x16x32_bf16 v[100:103], v[204:207], a[32:35], v[100:103]
s_cbranch_vccnz .LS_192_%=
.LB_193_%=:
s_add_u32 s75, s85, 45056
v_mfma_f32_16x16x32_bf16 v[116:119], v[204:207], a[36:39], v[116:119]
buffer_load_dwordx4 v37, s[16:19], s74 offen offset:1024 lds
v_mfma_f32_16x16x32_bf16 v[132:135], v[204:207], a[40:43], v[132:135]
s_waitcnt lgkmcnt(2)
v_add_f32 v96, v96, v112
v_add_f32 v97, v97, v113
v_add_f32 v98, v98, v114
v_add_f32 v99, v99, v115
v_mfma_f32_16x16x32_bf16 v[148:151], v[204:207], a[44:47], v[148:151]
buffer_load_dwordx4 v[204:207], v1, s[4:7], s75 offen offset:3072 sc1
s_waitcnt lgkmcnt(1)
v_add_f32 v96, v96, v128
v_add_f32 v97, v97, v129
v_mfma_f32_16x16x32_bf16 v[100:103], v[208:211], a[48:51], v[100:103]
s_add_u32 s75, s85, 49152
v_add_f32 v98, v98, v130
v_add_f32 v99, v99, v131
s_waitcnt lgkmcnt(0)
v_add_f32 v96, v96, v144
v_add_f32 v97, v97, v145
v_mfma_f32_16x16x32_bf16 v[116:119], v[208:211], a[52:55], v[116:119]
v_add_f32 v98, v98, v146
v_add_f32 v99, v99, v147
v_cvt_pk_bf16_f32 v224, v96, v97
v_cvt_pk_bf16_f32 v225, v98, v99
v_mfma_f32_16x16x32_bf16 v[132:135], v[208:211], a[56:59], v[132:135]
s_waitcnt vmcnt(14)
v_max3_u32 v18, v161, v163, v165
v_max_u32 v18, v18, v167
v_cmp_eq_u32 vcc, -1, v18
v_min_u32 v224, -2, v224
v_mfma_f32_16x16x32_bf16 v[148:151], v[208:211], a[60:63], v[148:151]
buffer_load_dwordx4 v[208:211], v1, s[4:7], s75 offen offset:3072 sc1
v_min_u32 v225, -2, v225
v_xor_b32 v234, 0, v232
v_mfma_f32_16x16x32_bf16 v[100:103], v[212:215], v[80:83], v[100:103]
s_cbranch_vccnz .LS_195_%=
.LB_196_%=:
s_add_u32 s75, s85, 53248
v_mfma_f32_16x16x32_bf16 v[116:119], v[212:215], v[84:87], v[116:119]
ds_write_b64 v234, v[224:225]
v_mfma_f32_16x16x32_bf16 v[132:135], v[212:215], v[88:91], v[132:135]
v_mfma_f32_16x16x32_bf16 v[148:151], v[212:215], v[92:95], v[148:151]
buffer_load_dwordx4 v[212:215], v1, s[4:7], s75 offen offset:3072 sc1
v_mfma_f32_16x16x32_bf16 v[104:107], v[160:163], a[112:115], v[104:107]
buffer_load_dwordx4 v38, s[16:19], s74 offen offset:2048 lds
v_mfma_f32_16x16x32_bf16 v[120:123], v[160:163], a[116:119], v[120:123]
v_mfma_f32_16x16x32_bf16 v[136:139], v[160:163], a[120:123], v[136:139]
s_waitcnt vmcnt(14)
s_add_u32 s98, s24, s66
v_max3_u32 v18, v169, v171, v173
v_max_u32 v18, v18, v175
v_cmp_eq_u32 vcc, -1, v18
v_mfma_f32_16x16x32_bf16 v[152:155], v[160:163], a[124:127], v[152:155]
s_lshr_b32 s98, s98, 7
s_sub_u32 s98, 3, s98
s_and_b32 s98, s98, 7
ds_write_b128 v74, v[116:119] offset:25600
v_mfma_f32_16x16x32_bf16 v[104:107], v[164:167], a[128:131], v[104:107]
s_cbranch_vccnz .LS_198_%=
.LB_199_%=:
buffer_load_dwordx4 v39, s[16:19], s74 offen offset:3072 lds
v_mfma_f32_16x16x32_bf16 v[120:123], v[164:167], a[132:135], v[120:123]
s_add_u32 m0, s60, 8192
s_add_u32 s74, s77, 788480
s_min_u32 s98, s98, 6
s_lshl_b32 s98, s98, 7
s_add_u32 s98, s98, s66
v_mfma_f32_16x16x32_bf16 v[136:139], v[164:167], a[136:139], v[136:139]
s_waitcnt vmcnt(12)
v_max3_u32 v18, v177, v179, v181
v_max_u32 v18, v18, v183
v_cmp_eq_u32 vcc, -1, v18
v_and_b32 v20, 15, v0
v_mfma_f32_16x16x32_bf16 v[152:155], v[164:167], a[140:143], v[152:155]
ds_write_b128 v75, v[132:135] offset:29696
v_mul_u32_u24 v20, 114688, v20
v_mfma_f32_16x16x32_bf16 v[104:107], v[168:171], a[0:3], v[104:107]
s_cbranch_vccnz .LS_201_%=
.LB_202_%=:
s_waitcnt vmcnt(10)
v_max3_u32 v18, v185, v187, v189
v_mfma_f32_16x16x32_bf16 v[120:123], v[168:171], a[4:7], v[120:123]
buffer_load_dwordx4 v36, s[16:19], s74 offen offset:0 lds
v_max_u32 v18, v18, v191
v_cmp_eq_u32 vcc, -1, v18
v_mfma_f32_16x16x32_bf16 v[136:139], v[168:171], a[8:11], v[136:139]
ds_write_b128 v76, v[148:151] offset:33792
s_bfm_b64 exec, 16, 0
buffer_load_dword v77, v20, s[24:27], s98 offen
s_mov_b64 exec, -1
v_mfma_f32_16x16x32_bf16 v[152:155], v[168:171], a[12:15], v[152:155]
s_cbranch_vccnz .LS_204_%=
.LB_205_%=:
s_waitcnt vmcnt(9)
v_max3_u32 v18, v193, v195, v197
v_mfma_f32_16x16x32_bf16 v[104:107], v[172:175], a[16:19], v[104:107]
s_lshr_b32 s98, s20, 7
s_sub_u32 s98, 3, s98
s_and_b32 s98, s98, 7
v_max_u32 v18, v18, v199
v_cmp_eq_u32 vcc, -1, v18
v_mfma_f32_16x16x32_bf16 v[120:123], v[172:175], a[20:23], v[120:123]
s_waitcnt lgkmcnt(0)
s_barrier
buffer_load_dwordx4 v37, s[16:19], s74 offen offset:1024 lds
ds_read_b128 v[116:119], v73 offset:25600
v_mfma_f32_16x16x32_bf16 v[136:139], v[172:175], a[24:27], v[136:139]
s_cbranch_vccnz .LS_207_%=
.LB_208_%=:
s_waitcnt vmcnt(7)
v_max3_u32 v18, v201, v203, v205
v_mfma_f32_16x16x32_bf16 v[152:155], v[172:175], a[28:31], v[152:155]
s_xor_b32 s99, s98, s56
ds_read_b128 v[132:135], v73 offset:29696
ds_read_b128 v[148:151], v73 offset:33792
v_max_u32 v18, v18, v207
v_cmp_eq_u32 vcc, -1, v18
v_mfma_f32_16x16x32_bf16 v[104:107], v[176:179], a[32:35], v[104:107]
s_and_b32 s99, s99, 4
s_lshl_b32 s99, s99, 28
s_lshl_b32 s98, s98, 7
s_or_b32 s99, s99, s98
s_lshl_b32 s98, s97, 12
v_mfma_f32_16x16x32_bf16 v[120:123], v[176:179], a[36:39], v[120:123]
s_cbranch_vccnz .LS_210_%=
.LB_211_%=:
s_waitcnt vmcnt(5)
v_max3_u32 v18, v209, v211, v213
v_mfma_f32_16x16x32_bf16 v[136:139], v[176:179], a[40:43], v[136:139]
buffer_load_dwordx4 v38, s[16:19], s74 offen offset:2048 lds
v_max_u32 v18, v18, v215
v_cmp_eq_u32 vcc, -1, v18
v_mfma_f32_16x16x32_bf16 v[152:155], v[176:179], a[44:47], v[152:155]
v_and_b32 v20, 3, v0
v_lshlrev_b32 v20, 10, v20
v_or_b32 v20, s99, v20
v_mfma_f32_16x16x32_bf16 v[104:107], v[180:183], a[48:51], v[104:107]
s_cbranch_vccnz .LS_213_%=
.LB_214_%=:
s_waitcnt lgkmcnt(2)
v_add_f32 v100, v100, v116
v_add_f32 v101, v101, v117
v_mfma_f32_16x16x32_bf16 v[120:123], v[180:183], a[52:55], v[120:123]
v_add_f32 v102, v102, v118
v_add_f32 v103, v103, v119
s_waitcnt lgkmcnt(1)
v_add_f32 v100, v100, v132
v_add_f32 v101, v101, v133
v_mfma_f32_16x16x32_bf16 v[136:139], v[180:183], a[56:59], v[136:139]
buffer_load_dwordx4 v39, s[16:19], s74 offen offset:3072 lds
v_add_f32 v102, v102, v134
v_add_f32 v103, v103, v135
v_mfma_f32_16x16x32_bf16 v[152:155], v[180:183], a[60:63], v[152:155]
s_waitcnt lgkmcnt(0)
v_add_f32 v100, v100, v148
v_add_f32 v101, v101, v149
v_add_f32 v102, v102, v150
v_add_f32 v103, v103, v151
v_mfma_f32_16x16x32_bf16 v[104:107], v[184:187], v[80:83], v[104:107]
v_cvt_pk_bf16_f32 v226, v100, v101
v_cvt_pk_bf16_f32 v227, v102, v103
v_min_u32 v226, -2, v226
v_min_u32 v227, -2, v227
v_mfma_f32_16x16x32_bf16 v[120:123], v[184:187], v[84:87], v[120:123]
s_bfm_b64 exec, 4, 0
buffer_load_dword v77, v20, s[20:23], s98 offen
s_mov_b64 exec, -1
v_xor_b32 v235, 32, v232
v_mfma_f32_16x16x32_bf16 v[136:139], v[184:187], v[88:91], v[136:139]
ds_write_b64 v235, v[226:227]
v_permlane16_swap_b32 v224, v226
v_mfma_f32_16x16x32_bf16 v[152:155], v[184:187], v[92:95], v[152:155]
v_permlane16_swap_b32 v225, v227
v_mfma_f32_16x16x32_bf16 v[124:127], v[188:191], a[116:119], v[124:127]
buffer_store_dwordx4 v[224:227], v67, s[32:35], s93 offen offset:0 sc1
v_mfma_f32_16x16x32_bf16 v[124:127], v[192:195], a[132:135], v[124:127]
v_mfma_f32_16x16x32_bf16 v[124:127], v[196:199], a[4:7], v[124:127]
ds_write_b128 v74, v[120:123] offset:26624
v_mfma_f32_16x16x32_bf16 v[124:127], v[200:203], a[20:23], v[124:127]
v_mfma_f32_16x16x32_bf16 v[124:127], v[204:207], a[36:39], v[124:127]
v_mfma_f32_16x16x32_bf16 v[124:127], v[208:211], a[52:55], v[124:127]
ds_write_b128 v75, v[136:139] offset:30720
v_mfma_f32_16x16x32_bf16 v[124:127], v[212:215], v[84:87], v[124:127]
v_mfma_f32_16x16x32_bf16 v[140:143], v[188:191], a[120:123], v[140:143]
v_mfma_f32_16x16x32_bf16 v[140:143], v[192:195], a[136:139], v[140:143]
ds_write_b128 v76, v[152:155] offset:34816
v_mfma_f32_16x16x32_bf16 v[140:143], v[196:199], a[8:11], v[140:143]
v_mfma_f32_16x16x32_bf16 v[140:143], v[200:203], a[24:27], v[140:143]
)ASM"
R"ASM(
v_mfma_f32_16x16x32_bf16 v[140:143], v[204:207], a[40:43], v[140:143]
s_nop 1
ds_write_b128 v74, v[124:127] offset:27648
v_mfma_f32_16x16x32_bf16 v[140:143], v[208:211], a[56:59], v[140:143]
v_mfma_f32_16x16x32_bf16 v[140:143], v[212:215], v[88:91], v[140:143]
v_mfma_f32_16x16x32_bf16 v[156:159], v[188:191], a[124:127], v[156:159]
v_mfma_f32_16x16x32_bf16 v[156:159], v[192:195], a[140:143], v[156:159]
v_mfma_f32_16x16x32_bf16 v[156:159], v[196:199], a[12:15], v[156:159]
s_nop 4
ds_write_b128 v75, v[140:143] offset:31744
v_mfma_f32_16x16x32_bf16 v[156:159], v[200:203], a[28:31], v[156:159]
v_mfma_f32_16x16x32_bf16 v[156:159], v[204:207], a[44:47], v[156:159]
v_mfma_f32_16x16x32_bf16 v[156:159], v[208:211], a[60:63], v[156:159]
v_mfma_f32_16x16x32_bf16 v[156:159], v[212:215], v[92:95], v[156:159]
v_mfma_f32_16x16x32_bf16 v[108:111], v[188:191], a[112:115], v[108:111]
v_mfma_f32_16x16x32_bf16 v[108:111], v[192:195], a[128:131], v[108:111]
v_mfma_f32_16x16x32_bf16 v[108:111], v[196:199], a[0:3], v[108:111]
s_nop 4
ds_write_b128 v76, v[156:159] offset:35840
v_mfma_f32_16x16x32_bf16 v[108:111], v[200:203], a[16:19], v[108:111]
v_mfma_f32_16x16x32_bf16 v[108:111], v[204:207], a[32:35], v[108:111]
v_mfma_f32_16x16x32_bf16 v[108:111], v[208:211], a[48:51], v[108:111]
s_waitcnt lgkmcnt(0)
s_barrier
ds_read_b128 v[120:123], v73 offset:26624
ds_read_b128 v[136:139], v73 offset:30720
ds_read_b128 v[152:155], v73 offset:34816
v_mfma_f32_16x16x32_bf16 v[108:111], v[212:215], v[80:83], v[108:111]
ds_read_b128 v[124:127], v73 offset:27648
ds_read_b128 v[140:143], v73 offset:31744
ds_read_b128 v[156:159], v73 offset:35840
s_waitcnt lgkmcnt(3)
v_pk_add_f32 v[136:137], v[136:137], v[152:153]
s_waitcnt lgkmcnt(0)
v_pk_add_f32 v[140:141], v[140:141], v[156:157]
v_pk_add_f32 v[104:105], v[104:105], v[120:121]
v_pk_add_f32 v[108:109], v[108:109], v[124:125]
v_pk_add_f32 v[138:139], v[138:139], v[154:155]
v_pk_add_f32 v[142:143], v[142:143], v[158:159]
v_pk_add_f32 v[106:107], v[106:107], v[122:123]
v_pk_add_f32 v[110:111], v[110:111], v[126:127]
v_pk_add_f32 v[104:105], v[104:105], v[136:137]
v_pk_add_f32 v[108:109], v[108:109], v[140:141]
v_pk_add_f32 v[106:107], v[106:107], v[138:139]
v_pk_add_f32 v[110:111], v[110:111], v[142:143]
v_cvt_pk_bf16_f32 v228, v104, v105
v_cvt_pk_bf16_f32 v230, v108, v109
v_cvt_pk_bf16_f32 v229, v106, v107
v_cvt_pk_bf16_f32 v231, v110, v111
v_min_u32 v228, -2, v228
v_min_u32 v230, -2, v230
v_min_u32 v229, -2, v229
v_min_u32 v231, -2, v231
v_xor_b32 v236, 64, v232
v_xor_b32 v237, 96, v232
ds_write_b64 v236, v[228:229]
ds_write_b64 v237, v[230:231]
v_permlane16_swap_b32 v228, v230
v_permlane16_swap_b32 v229, v231
buffer_store_dwordx4 v[228:231], v67, s[32:35], s93 offen offset:512 sc1
s_waitcnt vmcnt(3)
s_barrier
ds_read_b64_tr_b16 a[0:1], v30 offset:0
ds_read_b64_tr_b16 a[2:3], v30 offset:512
ds_read_b64_tr_b16 a[4:5], v30 offset:4096
ds_read_b64_tr_b16 a[6:7], v30 offset:4608
ds_read_b64_tr_b16 a[8:9], v30 offset:8192
ds_read_b64_tr_b16 a[10:11], v30 offset:8704
ds_read_b64_tr_b16 a[12:13], v31 offset:0
ds_read_b64_tr_b16 a[14:15], v31 offset:512
ds_read_b64_tr_b16 a[16:17], v31 offset:4096
ds_read_b64_tr_b16 a[18:19], v31 offset:4608
ds_read_b64_tr_b16 a[20:21], v31 offset:8192
ds_read_b64_tr_b16 a[22:23], v31 offset:8704
ds_read_b64_tr_b16 a[24:25], v32 offset:0
ds_read_b64_tr_b16 a[26:27], v32 offset:512
ds_read_b64_tr_b16 a[28:29], v32 offset:4096
ds_read_b64_tr_b16 a[30:31], v32 offset:4608
ds_read_b64_tr_b16 a[32:33], v32 offset:8192
ds_read_b64_tr_b16 a[34:35], v32 offset:8704
ds_read_b64_tr_b16 a[36:37], v33 offset:0
ds_read_b64_tr_b16 a[38:39], v33 offset:512
ds_read_b64_tr_b16 a[40:41], v33 offset:4096
ds_read_b64_tr_b16 a[42:43], v33 offset:4608
ds_read_b64_tr_b16 a[44:45], v33 offset:8192
ds_read_b64_tr_b16 a[46:47], v33 offset:8704
s_mov_b32 s59, 1
s_mov_b32 s32, s50
s_and_b32 s33, s51, 0xffff
s_add_u32 s81, s81, 2359296
s_add_u32 s82, s82, 2359296
s_add_u32 s83, s83, 2359296
s_add_u32 s84, s84, 2359296
s_add_u32 s85, s85, 2359296
s_add_u32 s92, s92, 2359296
s_add_u32 s87, s87, 131072
s_cmp_eq_u32 s55, 0
s_cselect_b32 s70, 1, 0
s_max_u32 s70, s70, s54
s_lshl_b32 s70, s70, 10
s_add_u32 s94, s70, s89
s_add_u32 s95, s89, 4096
s_waitcnt lgkmcnt(0)
s_barrier
s_sleep 2
v_mov_b32 v128, 0
s_add_u32 m0, s60, 0
s_nop 0
buffer_load_dwordx4 v62, s[12:15], s90 offen offset:0 sc1 lds
v_mov_b32 v129, 0
v_dot2c_f32_bf16 v128, v10, v224
v_dot2c_f32_bf16 v129, v224, v224
v_dot2c_f32_bf16 v128, v10, v225
v_dot2c_f32_bf16 v129, v225, v225
v_dot2c_f32_bf16 v128, v10, v226
v_dot2c_f32_bf16 v129, v226, v226
v_dot2c_f32_bf16 v128, v10, v227
v_dot2c_f32_bf16 v129, v227, v227
s_add_u32 s75, s91, 0
s_add_u32 m0, s60, 1024
s_nop 0
buffer_load_dwordx4 v62, s[12:15], s75 offen offset:0 sc1 lds
v_dot2c_f32_bf16 v128, v10, v228
v_dot2c_f32_bf16 v129, v228, v228
v_dot2c_f32_bf16 v128, v10, v229
v_dot2c_f32_bf16 v129, v229, v229
v_dot2c_f32_bf16 v128, v10, v230
v_dot2c_f32_bf16 v129, v230, v230
v_dot2c_f32_bf16 v128, v10, v231
v_dot2c_f32_bf16 v129, v231, v231
s_nop 1
v_mov_b32 v130, v128
s_add_u32 s75, s91, 16384
s_add_u32 m0, s60, 2048
s_nop 0
buffer_load_dwordx4 v62, s[12:15], s75 offen offset:0 sc1 lds
v_mov_b32 v131, v129
v_permlane16_swap_b32 v128, v130
s_nop 0
v_permlane16_swap_b32 v129, v131
v_add_f32 v128, v128, v130
v_add_f32 v129, v129, v131
v_mov_b32 v130, v128
v_mov_b32 v131, v129
s_nop 0
v_permlane32_swap_b32 v128, v130
s_add_u32 s75, s91, 32768
s_add_u32 m0, s60, 3072
s_nop 0
buffer_load_dwordx4 v62, s[12:15], s75 offen offset:0 sc1 lds
v_permlane32_swap_b32 v129, v131
v_add_f32 v128, v128, v130
v_add_f32 v129, v129, v131
v_min_u32 v128, -2, v128
v_min_u32 v129, -2, v129
v_lshlrev_b32 v18, 3, v0
v_cmp_gt_u32 vcc, 16, v0
s_and_saveexec_b64 s[68:69], vcc
buffer_store_dwordx2 v[128:129], v18, s[4:7], s88 offen sc1
s_mov_b64 exec, s[68:69]
s_add_u32 s75, s91, 49152
s_add_u32 m0, s60, 4096
s_nop 0
buffer_load_dwordx4 v62, s[12:15], s75 offen offset:0 sc1 lds
s_add_u32 m0, s60, 4864
s_nop 0
buffer_load_dwordx4 v62, s[12:15], s90 offen offset:256 sc1 lds
s_add_u32 s75, s91, 0
s_add_u32 m0, s60, 5888
s_nop 0
buffer_load_dwordx4 v62, s[12:15], s75 offen offset:256 sc1 lds
s_add_u32 s75, s91, 16384
s_add_u32 m0, s60, 6912
s_nop 0
buffer_load_dwordx4 v62, s[12:15], s75 offen offset:256 sc1 lds
s_add_u32 s75, s91, 32768
s_add_u32 m0, s60, 7936
s_nop 0
buffer_load_dwordx4 v62, s[12:15], s75 offen offset:256 sc1 lds
s_add_u32 s75, s91, 49152
s_add_u32 m0, s60, 8960
s_nop 0
buffer_load_dwordx4 v62, s[12:15], s75 offen offset:256 sc1 lds
s_add_u32 m0, s60, 24576
s_add_u32 s74, s77, 98304
buffer_load_dwordx4 v36, s[16:19], s74 offen offset:0 lds
buffer_load_dwordx4 v37, s[16:19], s74 offen offset:1024 lds
buffer_load_dwordx4 v38, s[16:19], s74 offen offset:2048 lds
buffer_load_dwordx4 v39, s[16:19], s74 offen offset:3072 lds
s_add_u32 m0, s60, 28672
s_add_u32 s74, s77, 99328
buffer_load_dwordx4 v36, s[16:19], s74 offen offset:0 lds
buffer_load_dwordx4 v37, s[16:19], s74 offen offset:1024 lds
buffer_load_dwordx4 v38, s[16:19], s74 offen offset:2048 lds
buffer_load_dwordx4 v39, s[16:19], s74 offen offset:3072 lds
s_add_u32 m0, s60, 32768
s_add_u32 s74, s77, 100352
buffer_load_dwordx4 v36, s[16:19], s74 offen offset:0 lds
buffer_load_dwordx4 v37, s[16:19], s74 offen offset:1024 lds
buffer_load_dwordx4 v38, s[16:19], s74 offen offset:2048 lds
buffer_load_dwordx4 v39, s[16:19], s74 offen offset:3072 lds
s_waitcnt vmcnt(17)
ds_read_b128 v[96:99], v73 offset:0
ds_read_b128 v[100:103], v73 offset:1024
ds_read_b128 v[104:107], v73 offset:2048
ds_read_b128 v[108:111], v73 offset:3072
ds_read_b128 v[112:115], v73 offset:4096
s_waitcnt lgkmcnt(0)
v_max3_u32 v226, v97, v99, v101
v_max3_u32 v226, v226, v103, v105
v_max3_u32 v226, v226, v107, v109
v_max3_u32 v226, v226, v111, v113
v_max_u32 v226, v226, v115
v_cmp_eq_u32 vcc, -1, v226
s_cbranch_vccnz .LS_303_%=
.LB_304_%=:
s_barrier
ds_read_b128 v[116:119], v63 offset:0
ds_read_b128 v[120:123], v63 offset:1024
ds_read_b128 v[124:127], v63 offset:2048
ds_read_b128 v[128:131], v63 offset:3072
ds_read_b128 v[132:135], v63 offset:4096
s_waitcnt lgkmcnt(4)
v_mfma_f32_16x16x32_bf16 v[156:159], a[68:71], v[116:119], 0
s_add_u32 m0, s60, 9728
s_waitcnt vmcnt(0)
ds_read_b128 v[136:139], v64 offset:0
v_mfma_f32_16x16x32_bf16 v[192:195], v[116:119], a[72:75], 0
s_waitcnt lgkmcnt(4)
buffer_load_dwordx4 v62, s[12:15], s90 offen offset:512 sc1 lds
v_mfma_f32_16x16x32_bf16 v[160:163], a[68:71], v[120:123], 0
s_add_u32 s75, s91, 0
s_add_u32 m0, s60, 10752
ds_read_b128 v[140:143], v64 offset:1024
v_mfma_f32_16x16x32_bf16 v[176:179], a[64:67], v[120:123], 0
s_barrier
ds_read_b64_tr_b16 a[48:49], v30 offset:24576
ds_read_b64_tr_b16 a[50:51], v30 offset:25088
v_mfma_f32_16x16x32_bf16 v[196:199], v[120:123], a[72:75], 0
s_waitcnt lgkmcnt(6)
ds_read_b128 v[144:147], v64 offset:2048
ds_read_b64_tr_b16 a[52:53], v30 offset:28672
v_mfma_f32_16x16x32_bf16 v[164:167], a[68:71], v[124:127], 0
buffer_load_dwordx4 v62, s[12:15], s75 offen offset:512 sc1 lds
v_mfma_f32_16x16x32_bf16 v[180:183], a[64:67], v[124:127], 0
s_add_u32 s75, s91, 16384
s_add_u32 m0, s60, 11776
ds_read_b128 v[148:151], v64 offset:3072
v_mfma_f32_16x16x32_bf16 v[200:203], v[124:127], a[72:75], 0
s_waitcnt lgkmcnt(8)
ds_read_b64_tr_b16 a[54:55], v30 offset:29184
ds_read_b64_tr_b16 a[56:57], v30 offset:32768
v_mfma_f32_16x16x32_bf16 v[168:171], a[68:71], v[128:131], 0
ds_read_b128 v[152:155], v64 offset:4096
ds_read_b64_tr_b16 a[58:59], v30 offset:33280
v_mfma_f32_16x16x32_bf16 v[184:187], a[64:67], v[128:131], 0
buffer_load_dwordx4 v62, s[12:15], s75 offen offset:512 sc1 lds
v_mfma_f32_16x16x32_bf16 v[204:207], v[128:131], a[72:75], 0
s_waitcnt lgkmcnt(11)
s_add_u32 s75, s91, 32768
s_add_u32 m0, s60, 12800
v_mfma_f32_16x16x32_bf16 v[172:175], a[68:71], v[132:135], 0
ds_read_b128 v[96:99], v73 offset:5120
ds_read_b128 v[100:103], v73 offset:6144
v_mfma_f32_16x16x32_bf16 v[188:191], a[64:67], v[132:135], 0
ds_read_b128 v[104:107], v73 offset:7168
ds_read_b128 v[108:111], v73 offset:8192
v_mfma_f32_16x16x32_bf16 v[208:211], v[132:135], a[72:75], 0
s_waitcnt lgkmcnt(14)
buffer_load_dwordx4 v62, s[12:15], s75 offen offset:512 sc1 lds
v_mfma_f32_16x16x32_bf16 v[156:159], a[80:83], v[136:139], v[156:159]
s_add_u32 s75, s91, 49152
s_add_u32 m0, s60, 13824
ds_read_b128 v[116:119], v63 offset:36864
v_mfma_f32_16x16x32_bf16 v[192:195], v[136:139], a[84:87], v[192:195]
s_waitcnt lgkmcnt(14)
ds_read_b128 v[112:115], v73 offset:9216
ds_read_b64_tr_b16 a[60:61], v31 offset:24576
v_mfma_f32_16x16x32_bf16 v[160:163], a[80:83], v[140:143], v[160:163]
ds_read_b128 v[120:123], v63 offset:37888
ds_read_b64_tr_b16 a[62:63], v31 offset:25088
v_mfma_f32_16x16x32_bf16 v[176:179], a[76:79], v[140:143], v[176:179]
buffer_load_dwordx4 v62, s[12:15], s75 offen offset:512 sc1 lds
v_mfma_f32_16x16x32_bf16 v[196:199], v[140:143], a[84:87], v[196:199]
s_waitcnt lgkmcnt(15)
ds_read_b128 v[124:127], v63 offset:38912
ds_read_b64_tr_b16 a[112:113], v31 offset:28672
v_mfma_f32_16x16x32_bf16 v[164:167], a[80:83], v[144:147], v[164:167]
ds_read_b64_tr_b16 a[114:115], v31 offset:29184
ds_read_b64_tr_b16 a[116:117], v31 offset:32768
v_mfma_f32_16x16x32_bf16 v[180:183], a[76:79], v[144:147], v[180:183]
ds_read_b128 v[128:131], v63 offset:39936
ds_read_b64_tr_b16 a[118:119], v31 offset:33280
v_mfma_f32_16x16x32_bf16 v[200:203], v[144:147], a[84:87], v[200:203]
s_waitcnt lgkmcnt(15)
buffer_load_dwordx4 v[216:219], v70, s[4:7], s94 offen sc1
v_mfma_f32_16x16x32_bf16 v[168:171], a[80:83], v[148:151], v[168:171]
s_waitcnt lgkmcnt(9)
ds_read_b128 v[132:135], v63 offset:40960
v_max3_u32 v226, v97, v99, v101
v_mfma_f32_16x16x32_bf16 v[184:187], a[76:79], v[148:151], v[184:187]
v_max3_u32 v226, v226, v103, v105
v_max3_u32 v226, v226, v107, v109
v_mfma_f32_16x16x32_bf16 v[204:207], v[148:151], a[84:87], v[204:207]
v_max3_u32 v226, v226, v111, v113
v_max_u32 v226, v226, v115
v_mfma_f32_16x16x32_bf16 v[172:175], a[80:83], v[152:155], v[172:175]
buffer_load_dwordx4 v[220:223], v70, s[4:7], s95 offen sc1
)ASM"
R"ASM(
v_mfma_f32_16x16x32_bf16 v[188:191], a[76:79], v[152:155], v[188:191]
s_add_u32 m0, s60, 14592
ds_read_b64_tr_b16 a[120:121], v32 offset:24576
v_cmp_eq_u32 vcc, -1, v226
v_mfma_f32_16x16x32_bf16 v[208:211], v[152:155], a[84:87], v[208:211]
ds_read_b64_tr_b16 a[122:123], v32 offset:25088
ds_read_b64_tr_b16 a[124:125], v32 offset:28672
v_mfma_f32_16x16x32_bf16 v[156:159], a[92:95], v[116:119], v[156:159]
ds_read_b128 v[136:139], v64 offset:36864
ds_read_b64_tr_b16 a[126:127], v32 offset:29184
v_mfma_f32_16x16x32_bf16 v[192:195], v[116:119], a[96:99], v[192:195]
s_waitcnt lgkmcnt(13)
buffer_load_dwordx4 v62, s[12:15], s90 offen offset:768 sc1 lds
v_mfma_f32_16x16x32_bf16 v[160:163], a[92:95], v[120:123], v[160:163]
s_add_u32 s75, s91, 0
s_add_u32 m0, s60, 15616
ds_read_b128 v[140:143], v64 offset:37888
v_mfma_f32_16x16x32_bf16 v[176:179], a[88:91], v[120:123], v[176:179]
s_cbranch_vccnz .LS_306_%=
.LB_307_%=:
v_mfma_f32_16x16x32_bf16 v[196:199], v[120:123], a[96:99], v[196:199]
s_waitcnt lgkmcnt(12)
ds_read_b128 v[144:147], v64 offset:38912
ds_read_b64_tr_b16 a[128:129], v32 offset:32768
v_mfma_f32_16x16x32_bf16 v[164:167], a[92:95], v[124:127], v[164:167]
buffer_load_dwordx4 v62, s[12:15], s75 offen offset:768 sc1 lds
v_mfma_f32_16x16x32_bf16 v[180:183], a[88:91], v[124:127], v[180:183]
s_add_u32 s75, s91, 16384
s_add_u32 m0, s60, 16640
ds_read_b128 v[148:151], v64 offset:39936
v_mfma_f32_16x16x32_bf16 v[200:203], v[124:127], a[96:99], v[200:203]
s_waitcnt lgkmcnt(11)
ds_read_b64_tr_b16 a[130:131], v32 offset:33280
ds_read_b64_tr_b16 a[132:133], v33 offset:24576
v_mfma_f32_16x16x32_bf16 v[168:171], a[92:95], v[128:131], v[168:171]
ds_read_b128 v[152:155], v64 offset:40960
ds_read_b64_tr_b16 a[134:135], v33 offset:25088
v_mfma_f32_16x16x32_bf16 v[184:187], a[88:91], v[128:131], v[184:187]
buffer_load_dwordx4 v62, s[12:15], s75 offen offset:768 sc1 lds
v_mfma_f32_16x16x32_bf16 v[204:207], v[128:131], a[96:99], v[204:207]
s_waitcnt lgkmcnt(13)
s_add_u32 s75, s91, 32768
s_add_u32 m0, s60, 17664
v_mfma_f32_16x16x32_bf16 v[172:175], a[92:95], v[132:135], v[172:175]
ds_read_b64_tr_b16 a[136:137], v33 offset:28672
ds_read_b64_tr_b16 a[138:139], v33 offset:29184
v_mfma_f32_16x16x32_bf16 v[188:191], a[88:91], v[132:135], v[188:191]
s_barrier
ds_read_b64_tr_b16 a[140:141], v33 offset:32768
v_mfma_f32_16x16x32_bf16 v[208:211], v[132:135], a[96:99], v[208:211]
s_waitcnt lgkmcnt(12)
buffer_load_dwordx4 v62, s[12:15], s75 offen offset:768 sc1 lds
v_mfma_f32_16x16x32_bf16 v[156:159], a[104:107], v[136:139], v[156:159]
s_add_u32 s75, s91, 49152
s_add_u32 m0, s60, 18688
ds_read_b128 v[116:119], v63 offset:5120
v_mfma_f32_16x16x32_bf16 v[192:195], v[136:139], a[108:111], v[192:195]
s_waitcnt lgkmcnt(11)
ds_read_b64_tr_b16 a[142:143], v33 offset:33280
v_mfma_f32_16x16x32_bf16 v[160:163], a[104:107], v[140:143], v[160:163]
ds_read_b128 v[120:123], v63 offset:6144
v_mfma_f32_16x16x32_bf16 v[176:179], a[100:103], v[140:143], v[176:179]
buffer_load_dwordx4 v62, s[12:15], s75 offen offset:768 sc1 lds
v_mfma_f32_16x16x32_bf16 v[196:199], v[140:143], a[108:111], v[196:199]
s_waitcnt lgkmcnt(12)
s_add_u32 m0, s60, 20480
ds_read_b128 v[124:127], v63 offset:7168
v_mfma_f32_16x16x32_bf16 v[164:167], a[104:107], v[144:147], v[164:167]
s_add_u32 s74, s77, 884736
v_mfma_f32_16x16x32_bf16 v[180:183], a[100:103], v[144:147], v[180:183]
s_waitcnt lgkmcnt(2)
s_barrier
ds_read_b128 v[128:131], v63 offset:8192
v_mfma_f32_16x16x32_bf16 v[200:203], v[144:147], a[108:111], v[200:203]
buffer_load_dwordx4 v36, s[16:19], s74 offen offset:0 lds
v_mfma_f32_16x16x32_bf16 v[168:171], a[104:107], v[148:151], v[168:171]
ds_read_b128 v[132:135], v63 offset:9216
v_mfma_f32_16x16x32_bf16 v[184:187], a[100:103], v[148:151], v[184:187]
v_mfma_f32_16x16x32_bf16 v[204:207], v[148:151], a[108:111], v[204:207]
v_mfma_f32_16x16x32_bf16 v[172:175], a[104:107], v[152:155], v[172:175]
buffer_load_dwordx4 v37, s[16:19], s74 offen offset:1024 lds
v_mfma_f32_16x16x32_bf16 v[188:191], a[100:103], v[152:155], v[188:191]
v_mfma_f32_16x16x32_bf16 v[208:211], v[152:155], a[108:111], v[208:211]
v_mfma_f32_16x16x32_bf16 v[156:159], a[4:7], v[116:119], v[156:159]
ds_read_b128 v[136:139], v64 offset:5120
v_mfma_f32_16x16x32_bf16 v[192:195], v[116:119], a[8:11], v[192:195]
s_waitcnt lgkmcnt(4)
s_barrier
v_mfma_f32_16x16x32_bf16 v[160:163], a[4:7], v[120:123], v[160:163]
ds_read_b128 v[140:143], v64 offset:6144
v_mfma_f32_16x16x32_bf16 v[176:179], a[0:3], v[120:123], v[176:179]
buffer_load_dwordx4 v38, s[16:19], s74 offen offset:2048 lds
v_mfma_f32_16x16x32_bf16 v[196:199], v[120:123], a[8:11], v[196:199]
s_waitcnt lgkmcnt(4)
ds_read_b128 v[144:147], v64 offset:7168
v_mfma_f32_16x16x32_bf16 v[164:167], a[4:7], v[124:127], v[164:167]
s_waitcnt vmcnt(10)
ds_read_b128 v[96:99], v73 offset:10240
ds_read_b128 v[100:103], v73 offset:11264
v_mfma_f32_16x16x32_bf16 v[180:183], a[0:3], v[124:127], v[180:183]
ds_read_b128 v[148:151], v64 offset:8192
ds_read_b128 v[104:107], v73 offset:12288
v_mfma_f32_16x16x32_bf16 v[200:203], v[124:127], a[8:11], v[200:203]
s_waitcnt lgkmcnt(8)
buffer_load_dwordx4 v39, s[16:19], s74 offen offset:3072 lds
v_mfma_f32_16x16x32_bf16 v[168:171], a[4:7], v[128:131], v[168:171]
s_add_u32 m0, s60, 24576
s_add_u32 s74, s77, 885760
ds_read_b128 v[152:155], v64 offset:9216
v_mfma_f32_16x16x32_bf16 v[184:187], a[0:3], v[128:131], v[184:187]
ds_read_b128 v[108:111], v73 offset:13312
ds_read_b128 v[112:115], v73 offset:14336
v_mfma_f32_16x16x32_bf16 v[204:207], v[128:131], a[8:11], v[204:207]
s_waitcnt lgkmcnt(10)
v_mfma_f32_16x16x32_bf16 v[172:175], a[4:7], v[132:135], v[172:175]
buffer_load_dwordx4 v36, s[16:19], s74 offen offset:0 lds
v_mfma_f32_16x16x32_bf16 v[188:191], a[0:3], v[132:135], v[188:191]
v_mfma_f32_16x16x32_bf16 v[208:211], v[132:135], a[8:11], v[208:211]
s_waitcnt lgkmcnt(9)
v_mfma_f32_16x16x32_bf16 v[156:159], a[16:19], v[136:139], v[156:159]
ds_read_b128 v[116:119], v63 offset:41984
v_mfma_f32_16x16x32_bf16 v[192:195], v[136:139], a[20:23], v[192:195]
s_waitcnt lgkmcnt(9)
buffer_load_dwordx4 v37, s[16:19], s74 offen offset:1024 lds
v_mfma_f32_16x16x32_bf16 v[160:163], a[16:19], v[140:143], v[160:163]
s_waitcnt lgkmcnt(1)
ds_read_b128 v[120:123], v63 offset:43008
v_max3_u32 v226, v97, v99, v101
v_mfma_f32_16x16x32_bf16 v[176:179], a[12:15], v[140:143], v[176:179]
v_max3_u32 v226, v226, v103, v105
v_max3_u32 v226, v226, v107, v109
v_mfma_f32_16x16x32_bf16 v[196:199], v[140:143], a[20:23], v[196:199]
ds_read_b128 v[124:127], v63 offset:44032
v_max3_u32 v226, v226, v111, v113
v_mfma_f32_16x16x32_bf16 v[164:167], a[16:19], v[144:147], v[164:167]
buffer_load_dwordx4 v38, s[16:19], s74 offen offset:2048 lds
v_mfma_f32_16x16x32_bf16 v[180:183], a[12:15], v[144:147], v[180:183]
ds_read_b128 v[128:131], v63 offset:45056
v_max_u32 v226, v226, v115
v_mfma_f32_16x16x32_bf16 v[200:203], v[144:147], a[20:23], v[200:203]
v_cmp_eq_u32 vcc, -1, v226
v_mfma_f32_16x16x32_bf16 v[168:171], a[16:19], v[148:151], v[168:171]
ds_read_b128 v[132:135], v63 offset:46080
v_mfma_f32_16x16x32_bf16 v[184:187], a[12:15], v[148:151], v[184:187]
buffer_load_dwordx4 v39, s[16:19], s74 offen offset:3072 lds
v_mfma_f32_16x16x32_bf16 v[204:207], v[148:151], a[20:23], v[204:207]
s_add_u32 m0, s60, 28672
s_add_u32 s74, s77, 886784
v_mfma_f32_16x16x32_bf16 v[172:175], a[16:19], v[152:155], v[172:175]
s_cbranch_vccnz .LS_309_%=
.LB_310_%=:
v_mfma_f32_16x16x32_bf16 v[188:191], a[12:15], v[152:155], v[188:191]
s_waitcnt vmcnt(13)
v_max3_u32 v20, v217, v219, v221
v_max_u32 v20, v20, v223
v_mfma_f32_16x16x32_bf16 v[208:211], v[152:155], a[20:23], v[208:211]
s_waitcnt lgkmcnt(4)
buffer_load_dwordx4 v36, s[16:19], s74 offen offset:0 lds
v_mfma_f32_16x16x32_bf16 v[156:159], a[28:31], v[116:119], v[156:159]
ds_read_b128 v[136:139], v64 offset:41984
v_cmp_eq_u32 vcc, -1, v20
v_mfma_f32_16x16x32_bf16 v[192:195], v[116:119], a[32:35], v[192:195]
s_waitcnt lgkmcnt(4)
v_mfma_f32_16x16x32_bf16 v[160:163], a[28:31], v[120:123], v[160:163]
ds_read_b128 v[140:143], v64 offset:43008
v_mfma_f32_16x16x32_bf16 v[176:179], a[24:27], v[120:123], v[176:179]
buffer_load_dwordx4 v37, s[16:19], s74 offen offset:1024 lds
v_mfma_f32_16x16x32_bf16 v[196:199], v[120:123], a[32:35], v[196:199]
s_waitcnt lgkmcnt(4)
ds_read_b128 v[144:147], v64 offset:44032
v_mfma_f32_16x16x32_bf16 v[164:167], a[28:31], v[124:127], v[164:167]
s_cbranch_vccnz .LS_300_%=
.LB_301_%=:
v_mfma_f32_16x16x32_bf16 v[180:183], a[24:27], v[124:127], v[180:183]
ds_read_b128 v[148:151], v64 offset:45056
s_waitcnt vmcnt(10)
ds_read_b128 v[96:99], v73 offset:15360
v_mfma_f32_16x16x32_bf16 v[200:203], v[124:127], a[32:35], v[200:203]
s_waitcnt lgkmcnt(6)
buffer_load_dwordx4 v38, s[16:19], s74 offen offset:2048 lds
v_mfma_f32_16x16x32_bf16 v[168:171], a[28:31], v[128:131], v[168:171]
ds_read_b128 v[152:155], v64 offset:46080
ds_read_b128 v[100:103], v73 offset:16384
v_mfma_f32_16x16x32_bf16 v[184:187], a[24:27], v[128:131], v[184:187]
ds_read_b128 v[104:107], v73 offset:17408
ds_read_b128 v[108:111], v73 offset:18432
v_mfma_f32_16x16x32_bf16 v[204:207], v[128:131], a[32:35], v[204:207]
s_waitcnt lgkmcnt(9)
ds_read_b128 v[112:115], v73 offset:19456
v_add_f32_dpp v216, v216, v216 row_ror:2 row_mask:0xf bank_mask:0xf
v_mfma_f32_16x16x32_bf16 v[172:175], a[28:31], v[132:135], v[172:175]
buffer_load_dwordx4 v39, s[16:19], s74 offen offset:3072 lds
v_mfma_f32_16x16x32_bf16 v[188:191], a[24:27], v[132:135], v[188:191]
s_barrier
s_add_u32 m0, s60, 0
v_mfma_f32_16x16x32_bf16 v[208:211], v[132:135], a[32:35], v[208:211]
s_waitcnt lgkmcnt(9)
s_add_u32 s74, s79, 344064
v_add_f32_dpp v220, v220, v220 row_ror:2 row_mask:0xf bank_mask:0xf
v_mfma_f32_16x16x32_bf16 v[156:159], a[40:43], v[136:139], v[156:159]
ds_read_b128 v[116:119], v63 offset:10240
v_add_f32_dpp v217, v217, v217 row_ror:2 row_mask:0xf bank_mask:0xf
v_mfma_f32_16x16x32_bf16 v[192:195], v[136:139], a[44:47], v[192:195]
s_waitcnt lgkmcnt(9)
buffer_load_dwordx4 v40, s[24:27], s74 offen offset:0 lds
v_mfma_f32_16x16x32_bf16 v[160:163], a[40:43], v[140:143], v[160:163]
s_waitcnt lgkmcnt(1)
ds_read_b128 v[120:123], v63 offset:11264
v_max3_u32 v226, v97, v99, v101
v_mfma_f32_16x16x32_bf16 v[176:179], a[36:39], v[140:143], v[176:179]
v_max3_u32 v226, v226, v103, v105
v_max3_u32 v226, v226, v107, v109
v_mfma_f32_16x16x32_bf16 v[196:199], v[140:143], a[44:47], v[196:199]
ds_read_b128 v[124:127], v63 offset:12288
v_max3_u32 v226, v226, v111, v113
v_mfma_f32_16x16x32_bf16 v[164:167], a[40:43], v[144:147], v[164:167]
buffer_load_dwordx4 v41, s[24:27], s74 offen offset:1024 lds
v_mfma_f32_16x16x32_bf16 v[180:183], a[36:39], v[144:147], v[180:183]
ds_read_b128 v[128:131], v63 offset:13312
v_max_u32 v226, v226, v115
v_mfma_f32_16x16x32_bf16 v[200:203], v[144:147], a[44:47], v[200:203]
v_cmp_eq_u32 vcc, -1, v226
v_add_f32_dpp v221, v221, v221 row_ror:2 row_mask:0xf bank_mask:0xf
v_mfma_f32_16x16x32_bf16 v[168:171], a[40:43], v[148:151], v[168:171]
ds_read_b128 v[132:135], v63 offset:14336
v_add_f32_dpp v218, v218, v218 row_ror:2 row_mask:0xf bank_mask:0xf
v_mfma_f32_16x16x32_bf16 v[184:187], a[36:39], v[148:151], v[184:187]
buffer_load_dwordx4 v42, s[24:27], s74 offen offset:2048 lds
v_mfma_f32_16x16x32_bf16 v[204:207], v[148:151], a[44:47], v[204:207]
s_cbranch_vccnz .LS_312_%=
.LB_313_%=:
v_mfma_f32_16x16x32_bf16 v[172:175], a[40:43], v[152:155], v[172:175]
v_add_f32_dpp v222, v222, v222 row_ror:2 row_mask:0xf bank_mask:0xf
v_add_f32_dpp v219, v219, v219 row_ror:2 row_mask:0xf bank_mask:0xf
v_mfma_f32_16x16x32_bf16 v[188:191], a[36:39], v[152:155], v[188:191]
v_add_f32_dpp v223, v223, v223 row_ror:2 row_mask:0xf bank_mask:0xf
v_add_f32_dpp v216, v216, v216 row_ror:4 row_mask:0xf bank_mask:0xf
v_mfma_f32_16x16x32_bf16 v[208:211], v[152:155], a[44:47], v[208:211]
s_waitcnt lgkmcnt(4)
buffer_load_dwordx4 v43, s[24:27], s74 offen offset:3072 lds
v_mfma_f32_16x16x32_bf16 v[156:159], a[52:55], v[116:119], v[156:159]
s_add_u32 m0, s60, 32768
s_add_u32 s74, s78, 98304
ds_read_b128 v[136:139], v64 offset:10240
)ASM"
R"ASM(
v_mfma_f32_16x16x32_bf16 v[192:195], v[116:119], a[56:59], v[192:195]
s_waitcnt lgkmcnt(4)
s_barrier
v_mfma_f32_16x16x32_bf16 v[160:163], a[52:55], v[120:123], v[160:163]
ds_read_b128 v[140:143], v64 offset:11264
v_add_f32_dpp v220, v220, v220 row_ror:4 row_mask:0xf bank_mask:0xf
v_mfma_f32_16x16x32_bf16 v[176:179], a[48:51], v[120:123], v[176:179]
buffer_load_dwordx4 v48, s[20:23], s74 offen offset:0 lds
v_mfma_f32_16x16x32_bf16 v[196:199], v[120:123], a[56:59], v[196:199]
s_waitcnt lgkmcnt(4)
ds_read_b128 v[144:147], v64 offset:12288
v_add_f32_dpp v217, v217, v217 row_ror:4 row_mask:0xf bank_mask:0xf
v_mfma_f32_16x16x32_bf16 v[164:167], a[52:55], v[124:127], v[164:167]
v_add_f32_dpp v221, v221, v221 row_ror:4 row_mask:0xf bank_mask:0xf
v_add_f32_dpp v218, v218, v218 row_ror:4 row_mask:0xf bank_mask:0xf
v_mfma_f32_16x16x32_bf16 v[180:183], a[48:51], v[124:127], v[180:183]
ds_read_b128 v[148:151], v64 offset:13312
v_add_f32_dpp v222, v222, v222 row_ror:4 row_mask:0xf bank_mask:0xf
v_mfma_f32_16x16x32_bf16 v[200:203], v[124:127], a[56:59], v[200:203]
s_waitcnt lgkmcnt(5)
buffer_load_dwordx4 v49, s[20:23], s74 offen offset:1024 lds
v_mfma_f32_16x16x32_bf16 v[168:171], a[52:55], v[128:131], v[168:171]
ds_read_b128 v[152:155], v64 offset:14336
v_add_f32_dpp v219, v219, v219 row_ror:4 row_mask:0xf bank_mask:0xf
v_mfma_f32_16x16x32_bf16 v[184:187], a[48:51], v[128:131], v[184:187]
v_add_f32_dpp v223, v223, v223 row_ror:4 row_mask:0xf bank_mask:0xf
v_add_f32_dpp v216, v216, v216 row_ror:8 row_mask:0xf bank_mask:0xf
v_mfma_f32_16x16x32_bf16 v[204:207], v[128:131], a[56:59], v[204:207]
s_waitcnt lgkmcnt(5)
v_add_f32_dpp v220, v220, v220 row_ror:8 row_mask:0xf bank_mask:0xf
v_add_f32_dpp v217, v217, v217 row_ror:8 row_mask:0xf bank_mask:0xf
v_mfma_f32_16x16x32_bf16 v[172:175], a[52:55], v[132:135], v[172:175]
buffer_load_dwordx4 v50, s[20:23], s74 offen offset:2048 lds
v_mfma_f32_16x16x32_bf16 v[188:191], a[48:51], v[132:135], v[188:191]
v_add_f32_dpp v221, v221, v221 row_ror:8 row_mask:0xf bank_mask:0xf
v_add_f32_dpp v218, v218, v218 row_ror:8 row_mask:0xf bank_mask:0xf
v_mfma_f32_16x16x32_bf16 v[208:211], v[132:135], a[56:59], v[208:211]
s_waitcnt lgkmcnt(4)
v_add_f32_dpp v222, v222, v222 row_ror:8 row_mask:0xf bank_mask:0xf
v_add_f32_dpp v219, v219, v219 row_ror:8 row_mask:0xf bank_mask:0xf
v_mfma_f32_16x16x32_bf16 v[156:159], a[112:115], v[136:139], v[156:159]
ds_read_b128 v[116:119], v63 offset:47104
v_add_f32_dpp v223, v223, v223 row_ror:8 row_mask:0xf bank_mask:0xf
v_mfma_f32_16x16x32_bf16 v[192:195], v[136:139], a[116:119], v[192:195]
s_waitcnt lgkmcnt(4)
buffer_load_dwordx4 v51, s[20:23], s74 offen offset:3072 lds
v_mfma_f32_16x16x32_bf16 v[160:163], a[112:115], v[140:143], v[160:163]
ds_read_b128 v[120:123], v63 offset:48128
v_mul_f32 v216, 0xbb000000, v216
v_mfma_f32_16x16x32_bf16 v[176:179], a[60:63], v[140:143], v[176:179]
v_mul_f32 v220, 0xbb000000, v220
v_mul_f32 v218, 0xbb000000, v218
v_mfma_f32_16x16x32_bf16 v[196:199], v[140:143], a[116:119], v[196:199]
s_waitcnt lgkmcnt(4)
ds_read_b128 v[124:127], v63 offset:49152
v_mul_f32 v222, 0xbb000000, v222
v_mfma_f32_16x16x32_bf16 v[164:167], a[112:115], v[144:147], v[164:167]
v_mul_f32 v217, 0x3b000000, v217
v_mul_f32 v221, 0x3b000000, v221
v_mfma_f32_16x16x32_bf16 v[180:183], a[60:63], v[144:147], v[180:183]
ds_read_b128 v[128:131], v63 offset:50176
v_mul_f32 v219, 0x3b000000, v219
v_mfma_f32_16x16x32_bf16 v[200:203], v[144:147], a[116:119], v[200:203]
s_waitcnt lgkmcnt(5)
v_mul_f32 v223, 0x3b000000, v223
v_fma_f32 v217, -v216, v216, v217
v_mfma_f32_16x16x32_bf16 v[168:171], a[112:115], v[148:151], v[168:171]
ds_read_b128 v[132:135], v63 offset:51200
v_fma_f32 v221, -v220, v220, v221
v_mfma_f32_16x16x32_bf16 v[184:187], a[60:63], v[148:151], v[184:187]
v_fma_f32 v219, -v218, v218, v219
v_fma_f32 v223, -v222, v222, v223
v_mfma_f32_16x16x32_bf16 v[204:207], v[148:151], a[116:119], v[204:207]
s_waitcnt lgkmcnt(5)
v_max_f32 v217, 0, v217
v_max_f32 v221, 0, v221
v_mfma_f32_16x16x32_bf16 v[172:175], a[112:115], v[152:155], v[172:175]
v_max_f32 v219, 0, v219
v_max_f32 v223, 0, v223
v_mfma_f32_16x16x32_bf16 v[188:191], a[60:63], v[152:155], v[188:191]
v_add_f32 v217, 0x3727c5ac, v217
v_add_f32 v221, 0x3727c5ac, v221
v_mfma_f32_16x16x32_bf16 v[208:211], v[152:155], a[116:119], v[208:211]
s_waitcnt lgkmcnt(4)
s_waitcnt vmcnt(8)
s_barrier
v_mfma_f32_16x16x32_bf16 v[156:159], a[124:127], v[116:119], v[156:159]
ds_read_b128 v[136:139], v64 offset:47104
ds_read_b64_tr_b16 a[64:65], v30 offset:20480
v_mfma_f32_16x16x32_bf16 v[192:195], v[116:119], a[128:131], v[192:195]
s_waitcnt lgkmcnt(5)
ds_read_b64_tr_b16 a[66:67], v30 offset:20992
ds_read_b64_tr_b16 a[76:77], v31 offset:20480
v_mfma_f32_16x16x32_bf16 v[160:163], a[124:127], v[120:123], v[160:163]
ds_read_b128 v[140:143], v64 offset:48128
ds_read_b64_tr_b16 a[78:79], v31 offset:20992
v_mfma_f32_16x16x32_bf16 v[176:179], a[120:123], v[120:123], v[176:179]
ds_read_b64_tr_b16 a[88:89], v32 offset:20480
ds_read_b64_tr_b16 a[90:91], v32 offset:20992
v_mfma_f32_16x16x32_bf16 v[196:199], v[120:123], a[128:131], v[196:199]
s_waitcnt lgkmcnt(10)
ds_read_b128 v[144:147], v64 offset:49152
ds_read_b64_tr_b16 a[100:101], v33 offset:20480
v_mfma_f32_16x16x32_bf16 v[164:167], a[124:127], v[124:127], v[164:167]
ds_read_b64_tr_b16 a[102:103], v33 offset:20992
ds_read_b64_tr_b16 a[68:69], v30 offset:24576
v_mfma_f32_16x16x32_bf16 v[180:183], a[120:123], v[124:127], v[180:183]
ds_read_b128 v[148:151], v64 offset:50176
ds_read_b64_tr_b16 a[70:71], v30 offset:25088
v_mfma_f32_16x16x32_bf16 v[200:203], v[124:127], a[128:131], v[200:203]
s_waitcnt lgkmcnt(15)
ds_read_b64_tr_b16 a[80:81], v31 offset:24576
ds_read_b64_tr_b16 a[82:83], v31 offset:25088
v_mfma_f32_16x16x32_bf16 v[168:171], a[124:127], v[128:131], v[168:171]
ds_read_b128 v[152:155], v64 offset:51200
ds_read_b64_tr_b16 a[92:93], v32 offset:24576
v_mfma_f32_16x16x32_bf16 v[184:187], a[120:123], v[128:131], v[184:187]
ds_read_b64_tr_b16 a[94:95], v32 offset:25088
ds_read_b64_tr_b16 a[104:105], v33 offset:24576
v_mfma_f32_16x16x32_bf16 v[204:207], v[128:131], a[128:131], v[204:207]
s_waitcnt lgkmcnt(15)
ds_read_b64_tr_b16 a[106:107], v33 offset:25088
ds_read_b64_tr_b16 a[72:73], v30 offset:28672
v_mfma_f32_16x16x32_bf16 v[172:175], a[124:127], v[132:135], v[172:175]
ds_read_b64_tr_b16 a[74:75], v30 offset:29184
ds_read_b64_tr_b16 a[84:85], v31 offset:28672
v_mfma_f32_16x16x32_bf16 v[188:191], a[120:123], v[132:135], v[188:191]
s_barrier
ds_read_b64_tr_b16 a[86:87], v31 offset:29184
v_mfma_f32_16x16x32_bf16 v[208:211], v[132:135], a[128:131], v[208:211]
ds_read_b64_tr_b16 a[96:97], v32 offset:28672
ds_read_b64_tr_b16 a[98:99], v32 offset:29184
v_mfma_f32_16x16x32_bf16 v[156:159], a[136:139], v[136:139], v[156:159]
ds_read_b128 v[116:119], v63 offset:15360
ds_read_b64_tr_b16 a[108:109], v33 offset:28672
v_mfma_f32_16x16x32_bf16 v[192:195], v[136:139], a[140:143], v[192:195]
ds_read_b64_tr_b16 a[110:111], v33 offset:29184
v_add_f32 v219, 0x3727c5ac, v219
v_mfma_f32_16x16x32_bf16 v[160:163], a[136:139], v[140:143], v[160:163]
ds_read_b128 v[120:123], v63 offset:16384
v_add_f32 v223, 0x3727c5ac, v223
v_mfma_f32_16x16x32_bf16 v[176:179], a[132:135], v[140:143], v[176:179]
v_rsq_f32 v217, v217
v_mfma_f32_16x16x32_bf16 v[196:199], v[140:143], a[140:143], v[196:199]
s_waitcnt lgkmcnt(15)
ds_read_b128 v[124:127], v63 offset:17408
s_waitcnt vmcnt(4)
ds_read_b64_tr_b16 a[0:1], v26 offset:0
v_mfma_f32_16x16x32_bf16 v[164:167], a[136:139], v[144:147], v[164:167]
v_rsq_f32 v221, v221
v_mfma_f32_16x16x32_bf16 v[180:183], a[132:135], v[144:147], v[180:183]
s_waitcnt lgkmcnt(3)
s_barrier
ds_read_b128 v[128:131], v63 offset:18432
v_mfma_f32_16x16x32_bf16 v[200:203], v[144:147], a[140:143], v[200:203]
s_add_u32 m0, s60, 20480
s_add_u32 s74, s78, 0
v_mfma_f32_16x16x32_bf16 v[168:171], a[136:139], v[148:151], v[168:171]
ds_read_b128 v[132:135], v63 offset:19456
ds_read_b64_tr_b16 a[2:3], v26 offset:512
v_mfma_f32_16x16x32_bf16 v[184:187], a[132:135], v[148:151], v[184:187]
buffer_load_dwordx4 v48, s[20:23], s74 offen offset:0 lds
v_mfma_f32_16x16x32_bf16 v[204:207], v[148:151], a[140:143], v[204:207]
v_rsq_f32 v219, v219
v_mfma_f32_16x16x32_bf16 v[172:175], a[136:139], v[152:155], v[172:175]
v_rsq_f32 v223, v223
v_mfma_f32_16x16x32_bf16 v[188:191], a[132:135], v[152:155], v[188:191]
s_lshl_b32 s70, s54, 6
s_add_u32 s70, s70, 155648
v_lshrrev_b32 v19, 1, v71
v_mfma_f32_16x16x32_bf16 v[208:211], v[152:155], a[140:143], v[208:211]
buffer_load_dwordx4 v49, s[20:23], s74 offen offset:1024 lds
v_add_u32 v18, s70, v19
v_add_u32 v19, 155904, v19
ds_write2_b32 v18, v216, v218 offset0:0 offset1:1
ds_write2_b32 v18, v217, v219 offset0:128 offset1:129
ds_write2_b32 v19, v220, v222 offset0:0 offset1:1
ds_write2_b32 v19, v221, v223 offset0:128 offset1:129
v_mfma_f32_16x16x32_bf16 v[156:159], a[68:71], v[116:119], v[156:159]
ds_read_b128 v[136:139], v64 offset:15360
ds_read_b64_tr_b16 a[4:5], v27 offset:0
v_mfma_f32_16x16x32_bf16 v[192:195], v[116:119], a[72:75], v[192:195]
s_waitcnt lgkmcnt(11)
s_waitcnt lgkmcnt(2)
s_barrier
v_mfma_f32_16x16x32_bf16 v[160:163], a[68:71], v[120:123], v[160:163]
ds_read_b128 v[140:143], v64 offset:16384
ds_read2_b32 v[212:213], v68 offset0:0 offset1:128
v_mfma_f32_16x16x32_bf16 v[176:179], a[64:67], v[120:123], v[176:179]
buffer_load_dwordx4 v50, s[20:23], s74 offen offset:2048 lds
v_mfma_f32_16x16x32_bf16 v[196:199], v[120:123], a[72:75], v[196:199]
ds_read_b128 v[144:147], v64 offset:17408
ds_read2_b32 v[214:215], v68 offset0:16 offset1:144
v_mfma_f32_16x16x32_bf16 v[164:167], a[68:71], v[124:127], v[164:167]
ds_read2_b32 v[216:217], v68 offset0:32 offset1:160
ds_read2_b32 v[218:219], v68 offset0:48 offset1:176
v_mfma_f32_16x16x32_bf16 v[180:183], a[64:67], v[124:127], v[180:183]
ds_read_b128 v[148:151], v64 offset:18432
ds_read2_b32 v[220:221], v68 offset0:64 offset1:192
v_mfma_f32_16x16x32_bf16 v[200:203], v[124:127], a[72:75], v[200:203]
buffer_load_dwordx4 v51, s[20:23], s74 offen offset:3072 lds
v_mfma_f32_16x16x32_bf16 v[168:171], a[68:71], v[128:131], v[168:171]
s_add_u32 m0, s60, 24576
s_add_u32 s74, s78, 32768
ds_read_b128 v[152:155], v64 offset:19456
v_mfma_f32_16x16x32_bf16 v[184:187], a[64:67], v[128:131], v[184:187]
ds_read_b128 v[96:99], v69 offset:0
ds_read_b128 v[100:103], v69 offset:64
v_mfma_f32_16x16x32_bf16 v[204:207], v[128:131], a[72:75], v[204:207]
ds_read_b128 v[104:107], v69 offset:128
ds_read_b128 v[108:111], v69 offset:192
v_mfma_f32_16x16x32_bf16 v[172:175], a[68:71], v[132:135], v[172:175]
buffer_load_dwordx4 v48, s[20:23], s74 offen offset:0 lds
v_mfma_f32_16x16x32_bf16 v[188:191], a[64:67], v[132:135], v[188:191]
ds_read_b128 v[112:115], v69 offset:256
ds_read_b128 v[228:231], v69 offset:512
v_mfma_f32_16x16x32_bf16 v[208:211], v[132:135], a[72:75], v[208:211]
s_waitcnt lgkmcnt(15)
ds_read_b128 v[232:235], v69 offset:576
ds_read_b128 v[236:239], v69 offset:640
v_mfma_f32_16x16x32_bf16 v[156:159], a[80:83], v[136:139], v[156:159]
ds_read_b128 v[116:119], v63 offset:52224
ds_read_b128 v[240:243], v69 offset:704
v_mfma_f32_16x16x32_bf16 v[192:195], v[136:139], a[84:87], v[192:195]
s_waitcnt lgkmcnt(15)
buffer_load_dwordx4 v49, s[20:23], s74 offen offset:1024 lds
v_mfma_f32_16x16x32_bf16 v[160:163], a[80:83], v[140:143], v[160:163]
ds_read_b128 v[120:123], v63 offset:53248
ds_read_b128 v[244:247], v69 offset:768
v_mfma_f32_16x16x32_bf16 v[176:179], a[76:79], v[140:143], v[176:179]
ds_read_b64_tr_b16 a[6:7], v27 offset:512
ds_read_b64_tr_b16 a[8:9], v28 offset:0
v_mfma_f32_16x16x32_bf16 v[196:199], v[140:143], a[84:87], v[196:199]
ds_read_b128 v[124:127], v63 offset:54272
ds_read_b64_tr_b16 a[10:11], v28 offset:512
v_mfma_f32_16x16x32_bf16 v[164:167], a[80:83], v[144:147], v[164:167]
buffer_load_dwordx4 v50, s[20:23], s74 offen offset:2048 lds
v_mfma_f32_16x16x32_bf16 v[180:183], a[76:79], v[144:147], v[180:183]
ds_read_b128 v[128:131], v63 offset:55296
ds_read_b64_tr_b16 a[12:13], v29 offset:0
v_mfma_f32_16x16x32_bf16 v[200:203], v[144:147], a[84:87], v[200:203]
)ASM"
R"ASM(
s_waitcnt lgkmcnt(15)
ds_read_b64_tr_b16 a[14:15], v29 offset:512
v_mfma_f32_16x16x32_bf16 v[168:171], a[80:83], v[148:151], v[168:171]
ds_read_b128 v[132:135], v63 offset:56320
v_mfma_f32_16x16x32_bf16 v[184:187], a[76:79], v[148:151], v[184:187]
buffer_load_dwordx4 v51, s[20:23], s74 offen offset:3072 lds
v_mfma_f32_16x16x32_bf16 v[204:207], v[148:151], a[84:87], v[204:207]
s_add_u32 m0, s60, 28672
s_add_u32 s74, s78, 65536
v_mfma_f32_16x16x32_bf16 v[172:175], a[80:83], v[152:155], v[172:175]
v_mfma_f32_16x16x32_bf16 v[188:191], a[76:79], v[152:155], v[188:191]
v_mfma_f32_16x16x32_bf16 v[208:211], v[152:155], a[84:87], v[208:211]
s_waitcnt lgkmcnt(11)
buffer_load_dwordx4 v48, s[20:23], s74 offen offset:0 lds
v_mfma_f32_16x16x32_bf16 v[156:159], a[92:95], v[116:119], v[156:159]
ds_read_b128 v[136:139], v64 offset:52224
v_mfma_f32_16x16x32_bf16 v[192:195], v[116:119], a[96:99], v[192:195]
s_waitcnt lgkmcnt(10)
v_mfma_f32_16x16x32_bf16 v[160:163], a[92:95], v[120:123], v[160:163]
ds_read_b128 v[140:143], v64 offset:53248
v_mfma_f32_16x16x32_bf16 v[176:179], a[88:91], v[120:123], v[176:179]
buffer_load_dwordx4 v49, s[20:23], s74 offen offset:1024 lds
v_mfma_f32_16x16x32_bf16 v[196:199], v[120:123], a[96:99], v[196:199]
s_waitcnt lgkmcnt(7)
ds_read_b128 v[144:147], v64 offset:54272
v_mfma_f32_16x16x32_bf16 v[164:167], a[92:95], v[124:127], v[164:167]
v_mfma_f32_16x16x32_bf16 v[180:183], a[88:91], v[124:127], v[180:183]
ds_read_b128 v[148:151], v64 offset:55296
v_mfma_f32_16x16x32_bf16 v[200:203], v[124:127], a[96:99], v[200:203]
s_waitcnt lgkmcnt(7)
buffer_load_dwordx4 v50, s[20:23], s74 offen offset:2048 lds
v_mfma_f32_16x16x32_bf16 v[168:171], a[92:95], v[128:131], v[168:171]
ds_read_b128 v[152:155], v64 offset:56320
v_mfma_f32_16x16x32_bf16 v[184:187], a[88:91], v[128:131], v[184:187]
v_mfma_f32_16x16x32_bf16 v[204:207], v[128:131], a[96:99], v[204:207]
s_waitcnt lgkmcnt(5)
v_mfma_f32_16x16x32_bf16 v[172:175], a[92:95], v[132:135], v[172:175]
buffer_load_dwordx4 v51, s[20:23], s74 offen offset:3072 lds
v_mfma_f32_16x16x32_bf16 v[188:191], a[88:91], v[132:135], v[188:191]
v_mfma_f32_16x16x32_bf16 v[208:211], v[132:135], a[96:99], v[208:211]
s_waitcnt lgkmcnt(4)
v_mfma_f32_16x16x32_bf16 v[156:159], a[104:107], v[136:139], v[156:159]
v_mfma_f32_16x16x32_bf16 v[192:195], v[136:139], a[108:111], v[192:195]
s_waitcnt lgkmcnt(3)
v_mfma_f32_16x16x32_bf16 v[160:163], a[104:107], v[140:143], v[160:163]
v_mfma_f32_16x16x32_bf16 v[176:179], a[100:103], v[140:143], v[176:179]
v_mfma_f32_16x16x32_bf16 v[196:199], v[140:143], a[108:111], v[196:199]
s_waitcnt lgkmcnt(2)
v_mfma_f32_16x16x32_bf16 v[164:167], a[104:107], v[144:147], v[164:167]
v_mfma_f32_16x16x32_bf16 v[180:183], a[100:103], v[144:147], v[180:183]
v_mfma_f32_16x16x32_bf16 v[200:203], v[144:147], a[108:111], v[200:203]
s_waitcnt lgkmcnt(1)
v_mfma_f32_16x16x32_bf16 v[168:171], a[104:107], v[148:151], v[168:171]
v_mfma_f32_16x16x32_bf16 v[184:187], a[100:103], v[148:151], v[184:187]
v_mfma_f32_16x16x32_bf16 v[204:207], v[148:151], a[108:111], v[204:207]
s_waitcnt lgkmcnt(0)
v_mfma_f32_16x16x32_bf16 v[172:175], a[104:107], v[152:155], v[172:175]
v_mfma_f32_16x16x32_bf16 v[188:191], a[100:103], v[152:155], v[188:191]
v_mfma_f32_16x16x32_bf16 v[208:211], v[152:155], a[108:111], v[208:211]
s_mov_b32 s18, 0
s_barrier
s_add_u32 s72, s81, 32768
v_mov_b32 v20, v52
v_pk_fma_f32 v[156:157], v[212:213], v[6:7], v[156:157] op_sel_hi:[0,1,1]
v_pk_fma_f32 v[158:159], v[212:213], v[8:9], v[158:159] op_sel_hi:[0,1,1]
v_pk_fma_f32 v[160:161], v[214:215], v[6:7], v[160:161] op_sel_hi:[0,1,1]
v_pk_fma_f32 v[176:177], v[214:215], v[2:3], v[176:177] op_sel_hi:[0,1,1]
v_pk_fma_f32 v[162:163], v[214:215], v[8:9], v[162:163] op_sel_hi:[0,1,1]
v_pk_fma_f32 v[178:179], v[214:215], v[4:5], v[178:179] op_sel_hi:[0,1,1]
v_pk_fma_f32 v[164:165], v[216:217], v[6:7], v[164:165] op_sel_hi:[0,1,1]
v_pk_fma_f32 v[180:181], v[216:217], v[2:3], v[180:181] op_sel_hi:[0,1,1]
s_add_u32 m0, s60, 4096
s_add_u32 s74, s79, 458752
buffer_load_dwordx4 v40, s[24:27], s74 offen offset:0 lds
v_pk_fma_f32 v[166:167], v[216:217], v[8:9], v[166:167] op_sel_hi:[0,1,1]
v_pk_fma_f32 v[182:183], v[216:217], v[4:5], v[182:183] op_sel_hi:[0,1,1]
v_pk_fma_f32 v[168:169], v[218:219], v[6:7], v[168:169] op_sel_hi:[0,1,1]
v_pk_fma_f32 v[184:185], v[218:219], v[2:3], v[184:185] op_sel_hi:[0,1,1]
v_pk_fma_f32 v[170:171], v[218:219], v[8:9], v[170:171] op_sel_hi:[0,1,1]
v_pk_fma_f32 v[186:187], v[218:219], v[4:5], v[186:187] op_sel_hi:[0,1,1]
v_pk_fma_f32 v[172:173], v[220:221], v[6:7], v[172:173] op_sel_hi:[0,1,1]
v_pk_fma_f32 v[188:189], v[220:221], v[2:3], v[188:189] op_sel_hi:[0,1,1]
v_pk_fma_f32 v[174:175], v[220:221], v[8:9], v[174:175] op_sel_hi:[0,1,1]
v_pk_fma_f32 v[190:191], v[220:221], v[4:5], v[190:191] op_sel_hi:[0,1,1]
v_pk_fma_f32 v[192:193], v[96:97], v[20:21], v[192:193] op_sel_hi:[1,0,1]
v_pk_fma_f32 v[194:195], v[98:99], v[20:21], v[194:195] op_sel_hi:[1,0,1]
v_pk_fma_f32 v[196:197], v[100:101], v[20:21], v[196:197] op_sel_hi:[1,0,1]
v_pk_fma_f32 v[198:199], v[102:103], v[20:21], v[198:199] op_sel_hi:[1,0,1]
v_pk_fma_f32 v[200:201], v[104:105], v[20:21], v[200:201] op_sel_hi:[1,0,1]
v_pk_fma_f32 v[202:203], v[106:107], v[20:21], v[202:203] op_sel_hi:[1,0,1]
v_pk_fma_f32 v[204:205], v[108:109], v[20:21], v[204:205] op_sel_hi:[1,0,1]
v_pk_fma_f32 v[206:207], v[110:111], v[20:21], v[206:207] op_sel_hi:[1,0,1]
buffer_load_dwordx4 v41, s[24:27], s74 offen offset:1024 lds
v_pk_fma_f32 v[208:209], v[112:113], v[20:21], v[208:209] op_sel_hi:[1,0,1]
v_pk_fma_f32 v[210:211], v[114:115], v[20:21], v[210:211] op_sel_hi:[1,0,1]
v_pk_mul_f32 v[156:157], v[212:213], v[156:157] op_sel:[1,0] op_sel_hi:[1,1]
v_pk_mul_f32 v[158:159], v[212:213], v[158:159] op_sel:[1,0] op_sel_hi:[1,1]
v_pk_mul_f32 v[160:161], v[214:215], v[160:161] op_sel:[1,0] op_sel_hi:[1,1]
v_pk_mul_f32 v[162:163], v[214:215], v[162:163] op_sel:[1,0] op_sel_hi:[1,1]
v_pk_mul_f32 v[164:165], v[216:217], v[164:165] op_sel:[1,0] op_sel_hi:[1,1]
v_pk_mul_f32 v[166:167], v[216:217], v[166:167] op_sel:[1,0] op_sel_hi:[1,1]
v_pk_mul_f32 v[168:169], v[218:219], v[168:169] op_sel:[1,0] op_sel_hi:[1,1]
v_pk_mul_f32 v[170:171], v[218:219], v[170:171] op_sel:[1,0] op_sel_hi:[1,1]
v_pk_mul_f32 v[172:173], v[220:221], v[172:173] op_sel:[1,0] op_sel_hi:[1,1]
v_pk_mul_f32 v[174:175], v[220:221], v[174:175] op_sel:[1,0] op_sel_hi:[1,1]
v_pk_mul_f32 v[192:193], v[192:193], v[228:229]
v_pk_mul_f32 v[194:195], v[194:195], v[230:231]
v_pk_mul_f32 v[196:197], v[196:197], v[232:233]
v_pk_mul_f32 v[198:199], v[198:199], v[234:235]
v_pk_mul_f32 v[200:201], v[200:201], v[236:237]
v_pk_mul_f32 v[202:203], v[202:203], v[238:239]
buffer_load_dwordx4 v42, s[24:27], s74 offen offset:2048 lds
v_pk_mul_f32 v[204:205], v[204:205], v[240:241]
v_pk_mul_f32 v[206:207], v[206:207], v[242:243]
v_pk_mul_f32 v[208:209], v[208:209], v[244:245]
v_pk_mul_f32 v[210:211], v[210:211], v[246:247]
v_cvt_pk_bf16_f32 v128, v156, v157
v_cvt_pk_bf16_f32 v129, v158, v159
v_cvt_pk_bf16_f32 v138, v176, v177
v_cvt_pk_bf16_f32 v139, v178, v179
v_cvt_pk_bf16_f32 v130, v160, v161
v_cvt_pk_bf16_f32 v131, v162, v163
v_cvt_pk_bf16_f32 v140, v180, v181
v_cvt_pk_bf16_f32 v141, v182, v183
v_mfma_f32_16x16x16_bf16 v[156:159], v[128:129], v[138:139], 0
v_cvt_pk_bf16_f32 v132, v164, v165
v_cvt_pk_bf16_f32 v133, v166, v167
v_mfma_f32_16x16x16_bf16 v[176:179], v[130:131], v[138:139], 0
buffer_load_dwordx4 v43, s[24:27], s74 offen offset:3072 lds
v_cvt_pk_bf16_f32 v142, v184, v185
v_cvt_pk_bf16_f32 v143, v186, v187
v_mfma_f32_16x16x16_bf16 v[160:163], v[130:131], v[140:141], 0
v_cvt_pk_bf16_f32 v134, v168, v169
v_cvt_pk_bf16_f32 v135, v170, v171
v_mfma_f32_16x16x16_bf16 v[180:183], v[132:133], v[140:141], 0
v_cvt_pk_bf16_f32 v144, v188, v189
v_cvt_pk_bf16_f32 v145, v190, v191
v_mfma_f32_16x16x16_bf16 v[164:167], v[132:133], v[142:143], 0
v_cvt_pk_bf16_f32 v136, v172, v173
v_cvt_pk_bf16_f32 v137, v174, v175
v_mfma_f32_16x16x16_bf16 v[184:187], v[134:135], v[142:143], 0
v_cvt_pk_bf16_f32 v146, v192, v193
v_cvt_pk_bf16_f32 v147, v194, v195
s_add_u32 m0, s60, 0
s_add_u32 s74, s79, 573440
buffer_load_dwordx4 v40, s[24:27], s74 offen offset:0 lds
v_mfma_f32_16x16x16_bf16 v[168:171], v[134:135], v[144:145], 0
v_cvt_pk_bf16_f32 v148, v196, v197
v_cvt_pk_bf16_f32 v149, v198, v199
v_mfma_f32_16x16x16_bf16 v[188:191], v[136:137], v[144:145], 0
v_cvt_pk_bf16_f32 v150, v200, v201
v_cvt_pk_bf16_f32 v151, v202, v203
v_cvt_pk_bf16_f32 v152, v204, v205
v_cvt_pk_bf16_f32 v153, v206, v207
v_cvt_pk_bf16_f32 v154, v208, v209
v_cvt_pk_bf16_f32 v155, v210, v211
v_mul_f32 v248, 0x3eb8aa3b, v215
v_mul_f32 v250, 0x3eb8aa3b, v217
v_mul_f32 v252, 0x3eb8aa3b, v219
v_mul_f32 v254, 0x3eb8aa3b, v221
v_cndmask_b32 v96, v156, v157, s[64:65]
v_cndmask_b32 v100, v160, v161, s[64:65]
buffer_load_dwordx4 v41, s[24:27], s74 offen offset:1024 lds
v_cndmask_b32 v104, v164, v165, s[64:65]
v_cndmask_b32 v108, v168, v169, s[64:65]
v_cndmask_b32 v97, v158, v159, s[64:65]
v_cndmask_b32 v101, v162, v163, s[64:65]
v_cndmask_b32 v105, v166, v167, s[64:65]
v_cndmask_b32 v109, v170, v171, s[64:65]
v_cndmask_b32 v98, v176, v177, s[64:65]
v_cndmask_b32 v102, v180, v181, s[64:65]
v_cndmask_b32 v106, v184, v185, s[64:65]
v_cndmask_b32 v110, v188, v189, s[64:65]
v_cndmask_b32 v99, v178, v179, s[64:65]
v_cndmask_b32 v103, v182, v183, s[64:65]
v_cndmask_b32 v107, v186, v187, s[64:65]
v_cndmask_b32 v111, v190, v191, s[64:65]
v_pk_fma_f32 v[96:97], v[96:97], v[248:249], v[58:59] op_sel_hi:[1,0,1]
v_pk_fma_f32 v[100:101], v[100:101], v[250:251], v[54:55] op_sel_hi:[1,0,1]
v_pk_fma_f32 v[104:105], v[104:105], v[252:253], v[54:55] op_sel_hi:[1,0,1]
v_pk_fma_f32 v[108:109], v[108:109], v[254:255], v[54:55] op_sel_hi:[1,0,1]
buffer_load_dwordx4 v42, s[24:27], s74 offen offset:2048 lds
v_pk_fma_f32 v[98:99], v[98:99], v[248:249], v[56:57] op_sel_hi:[1,0,1]
v_pk_fma_f32 v[102:103], v[102:103], v[250:251], v[56:57] op_sel_hi:[1,0,1]
v_pk_fma_f32 v[106:107], v[106:107], v[252:253], v[56:57] op_sel_hi:[1,0,1]
v_pk_fma_f32 v[110:111], v[110:111], v[254:255], v[56:57] op_sel_hi:[1,0,1]
v_max3_f32 v112, v96, v97, v98
v_max3_f32 v114, v100, v101, v102
v_max3_f32 v116, v104, v105, v106
v_max3_f32 v118, v108, v109, v110
v_max_f32 v112, v112, v99
v_max_f32 v114, v114, v103
v_max_f32 v116, v116, v107
v_max_f32 v118, v118, v111
v_mov_b32 v113, v112
v_mov_b32 v115, v114
v_mov_b32 v117, v116
v_mov_b32 v119, v118
v_permlane16_swap_b32 v112, v113
v_permlane16_swap_b32 v114, v115
buffer_load_dwordx4 v43, s[24:27], s74 offen offset:3072 lds
v_permlane16_swap_b32 v116, v117
v_permlane16_swap_b32 v118, v119
v_max_f32 v112, v112, v113
v_max_f32 v114, v114, v115
v_max_f32 v116, v116, v117
v_max_f32 v118, v118, v119
v_mov_b32 v113, v112
v_mov_b32 v115, v114
v_mov_b32 v117, v116
v_mov_b32 v119, v118
v_permlane32_swap_b32 v112, v113
v_permlane32_swap_b32 v114, v115
v_permlane32_swap_b32 v116, v117
v_permlane32_swap_b32 v118, v119
v_max_f32 v112, v112, v113
s_add_u32 m0, s60, 8192
s_add_u32 s74, s79, 688128
buffer_load_dwordx4 v40, s[24:27], s74 offen offset:0 lds
v_max_f32 v114, v114, v115
v_max_f32 v116, v116, v117
v_max_f32 v118, v118, v119
v_pk_add_f32 v[96:97], v[96:97], v[112:113] op_sel_hi:[1,0] neg_lo:[0,1] neg_hi:[0,1]
v_pk_add_f32 v[100:101], v[100:101], v[114:115] op_sel_hi:[1,0] neg_lo:[0,1] neg_hi:[0,1]
v_pk_add_f32 v[104:105], v[104:105], v[116:117] op_sel_hi:[1,0] neg_lo:[0,1] neg_hi:[0,1]
v_pk_add_f32 v[108:109], v[108:109], v[118:119] op_sel_hi:[1,0] neg_lo:[0,1] neg_hi:[0,1]
v_pk_add_f32 v[98:99], v[98:99], v[112:113] op_sel_hi:[1,0] neg_lo:[0,1] neg_hi:[0,1]
v_pk_add_f32 v[102:103], v[102:103], v[114:115] op_sel_hi:[1,0] neg_lo:[0,1] neg_hi:[0,1]
v_pk_add_f32 v[106:107], v[106:107], v[116:117] op_sel_hi:[1,0] neg_lo:[0,1] neg_hi:[0,1]
v_pk_add_f32 v[110:111], v[110:111], v[118:119] op_sel_hi:[1,0] neg_lo:[0,1] neg_hi:[0,1]
v_exp_f32 v96, v96
v_exp_f32 v100, v100
v_exp_f32 v104, v104
v_exp_f32 v108, v108
v_exp_f32 v97, v97
buffer_load_dwordx4 v41, s[24:27], s74 offen offset:1024 lds
)ASM"
R"ASM(
v_exp_f32 v101, v101
v_exp_f32 v105, v105
v_exp_f32 v109, v109
v_exp_f32 v98, v98
v_exp_f32 v102, v102
v_exp_f32 v106, v106
v_exp_f32 v110, v110
v_exp_f32 v99, v99
v_exp_f32 v103, v103
v_exp_f32 v107, v107
v_exp_f32 v111, v111
v_cvt_pk_bf16_f32 v96, v96, v97
buffer_load_dwordx4 v42, s[24:27], s74 offen offset:2048 lds
v_cvt_pk_bf16_f32 v100, v100, v101
v_cvt_pk_bf16_f32 v104, v104, v105
v_cvt_pk_bf16_f32 v108, v108, v109
v_cvt_pk_bf16_f32 v98, v98, v99
v_cvt_pk_bf16_f32 v102, v102, v103
v_cvt_pk_bf16_f32 v106, v106, v107
v_cvt_pk_bf16_f32 v110, v110, v111
v_perm_b32 v192, v96, v96, v60
v_perm_b32 v193, v96, v96, v61
v_perm_b32 v194, v98, v98, v60
v_perm_b32 v195, v98, v98, v61
v_perm_b32 v196, v100, v100, v60
v_perm_b32 v197, v100, v100, v61
v_mfma_f32_16x16x32_bf16 v[156:159], v[10:13], v[192:195], 0
v_perm_b32 v198, v102, v102, v60
v_perm_b32 v199, v102, v102, v61
v_mfma_f32_16x16x32_bf16 v[176:179], v[146:149], v[192:195], 0
buffer_load_dwordx4 v43, s[24:27], s74 offen offset:3072 lds
v_perm_b32 v200, v104, v104, v60
v_perm_b32 v201, v104, v104, v61
v_mfma_f32_16x16x32_bf16 v[160:163], v[10:13], v[196:199], 0
v_perm_b32 v202, v106, v106, v60
v_perm_b32 v203, v106, v106, v61
v_mfma_f32_16x16x32_bf16 v[180:183], v[148:151], v[196:199], 0
v_perm_b32 v204, v108, v108, v60
v_perm_b32 v205, v108, v108, v61
v_mfma_f32_16x16x32_bf16 v[164:167], v[10:13], v[200:203], 0
v_perm_b32 v206, v110, v110, v60
v_perm_b32 v207, v110, v110, v61
v_mfma_f32_16x16x32_bf16 v[184:187], v[150:153], v[200:203], 0
v_rcp_f32 v156, v156
s_nop 0
v_mfma_f32_16x16x32_bf16 v[168:171], v[10:13], v[204:207], 0
v_rcp_f32 v160, v160
s_nop 0
v_mfma_f32_16x16x32_bf16 v[188:191], v[152:155], v[204:207], 0
v_pk_mul_f32 v[176:177], v[176:177], v[156:157] op_sel_hi:[1,0]
v_pk_mul_f32 v[180:181], v[180:181], v[160:161] op_sel_hi:[1,0]
v_pk_mul_f32 v[178:179], v[178:179], v[156:157] op_sel_hi:[1,0]
v_pk_mul_f32 v[182:183], v[182:183], v[160:161] op_sel_hi:[1,0]
v_cvt_pk_bf16_f32 v120, v176, v177
v_cvt_pk_bf16_f32 v122, v180, v181
v_cvt_pk_bf16_f32 v121, v178, v179
v_cvt_pk_bf16_f32 v123, v182, v183
v_min_u32 v120, -2, v120
v_min_u32 v122, -2, v122
v_min_u32 v121, -2, v121
v_min_u32 v123, -2, v123
v_rcp_f32 v164, v164
v_rcp_f32 v168, v168
v_pk_mul_f32 v[184:185], v[184:185], v[164:165] op_sel_hi:[1,0]
v_pk_mul_f32 v[188:189], v[188:189], v[168:169] op_sel_hi:[1,0]
v_pk_mul_f32 v[186:187], v[186:187], v[164:165] op_sel_hi:[1,0]
v_pk_mul_f32 v[190:191], v[190:191], v[168:169] op_sel_hi:[1,0]
v_cvt_pk_bf16_f32 v124, v184, v185
v_cvt_pk_bf16_f32 v126, v188, v189
v_cvt_pk_bf16_f32 v125, v186, v187
v_cvt_pk_bf16_f32 v127, v190, v191
v_min_u32 v124, -2, v124
v_min_u32 v126, -2, v126
v_min_u32 v125, -2, v125
v_min_u32 v127, -2, v127
v_permlane16_swap_b32 v120, v122
v_permlane16_swap_b32 v121, v123
buffer_store_dwordx4 v[120:123], v65, s[4:7], s81 offen offset:0 sc1
v_permlane16_swap_b32 v124, v126
v_permlane16_swap_b32 v125, v127
buffer_store_dwordx4 v[124:127], v65, s[4:7], s72 offen offset:0 sc1
s_waitcnt vmcnt(14)
s_barrier
s_lshl_b32 s70, s54, 5
s_add_u32 m0, s60, 12288
s_add_u32 s74, s79, 802816
buffer_load_dwordx4 v40, s[24:27], s74 offen offset:0 lds
v_xor_b32 v240, s70, v30
v_add_u32 v240, 20480, v240
v_add_u32 v244, 73728, v240
v_xor_b32 v241, 32, v240
v_xor_b32 v242, 64, v240
v_xor_b32 v243, 96, v240
v_xor_b32 v245, 32, v244
v_xor_b32 v246, 64, v244
buffer_load_dwordx4 v41, s[24:27], s74 offen offset:1024 lds
v_xor_b32 v247, 96, v244
v_xor_b32 v18, 0, v72
ds_read_b64_tr_b16 v[96:97], v18
v_xor_b32 v18, 32, v72
ds_read_b64_tr_b16 v[98:99], v18
v_xor_b32 v18, 64, v72
ds_read_b64_tr_b16 v[100:101], v18
buffer_load_dwordx4 v42, s[24:27], s74 offen offset:2048 lds
v_xor_b32 v18, 96, v72
ds_read_b64_tr_b16 v[102:103], v18
s_waitcnt lgkmcnt(3)
v_lshlrev_b32 v224, 16, v96
v_and_b32 v225, 0xffff0000, v96
v_lshlrev_b32 v226, 16, v97
v_and_b32 v227, 0xffff0000, v97
s_waitcnt lgkmcnt(2)
v_lshlrev_b32 v228, 16, v98
v_and_b32 v229, 0xffff0000, v98
buffer_load_dwordx4 v43, s[24:27], s74 offen offset:3072 lds
v_lshlrev_b32 v230, 16, v99
v_and_b32 v231, 0xffff0000, v99
s_waitcnt lgkmcnt(1)
v_lshlrev_b32 v232, 16, v100
v_and_b32 v233, 0xffff0000, v100
v_lshlrev_b32 v234, 16, v101
v_and_b32 v235, 0xffff0000, v101
s_waitcnt lgkmcnt(0)
v_lshlrev_b32 v236, 16, v102
v_and_b32 v237, 0xffff0000, v102
s_add_u32 m0, s60, 16384
s_add_u32 s74, s79, 917504
buffer_load_dwordx4 v40, s[24:27], s74 offen offset:0 lds
v_lshlrev_b32 v238, 16, v103
v_and_b32 v239, 0xffff0000, v103
buffer_load_dwordx4 v41, s[24:27], s74 offen offset:1024 lds
buffer_load_dwordx4 v42, s[24:27], s74 offen offset:2048 lds
buffer_load_dwordx4 v43, s[24:27], s74 offen offset:3072 lds
s_sleep 2
s_branch .LATT_%=
.LLAST_%=:
.LQ_129_%=:
s_add_u32 s75, s85, 0
buffer_load_dwordx4 v[160:163], v1, s[4:7], s75 offen offset:0 sc1
s_lshl_b32 s70, s54, 11
s_add_u32 s70, s70, 147456
v_lshl_add_u32 v20, v0, 3, s70
ds_read_b64_tr_b16 v[234:235], v20 offset:0
ds_read_b64_tr_b16 v[236:237], v20 offset:512
s_add_u32 s75, s85, 0
buffer_load_dwordx4 v[164:167], v1, s[4:7], s75 offen offset:1024 sc1
ds_read_b64_tr_b16 v[238:239], v20 offset:1024
ds_read_b64_tr_b16 v[240:241], v20 offset:1536
v_and_b32 v20, 15, v0
v_bfe_u32 v232, v0, 1, 2
v_lshlrev_b32 v232, 5, v232
s_add_u32 s75, s85, 4096
buffer_load_dwordx4 v[168:171], v1, s[4:7], s75 offen offset:0 sc1
v_lshl_or_b32 v232, v20, 7, v232
v_bfe_u32 v19, v0, 4, 2
v_lshl_or_b32 v232, v19, 3, v232
v_add_u32 v232, s70, v232
s_mul_i32 s70, s54, 288
s_add_u32 s70, s70, 158848
s_add_u32 s75, s85, 4096
buffer_load_dwordx4 v[172:175], v1, s[4:7], s75 offen offset:1024 sc1
v_lshl_add_u32 v20, v20, 1, s70
ds_read_u16 v242, v20
s_waitcnt lgkmcnt(4)
v_lshlrev_b32 v96, 16, v234
v_and_b32 v97, 0xffff0000, v234
v_lshlrev_b32 v98, 16, v235
v_and_b32 v99, 0xffff0000, v235
s_add_u32 s75, s85, 8192
buffer_load_dwordx4 v[176:179], v1, s[4:7], s75 offen offset:0 sc1
s_waitcnt lgkmcnt(3)
v_lshlrev_b32 v100, 16, v236
v_and_b32 v101, 0xffff0000, v236
v_lshlrev_b32 v102, 16, v237
v_and_b32 v103, 0xffff0000, v237
s_waitcnt lgkmcnt(2)
v_lshlrev_b32 v104, 16, v238
v_and_b32 v105, 0xffff0000, v238
s_add_u32 s75, s85, 8192
buffer_load_dwordx4 v[180:183], v1, s[4:7], s75 offen offset:1024 sc1
v_lshlrev_b32 v106, 16, v239
v_and_b32 v107, 0xffff0000, v239
s_waitcnt lgkmcnt(1)
v_lshlrev_b32 v108, 16, v240
v_and_b32 v109, 0xffff0000, v240
v_lshlrev_b32 v110, 16, v241
v_and_b32 v111, 0xffff0000, v241
s_add_u32 s75, s85, 12288
buffer_load_dwordx4 v[184:187], v1, s[4:7], s75 offen offset:0 sc1
s_waitcnt lgkmcnt(0)
v_lshlrev_b32 v242, 16, v242
v_pk_add_f32 v[96:97], v[96:97], v[242:243] op_sel_hi:[1,0]
v_pk_add_f32 v[98:99], v[98:99], v[242:243] op_sel_hi:[1,0]
v_pk_add_f32 v[100:101], v[100:101], v[242:243] op_sel_hi:[1,0]
v_pk_add_f32 v[102:103], v[102:103], v[242:243] op_sel_hi:[1,0]
v_pk_add_f32 v[104:105], v[104:105], v[242:243] op_sel_hi:[1,0]
s_add_u32 s75, s85, 12288
buffer_load_dwordx4 v[188:191], v1, s[4:7], s75 offen offset:1024 sc1
v_pk_add_f32 v[106:107], v[106:107], v[242:243] op_sel_hi:[1,0]
v_pk_add_f32 v[108:109], v[108:109], v[242:243] op_sel_hi:[1,0]
v_pk_add_f32 v[110:111], v[110:111], v[242:243] op_sel_hi:[1,0]
s_add_u32 s75, s85, 16384
buffer_load_dwordx4 v[192:195], v1, s[4:7], s75 offen offset:0 sc1
s_add_u32 s75, s85, 16384
buffer_load_dwordx4 v[196:199], v1, s[4:7], s75 offen offset:1024 sc1
s_add_u32 s75, s85, 20480
buffer_load_dwordx4 v[200:203], v1, s[4:7], s75 offen offset:0 sc1
s_add_u32 s75, s85, 20480
buffer_load_dwordx4 v[204:207], v1, s[4:7], s75 offen offset:1024 sc1
s_add_u32 s75, s85, 24576
buffer_load_dwordx4 v[208:211], v1, s[4:7], s75 offen offset:0 sc1
s_add_u32 s75, s85, 24576
buffer_load_dwordx4 v[212:215], v1, s[4:7], s75 offen offset:1024 sc1
s_waitcnt vmcnt(12)
v_max3_u32 v18, v161, v163, v165
v_max_u32 v18, v18, v167
v_cmp_eq_u32 vcc, -1, v18
s_cbranch_vccnz .LS_216_%=
.LB_217_%=:
v_mfma_f32_16x16x32_bf16 v[96:99], v[160:163], a[0:3], v[96:99]
s_add_u32 s75, s85, 28672
ds_read_b64_tr_b16 a[128:129], v26 offset:20480
ds_read_b64_tr_b16 a[130:131], v26 offset:20992
v_mfma_f32_16x16x32_bf16 v[112:115], v[160:163], a[4:7], 0
ds_read_b64_tr_b16 a[132:133], v27 offset:20480
ds_read_b64_tr_b16 a[134:135], v27 offset:20992
v_mfma_f32_16x16x32_bf16 v[128:131], v[160:163], a[8:11], 0
s_waitcnt vmcnt(10)
v_max3_u32 v18, v169, v171, v173
v_max_u32 v18, v18, v175
v_mfma_f32_16x16x32_bf16 v[144:147], v[160:163], a[12:15], 0
ds_read_b64_tr_b16 a[136:137], v28 offset:20480
v_cmp_eq_u32 vcc, -1, v18
v_mfma_f32_16x16x32_bf16 v[100:103], v[164:167], a[0:3], v[100:103]
buffer_load_dwordx4 v[160:163], v1, s[4:7], s75 offen offset:0 sc1
v_mfma_f32_16x16x32_bf16 v[116:119], v[164:167], a[4:7], 0
s_cbranch_vccnz .LS_219_%=
.LB_220_%=:
v_mfma_f32_16x16x32_bf16 v[132:135], v[164:167], a[8:11], 0
s_add_u32 s75, s85, 32768
ds_read_b64_tr_b16 a[138:139], v28 offset:20992
ds_read_b64_tr_b16 a[140:141], v29 offset:20480
v_mfma_f32_16x16x32_bf16 v[148:151], v[164:167], a[12:15], 0
buffer_load_dwordx4 v[164:167], v1, s[4:7], s75 offen offset:0 sc1
v_mfma_f32_16x16x32_bf16 v[96:99], v[168:171], a[16:19], v[96:99]
s_add_u32 s75, s85, 0
ds_read_b64_tr_b16 a[142:143], v29 offset:20992
v_mfma_f32_16x16x32_bf16 v[112:115], v[168:171], a[20:23], v[112:115]
v_mfma_f32_16x16x32_bf16 v[128:131], v[168:171], a[24:27], v[128:131]
s_waitcnt vmcnt(10)
v_max3_u32 v18, v177, v179, v181
v_max_u32 v18, v18, v183
v_mfma_f32_16x16x32_bf16 v[144:147], v[168:171], a[28:31], v[144:147]
v_cmp_eq_u32 vcc, -1, v18
v_mfma_f32_16x16x32_bf16 v[100:103], v[172:175], a[16:19], v[100:103]
buffer_load_dwordx4 v[168:171], v1, s[4:7], s75 offen offset:2048 sc1
v_mfma_f32_16x16x32_bf16 v[116:119], v[172:175], a[20:23], v[116:119]
s_cbranch_vccnz .LS_222_%=
.LB_223_%=:
v_mfma_f32_16x16x32_bf16 v[132:135], v[172:175], a[24:27], v[132:135]
s_add_u32 s75, s85, 0
v_mfma_f32_16x16x32_bf16 v[148:151], v[172:175], a[28:31], v[148:151]
buffer_load_dwordx4 v[172:175], v1, s[4:7], s75 offen offset:3072 sc1
v_mfma_f32_16x16x32_bf16 v[96:99], v[176:179], a[32:35], v[96:99]
s_add_u32 s75, s85, 4096
v_mfma_f32_16x16x32_bf16 v[112:115], v[176:179], a[36:39], v[112:115]
v_mfma_f32_16x16x32_bf16 v[128:131], v[176:179], a[40:43], v[128:131]
s_waitcnt vmcnt(10)
v_max3_u32 v18, v185, v187, v189
v_max_u32 v18, v18, v191
v_mfma_f32_16x16x32_bf16 v[144:147], v[176:179], a[44:47], v[144:147]
v_cmp_eq_u32 vcc, -1, v18
v_mfma_f32_16x16x32_bf16 v[100:103], v[180:183], a[32:35], v[100:103]
buffer_load_dwordx4 v[176:179], v1, s[4:7], s75 offen offset:2048 sc1
v_mfma_f32_16x16x32_bf16 v[116:119], v[180:183], a[36:39], v[116:119]
s_cbranch_vccnz .LS_225_%=
.LB_226_%=:
v_mfma_f32_16x16x32_bf16 v[132:135], v[180:183], a[40:43], v[132:135]
s_add_u32 s75, s85, 4096
v_mfma_f32_16x16x32_bf16 v[148:151], v[180:183], a[44:47], v[148:151]
buffer_load_dwordx4 v[180:183], v1, s[4:7], s75 offen offset:3072 sc1
v_mfma_f32_16x16x32_bf16 v[96:99], v[184:187], a[48:51], v[96:99]
s_add_u32 s75, s85, 8192
v_mfma_f32_16x16x32_bf16 v[112:115], v[184:187], a[52:55], v[112:115]
v_mfma_f32_16x16x32_bf16 v[128:131], v[184:187], a[56:59], v[128:131]
s_waitcnt vmcnt(10)
v_max3_u32 v18, v193, v195, v197
v_max_u32 v18, v18, v199
v_mfma_f32_16x16x32_bf16 v[144:147], v[184:187], a[60:63], v[144:147]
v_cmp_eq_u32 vcc, -1, v18
v_mfma_f32_16x16x32_bf16 v[100:103], v[188:191], a[48:51], v[100:103]
buffer_load_dwordx4 v[184:187], v1, s[4:7], s75 offen offset:2048 sc1
v_mfma_f32_16x16x32_bf16 v[116:119], v[188:191], a[52:55], v[116:119]
s_cbranch_vccnz .LS_228_%=
.LB_229_%=:
v_mfma_f32_16x16x32_bf16 v[132:135], v[188:191], a[56:59], v[132:135]
s_add_u32 s75, s85, 8192
v_mfma_f32_16x16x32_bf16 v[148:151], v[188:191], a[60:63], v[148:151]
buffer_load_dwordx4 v[188:191], v1, s[4:7], s75 offen offset:3072 sc1
v_mfma_f32_16x16x32_bf16 v[96:99], v[192:195], a[64:67], v[96:99]
s_add_u32 s75, s85, 12288
v_mfma_f32_16x16x32_bf16 v[112:115], v[192:195], a[68:71], v[112:115]
v_mfma_f32_16x16x32_bf16 v[128:131], v[192:195], a[72:75], v[128:131]
s_waitcnt vmcnt(10)
v_max3_u32 v18, v201, v203, v205
)ASM"
R"ASM(
v_max_u32 v18, v18, v207
v_mfma_f32_16x16x32_bf16 v[144:147], v[192:195], a[76:79], v[144:147]
v_cmp_eq_u32 vcc, -1, v18
v_mfma_f32_16x16x32_bf16 v[100:103], v[196:199], a[64:67], v[100:103]
buffer_load_dwordx4 v[192:195], v1, s[4:7], s75 offen offset:2048 sc1
v_mfma_f32_16x16x32_bf16 v[116:119], v[196:199], a[68:71], v[116:119]
s_cbranch_vccnz .LS_231_%=
.LB_232_%=:
v_mfma_f32_16x16x32_bf16 v[132:135], v[196:199], a[72:75], v[132:135]
s_add_u32 s75, s85, 12288
v_mfma_f32_16x16x32_bf16 v[148:151], v[196:199], a[76:79], v[148:151]
buffer_load_dwordx4 v[196:199], v1, s[4:7], s75 offen offset:3072 sc1
v_mfma_f32_16x16x32_bf16 v[96:99], v[200:203], a[80:83], v[96:99]
s_add_u32 s75, s85, 16384
v_mfma_f32_16x16x32_bf16 v[112:115], v[200:203], a[84:87], v[112:115]
v_mfma_f32_16x16x32_bf16 v[128:131], v[200:203], a[88:91], v[128:131]
s_waitcnt vmcnt(10)
v_max3_u32 v18, v209, v211, v213
v_max_u32 v18, v18, v215
v_mfma_f32_16x16x32_bf16 v[144:147], v[200:203], a[92:95], v[144:147]
v_cmp_eq_u32 vcc, -1, v18
v_mfma_f32_16x16x32_bf16 v[100:103], v[204:207], a[80:83], v[100:103]
buffer_load_dwordx4 v[200:203], v1, s[4:7], s75 offen offset:2048 sc1
v_mfma_f32_16x16x32_bf16 v[116:119], v[204:207], a[84:87], v[116:119]
s_cbranch_vccnz .LS_234_%=
.LB_235_%=:
v_mfma_f32_16x16x32_bf16 v[132:135], v[204:207], a[88:91], v[132:135]
s_add_u32 s75, s85, 16384
v_mfma_f32_16x16x32_bf16 v[148:151], v[204:207], a[92:95], v[148:151]
buffer_load_dwordx4 v[204:207], v1, s[4:7], s75 offen offset:3072 sc1
v_mfma_f32_16x16x32_bf16 v[96:99], v[208:211], a[96:99], v[96:99]
s_add_u32 s75, s85, 20480
v_mfma_f32_16x16x32_bf16 v[112:115], v[208:211], a[100:103], v[112:115]
v_mfma_f32_16x16x32_bf16 v[128:131], v[208:211], a[104:107], v[128:131]
s_waitcnt vmcnt(10)
v_max3_u32 v18, v161, v163, v165
v_max_u32 v18, v18, v167
v_mfma_f32_16x16x32_bf16 v[144:147], v[208:211], a[108:111], v[144:147]
v_cmp_eq_u32 vcc, -1, v18
v_mfma_f32_16x16x32_bf16 v[100:103], v[212:215], a[96:99], v[100:103]
buffer_load_dwordx4 v[208:211], v1, s[4:7], s75 offen offset:2048 sc1
v_mfma_f32_16x16x32_bf16 v[116:119], v[212:215], a[100:103], v[116:119]
s_cbranch_vccnz .LS_237_%=
.LB_238_%=:
v_mfma_f32_16x16x32_bf16 v[132:135], v[212:215], a[104:107], v[132:135]
s_add_u32 s75, s85, 20480
v_mfma_f32_16x16x32_bf16 v[148:151], v[212:215], a[108:111], v[148:151]
buffer_load_dwordx4 v[212:215], v1, s[4:7], s75 offen offset:3072 sc1
v_mfma_f32_16x16x32_bf16 v[96:99], v[160:163], a[112:115], v[96:99]
s_add_u32 s75, s85, 24576
v_mfma_f32_16x16x32_bf16 v[112:115], v[160:163], a[116:119], v[112:115]
v_mfma_f32_16x16x32_bf16 v[128:131], v[160:163], a[120:123], v[128:131]
s_waitcnt vmcnt(10)
v_max3_u32 v18, v169, v171, v173
v_max_u32 v18, v18, v175
v_mfma_f32_16x16x32_bf16 v[144:147], v[160:163], a[124:127], v[144:147]
v_cmp_eq_u32 vcc, -1, v18
s_waitcnt lgkmcnt(6)
v_mfma_f32_16x16x32_bf16 v[96:99], v[164:167], a[128:131], v[96:99]
buffer_load_dwordx4 v[160:163], v1, s[4:7], s75 offen offset:2048 sc1
s_waitcnt lgkmcnt(4)
v_mfma_f32_16x16x32_bf16 v[112:115], v[164:167], a[132:135], v[112:115]
s_cbranch_vccnz .LS_240_%=
.LB_241_%=:
s_waitcnt lgkmcnt(2)
v_mfma_f32_16x16x32_bf16 v[128:131], v[164:167], a[136:139], v[128:131]
s_add_u32 s75, s85, 24576
s_waitcnt lgkmcnt(0)
v_mfma_f32_16x16x32_bf16 v[144:147], v[164:167], a[140:143], v[144:147]
buffer_load_dwordx4 v[164:167], v1, s[4:7], s75 offen offset:3072 sc1
v_mfma_f32_16x16x32_bf16 v[104:107], v[168:171], a[0:3], v[104:107]
s_add_u32 s75, s85, 36864
v_mfma_f32_16x16x32_bf16 v[120:123], v[168:171], a[4:7], 0
v_mfma_f32_16x16x32_bf16 v[136:139], v[168:171], a[8:11], 0
s_waitcnt vmcnt(10)
v_max3_u32 v18, v177, v179, v181
v_max_u32 v18, v18, v183
v_mfma_f32_16x16x32_bf16 v[152:155], v[168:171], a[12:15], 0
v_cmp_eq_u32 vcc, -1, v18
v_mfma_f32_16x16x32_bf16 v[108:111], v[172:175], a[0:3], v[108:111]
buffer_load_dwordx4 v[168:171], v1, s[4:7], s75 offen offset:0 sc1
v_mfma_f32_16x16x32_bf16 v[124:127], v[172:175], a[4:7], 0
s_cbranch_vccnz .LS_243_%=
.LB_244_%=:
v_mfma_f32_16x16x32_bf16 v[140:143], v[172:175], a[8:11], 0
s_add_u32 s75, s85, 40960
v_mfma_f32_16x16x32_bf16 v[156:159], v[172:175], a[12:15], 0
buffer_load_dwordx4 v[172:175], v1, s[4:7], s75 offen offset:0 sc1
v_mfma_f32_16x16x32_bf16 v[104:107], v[176:179], a[16:19], v[104:107]
s_add_u32 s75, s85, 45056
ds_read_b64_tr_b16 a[0:1], v26 offset:16384
ds_read_b64_tr_b16 a[2:3], v26 offset:16896
v_mfma_f32_16x16x32_bf16 v[120:123], v[176:179], a[20:23], v[120:123]
ds_read_b64_tr_b16 a[4:5], v27 offset:16384
ds_read_b64_tr_b16 a[6:7], v27 offset:16896
v_mfma_f32_16x16x32_bf16 v[136:139], v[176:179], a[24:27], v[136:139]
s_waitcnt vmcnt(10)
v_max3_u32 v18, v185, v187, v189
v_max_u32 v18, v18, v191
v_mfma_f32_16x16x32_bf16 v[152:155], v[176:179], a[28:31], v[152:155]
ds_read_b64_tr_b16 a[8:9], v28 offset:16384
v_cmp_eq_u32 vcc, -1, v18
v_mfma_f32_16x16x32_bf16 v[108:111], v[180:183], a[16:19], v[108:111]
buffer_load_dwordx4 v[176:179], v1, s[4:7], s75 offen offset:0 sc1
v_mfma_f32_16x16x32_bf16 v[124:127], v[180:183], a[20:23], v[124:127]
s_cbranch_vccnz .LS_246_%=
.LB_247_%=:
v_mfma_f32_16x16x32_bf16 v[140:143], v[180:183], a[24:27], v[140:143]
s_add_u32 s75, s85, 49152
ds_read_b64_tr_b16 a[10:11], v28 offset:16896
ds_read_b64_tr_b16 a[12:13], v29 offset:16384
v_mfma_f32_16x16x32_bf16 v[156:159], v[180:183], a[28:31], v[156:159]
buffer_load_dwordx4 v[180:183], v1, s[4:7], s75 offen offset:0 sc1
v_mfma_f32_16x16x32_bf16 v[104:107], v[184:187], a[32:35], v[104:107]
s_add_u32 s75, s85, 53248
ds_read_b64_tr_b16 a[14:15], v29 offset:16896
ds_read_b64_tr_b16 a[16:17], v26 offset:0
v_mfma_f32_16x16x32_bf16 v[120:123], v[184:187], a[36:39], v[120:123]
ds_read_b64_tr_b16 a[18:19], v26 offset:512
ds_read_b64_tr_b16 a[20:21], v27 offset:0
v_mfma_f32_16x16x32_bf16 v[136:139], v[184:187], a[40:43], v[136:139]
s_waitcnt vmcnt(10)
v_max3_u32 v18, v193, v195, v197
v_max_u32 v18, v18, v199
v_mfma_f32_16x16x32_bf16 v[152:155], v[184:187], a[44:47], v[152:155]
ds_read_b64_tr_b16 a[22:23], v27 offset:512
v_cmp_eq_u32 vcc, -1, v18
v_mfma_f32_16x16x32_bf16 v[108:111], v[188:191], a[32:35], v[108:111]
buffer_load_dwordx4 v[184:187], v1, s[4:7], s75 offen offset:0 sc1
v_mfma_f32_16x16x32_bf16 v[124:127], v[188:191], a[36:39], v[124:127]
s_cbranch_vccnz .LS_249_%=
.LB_250_%=:
v_mfma_f32_16x16x32_bf16 v[140:143], v[188:191], a[40:43], v[140:143]
s_add_u32 s75, s85, 28672
ds_read_b64_tr_b16 a[24:25], v28 offset:0
ds_read_b64_tr_b16 a[26:27], v28 offset:512
v_mfma_f32_16x16x32_bf16 v[156:159], v[188:191], a[44:47], v[156:159]
buffer_load_dwordx4 v[188:191], v1, s[4:7], s75 offen offset:1024 sc1
v_mfma_f32_16x16x32_bf16 v[104:107], v[192:195], a[48:51], v[104:107]
s_add_u32 s75, s85, 32768
ds_read_b64_tr_b16 a[28:29], v29 offset:0
ds_read_b64_tr_b16 a[30:31], v29 offset:512
v_mfma_f32_16x16x32_bf16 v[120:123], v[192:195], a[52:55], v[120:123]
ds_read_b64_tr_b16 a[32:33], v26 offset:4096
ds_read_b64_tr_b16 a[34:35], v26 offset:4608
v_mfma_f32_16x16x32_bf16 v[136:139], v[192:195], a[56:59], v[136:139]
s_waitcnt vmcnt(10)
v_max3_u32 v18, v201, v203, v205
v_max_u32 v18, v18, v207
v_mfma_f32_16x16x32_bf16 v[152:155], v[192:195], a[60:63], v[152:155]
ds_read_b64_tr_b16 a[36:37], v27 offset:4096
v_cmp_eq_u32 vcc, -1, v18
v_mfma_f32_16x16x32_bf16 v[108:111], v[196:199], a[48:51], v[108:111]
buffer_load_dwordx4 v[192:195], v1, s[4:7], s75 offen offset:1024 sc1
v_mfma_f32_16x16x32_bf16 v[124:127], v[196:199], a[52:55], v[124:127]
s_cbranch_vccnz .LS_252_%=
.LB_253_%=:
v_mfma_f32_16x16x32_bf16 v[140:143], v[196:199], a[56:59], v[140:143]
s_add_u32 s75, s85, 36864
ds_read_b64_tr_b16 a[38:39], v27 offset:4608
ds_read_b64_tr_b16 a[40:41], v28 offset:4096
v_mfma_f32_16x16x32_bf16 v[156:159], v[196:199], a[60:63], v[156:159]
buffer_load_dwordx4 v[196:199], v1, s[4:7], s75 offen offset:1024 sc1
v_mfma_f32_16x16x32_bf16 v[104:107], v[200:203], a[64:67], v[104:107]
s_add_u32 s75, s85, 40960
ds_read_b64_tr_b16 a[42:43], v28 offset:4608
ds_read_b64_tr_b16 a[44:45], v29 offset:4096
v_mfma_f32_16x16x32_bf16 v[120:123], v[200:203], a[68:71], v[120:123]
ds_read_b64_tr_b16 a[46:47], v29 offset:4608
ds_read_b64_tr_b16 a[48:49], v26 offset:8192
v_mfma_f32_16x16x32_bf16 v[136:139], v[200:203], a[72:75], v[136:139]
s_waitcnt vmcnt(10)
v_max3_u32 v18, v209, v211, v213
v_max_u32 v18, v18, v215
v_mfma_f32_16x16x32_bf16 v[152:155], v[200:203], a[76:79], v[152:155]
ds_read_b64_tr_b16 a[50:51], v26 offset:8704
v_cmp_eq_u32 vcc, -1, v18
v_mfma_f32_16x16x32_bf16 v[108:111], v[204:207], a[64:67], v[108:111]
buffer_load_dwordx4 v[200:203], v1, s[4:7], s75 offen offset:1024 sc1
v_mfma_f32_16x16x32_bf16 v[124:127], v[204:207], a[68:71], v[124:127]
s_cbranch_vccnz .LS_255_%=
.LB_256_%=:
v_mfma_f32_16x16x32_bf16 v[140:143], v[204:207], a[72:75], v[140:143]
s_add_u32 s75, s85, 45056
ds_read_b64_tr_b16 a[52:53], v27 offset:8192
ds_read_b64_tr_b16 a[54:55], v27 offset:8704
v_mfma_f32_16x16x32_bf16 v[156:159], v[204:207], a[76:79], v[156:159]
buffer_load_dwordx4 v[204:207], v1, s[4:7], s75 offen offset:1024 sc1
v_mfma_f32_16x16x32_bf16 v[104:107], v[208:211], a[80:83], v[104:107]
s_add_u32 s75, s85, 49152
ds_read_b64_tr_b16 a[56:57], v28 offset:8192
ds_read_b64_tr_b16 a[58:59], v28 offset:8704
v_mfma_f32_16x16x32_bf16 v[120:123], v[208:211], a[84:87], v[120:123]
ds_read_b64_tr_b16 a[60:61], v29 offset:8192
ds_read_b64_tr_b16 a[62:63], v29 offset:8704
v_mfma_f32_16x16x32_bf16 v[136:139], v[208:211], a[88:91], v[136:139]
s_waitcnt vmcnt(10)
v_max3_u32 v18, v161, v163, v165
v_max_u32 v18, v18, v167
v_mfma_f32_16x16x32_bf16 v[152:155], v[208:211], a[92:95], v[152:155]
s_barrier
v_cmp_eq_u32 vcc, -1, v18
v_mfma_f32_16x16x32_bf16 v[108:111], v[212:215], a[80:83], v[108:111]
buffer_load_dwordx4 v[208:211], v1, s[4:7], s75 offen offset:1024 sc1
v_mfma_f32_16x16x32_bf16 v[124:127], v[212:215], a[84:87], v[124:127]
s_cbranch_vccnz .LS_258_%=
.LB_259_%=:
v_mfma_f32_16x16x32_bf16 v[140:143], v[212:215], a[88:91], v[140:143]
s_add_u32 s75, s85, 53248
v_mfma_f32_16x16x32_bf16 v[156:159], v[212:215], a[92:95], v[156:159]
buffer_load_dwordx4 v[212:215], v1, s[4:7], s75 offen offset:1024 sc1
v_mfma_f32_16x16x32_bf16 v[104:107], v[160:163], a[96:99], v[104:107]
s_add_u32 s75, s85, 28672
v_mfma_f32_16x16x32_bf16 v[120:123], v[160:163], a[100:103], v[120:123]
v_mfma_f32_16x16x32_bf16 v[136:139], v[160:163], a[104:107], v[136:139]
s_waitcnt vmcnt(10)
v_max3_u32 v18, v169, v171, v173
v_max_u32 v18, v18, v175
v_mfma_f32_16x16x32_bf16 v[152:155], v[160:163], a[108:111], v[152:155]
v_cmp_eq_u32 vcc, -1, v18
v_mfma_f32_16x16x32_bf16 v[108:111], v[164:167], a[96:99], v[108:111]
buffer_load_dwordx4 v[160:163], v1, s[4:7], s75 offen offset:2048 sc1
v_mfma_f32_16x16x32_bf16 v[124:127], v[164:167], a[100:103], v[124:127]
s_cbranch_vccnz .LS_261_%=
.LB_262_%=:
v_mfma_f32_16x16x32_bf16 v[140:143], v[164:167], a[104:107], v[140:143]
s_add_u32 s75, s85, 32768
v_mfma_f32_16x16x32_bf16 v[156:159], v[164:167], a[108:111], v[156:159]
buffer_load_dwordx4 v[164:167], v1, s[4:7], s75 offen offset:2048 sc1
s_waitcnt lgkmcnt(15)
v_mfma_f32_16x16x32_bf16 v[96:99], v[168:171], a[0:3], v[96:99]
s_add_u32 s75, s85, 36864
v_mfma_f32_16x16x32_bf16 v[112:115], v[168:171], a[4:7], v[112:115]
v_mfma_f32_16x16x32_bf16 v[128:131], v[168:171], a[8:11], v[128:131]
s_waitcnt vmcnt(10)
v_max3_u32 v18, v177, v179, v181
v_max_u32 v18, v18, v183
v_mfma_f32_16x16x32_bf16 v[144:147], v[168:171], a[12:15], v[144:147]
v_cmp_eq_u32 vcc, -1, v18
v_mfma_f32_16x16x32_bf16 v[96:99], v[172:175], a[16:19], v[96:99]
buffer_load_dwordx4 v[168:171], v1, s[4:7], s75 offen offset:2048 sc1
v_mfma_f32_16x16x32_bf16 v[112:115], v[172:175], a[20:23], v[112:115]
s_cbranch_vccnz .LS_264_%=
.LB_265_%=:
v_mfma_f32_16x16x32_bf16 v[128:131], v[172:175], a[24:27], v[128:131]
s_add_u32 s75, s85, 40960
v_mfma_f32_16x16x32_bf16 v[144:147], v[172:175], a[28:31], v[144:147]
buffer_load_dwordx4 v[172:175], v1, s[4:7], s75 offen offset:2048 sc1
s_waitcnt lgkmcnt(14)
v_mfma_f32_16x16x32_bf16 v[96:99], v[176:179], a[32:35], v[96:99]
)ASM"
R"ASM(
s_add_u32 s75, s85, 45056
s_waitcnt lgkmcnt(12)
v_mfma_f32_16x16x32_bf16 v[112:115], v[176:179], a[36:39], v[112:115]
s_waitcnt lgkmcnt(10)
v_mfma_f32_16x16x32_bf16 v[128:131], v[176:179], a[40:43], v[128:131]
s_waitcnt vmcnt(10)
v_max3_u32 v18, v185, v187, v189
v_max_u32 v18, v18, v191
s_waitcnt lgkmcnt(8)
v_mfma_f32_16x16x32_bf16 v[144:147], v[176:179], a[44:47], v[144:147]
v_cmp_eq_u32 vcc, -1, v18
s_waitcnt lgkmcnt(6)
v_mfma_f32_16x16x32_bf16 v[96:99], v[180:183], a[48:51], v[96:99]
buffer_load_dwordx4 v[176:179], v1, s[4:7], s75 offen offset:2048 sc1
s_waitcnt lgkmcnt(4)
v_mfma_f32_16x16x32_bf16 v[112:115], v[180:183], a[52:55], v[112:115]
s_cbranch_vccnz .LS_267_%=
.LB_268_%=:
s_waitcnt lgkmcnt(2)
v_mfma_f32_16x16x32_bf16 v[128:131], v[180:183], a[56:59], v[128:131]
s_add_u32 s75, s85, 49152
s_waitcnt lgkmcnt(0)
v_mfma_f32_16x16x32_bf16 v[144:147], v[180:183], a[60:63], v[144:147]
buffer_load_dwordx4 v[180:183], v1, s[4:7], s75 offen offset:2048 sc1
v_mfma_f32_16x16x32_bf16 v[96:99], v[184:187], v[80:83], v[96:99]
s_add_u32 s75, s85, 53248
v_mfma_f32_16x16x32_bf16 v[112:115], v[184:187], v[84:87], v[112:115]
v_mfma_f32_16x16x32_bf16 v[128:131], v[184:187], v[88:91], v[128:131]
s_waitcnt vmcnt(10)
v_max3_u32 v18, v193, v195, v197
v_max_u32 v18, v18, v199
v_mfma_f32_16x16x32_bf16 v[144:147], v[184:187], v[92:95], v[144:147]
v_cmp_eq_u32 vcc, -1, v18
v_mfma_f32_16x16x32_bf16 v[100:103], v[188:191], a[112:115], v[100:103]
buffer_load_dwordx4 v[184:187], v1, s[4:7], s75 offen offset:2048 sc1
s_add_u32 s75, s85, 28672
v_mfma_f32_16x16x32_bf16 v[116:119], v[188:191], a[116:119], v[116:119]
s_cbranch_vccnz .LS_270_%=
.LB_271_%=:
v_mfma_f32_16x16x32_bf16 v[132:135], v[188:191], a[120:123], v[132:135]
ds_write_b128 v74, v[112:115] offset:24576
v_mfma_f32_16x16x32_bf16 v[148:151], v[188:191], a[124:127], v[148:151]
buffer_load_dwordx4 v[188:191], v1, s[4:7], s75 offen offset:3072 sc1
v_mfma_f32_16x16x32_bf16 v[100:103], v[192:195], a[128:131], v[100:103]
s_add_u32 s75, s85, 32768
v_mfma_f32_16x16x32_bf16 v[116:119], v[192:195], a[132:135], v[116:119]
ds_write_b128 v75, v[128:131] offset:28672
v_mfma_f32_16x16x32_bf16 v[132:135], v[192:195], a[136:139], v[132:135]
s_waitcnt vmcnt(10)
v_max3_u32 v18, v201, v203, v205
v_max_u32 v18, v18, v207
v_cmp_eq_u32 vcc, -1, v18
v_mfma_f32_16x16x32_bf16 v[148:151], v[192:195], a[140:143], v[148:151]
buffer_load_dwordx4 v[192:195], v1, s[4:7], s75 offen offset:3072 sc1
v_mfma_f32_16x16x32_bf16 v[100:103], v[196:199], a[0:3], v[100:103]
s_cbranch_vccnz .LS_273_%=
.LB_274_%=:
s_add_u32 s75, s85, 36864
v_mfma_f32_16x16x32_bf16 v[116:119], v[196:199], a[4:7], v[116:119]
ds_write_b128 v76, v[144:147] offset:32768
v_mfma_f32_16x16x32_bf16 v[132:135], v[196:199], a[8:11], v[132:135]
v_mfma_f32_16x16x32_bf16 v[148:151], v[196:199], a[12:15], v[148:151]
buffer_load_dwordx4 v[196:199], v1, s[4:7], s75 offen offset:3072 sc1
v_mfma_f32_16x16x32_bf16 v[100:103], v[200:203], a[16:19], v[100:103]
s_add_u32 s75, s85, 40960
s_waitcnt lgkmcnt(0)
s_barrier
ds_read_b128 v[112:115], v73 offset:24576
ds_read_b128 v[128:131], v73 offset:28672
v_mfma_f32_16x16x32_bf16 v[116:119], v[200:203], a[20:23], v[116:119]
ds_read_b128 v[144:147], v73 offset:32768
v_mfma_f32_16x16x32_bf16 v[132:135], v[200:203], a[24:27], v[132:135]
s_waitcnt vmcnt(10)
v_max3_u32 v18, v209, v211, v213
v_max_u32 v18, v18, v215
v_cmp_eq_u32 vcc, -1, v18
v_mfma_f32_16x16x32_bf16 v[148:151], v[200:203], a[28:31], v[148:151]
buffer_load_dwordx4 v[200:203], v1, s[4:7], s75 offen offset:3072 sc1
v_mfma_f32_16x16x32_bf16 v[100:103], v[204:207], a[32:35], v[100:103]
s_cbranch_vccnz .LS_276_%=
.LB_277_%=:
s_add_u32 s75, s85, 45056
v_mfma_f32_16x16x32_bf16 v[116:119], v[204:207], a[36:39], v[116:119]
v_mfma_f32_16x16x32_bf16 v[132:135], v[204:207], a[40:43], v[132:135]
s_waitcnt lgkmcnt(2)
v_add_f32 v96, v96, v112
v_add_f32 v97, v97, v113
v_add_f32 v98, v98, v114
v_add_f32 v99, v99, v115
v_mfma_f32_16x16x32_bf16 v[148:151], v[204:207], a[44:47], v[148:151]
buffer_load_dwordx4 v[204:207], v1, s[4:7], s75 offen offset:3072 sc1
s_waitcnt lgkmcnt(1)
v_add_f32 v96, v96, v128
v_add_f32 v97, v97, v129
v_mfma_f32_16x16x32_bf16 v[100:103], v[208:211], a[48:51], v[100:103]
s_add_u32 s75, s85, 49152
v_add_f32 v98, v98, v130
v_add_f32 v99, v99, v131
s_waitcnt lgkmcnt(0)
v_add_f32 v96, v96, v144
v_add_f32 v97, v97, v145
v_mfma_f32_16x16x32_bf16 v[116:119], v[208:211], a[52:55], v[116:119]
v_add_f32 v98, v98, v146
v_add_f32 v99, v99, v147
v_cvt_pk_bf16_f32 v224, v96, v97
v_cvt_pk_bf16_f32 v225, v98, v99
v_mfma_f32_16x16x32_bf16 v[132:135], v[208:211], a[56:59], v[132:135]
s_waitcnt vmcnt(10)
v_max3_u32 v18, v161, v163, v165
v_max_u32 v18, v18, v167
v_cmp_eq_u32 vcc, -1, v18
v_mfma_f32_16x16x32_bf16 v[148:151], v[208:211], a[60:63], v[148:151]
buffer_load_dwordx4 v[208:211], v1, s[4:7], s75 offen offset:3072 sc1
v_mfma_f32_16x16x32_bf16 v[100:103], v[212:215], v[80:83], v[100:103]
s_cbranch_vccnz .LS_279_%=
.LB_280_%=:
s_add_u32 s75, s85, 53248
v_mfma_f32_16x16x32_bf16 v[116:119], v[212:215], v[84:87], v[116:119]
v_mfma_f32_16x16x32_bf16 v[132:135], v[212:215], v[88:91], v[132:135]
v_mfma_f32_16x16x32_bf16 v[148:151], v[212:215], v[92:95], v[148:151]
buffer_load_dwordx4 v[212:215], v1, s[4:7], s75 offen offset:3072 sc1
v_mfma_f32_16x16x32_bf16 v[104:107], v[160:163], a[112:115], v[104:107]
v_mfma_f32_16x16x32_bf16 v[120:123], v[160:163], a[116:119], v[120:123]
v_mfma_f32_16x16x32_bf16 v[136:139], v[160:163], a[120:123], v[136:139]
s_waitcnt vmcnt(10)
v_max3_u32 v18, v169, v171, v173
v_max_u32 v18, v18, v175
v_cmp_eq_u32 vcc, -1, v18
v_mfma_f32_16x16x32_bf16 v[152:155], v[160:163], a[124:127], v[152:155]
ds_write_b128 v74, v[116:119] offset:25600
v_mfma_f32_16x16x32_bf16 v[104:107], v[164:167], a[128:131], v[104:107]
s_cbranch_vccnz .LS_282_%=
.LB_283_%=:
v_mfma_f32_16x16x32_bf16 v[120:123], v[164:167], a[132:135], v[120:123]
v_mfma_f32_16x16x32_bf16 v[136:139], v[164:167], a[136:139], v[136:139]
s_waitcnt vmcnt(8)
v_max3_u32 v18, v177, v179, v181
v_max_u32 v18, v18, v183
v_cmp_eq_u32 vcc, -1, v18
v_mfma_f32_16x16x32_bf16 v[152:155], v[164:167], a[140:143], v[152:155]
ds_write_b128 v75, v[132:135] offset:29696
v_mfma_f32_16x16x32_bf16 v[104:107], v[168:171], a[0:3], v[104:107]
s_cbranch_vccnz .LS_285_%=
.LB_286_%=:
s_waitcnt vmcnt(6)
v_max3_u32 v18, v185, v187, v189
v_mfma_f32_16x16x32_bf16 v[120:123], v[168:171], a[4:7], v[120:123]
v_max_u32 v18, v18, v191
v_cmp_eq_u32 vcc, -1, v18
v_mfma_f32_16x16x32_bf16 v[136:139], v[168:171], a[8:11], v[136:139]
ds_write_b128 v76, v[148:151] offset:33792
v_mfma_f32_16x16x32_bf16 v[152:155], v[168:171], a[12:15], v[152:155]
s_cbranch_vccnz .LS_288_%=
.LB_289_%=:
s_waitcnt vmcnt(4)
v_max3_u32 v18, v193, v195, v197
v_mfma_f32_16x16x32_bf16 v[104:107], v[172:175], a[16:19], v[104:107]
v_max_u32 v18, v18, v199
v_cmp_eq_u32 vcc, -1, v18
v_mfma_f32_16x16x32_bf16 v[120:123], v[172:175], a[20:23], v[120:123]
s_waitcnt lgkmcnt(0)
s_barrier
ds_read_b128 v[116:119], v73 offset:25600
ds_read_b128 v[132:135], v73 offset:29696
ds_read_b128 v[148:151], v73 offset:33792
v_mfma_f32_16x16x32_bf16 v[136:139], v[172:175], a[24:27], v[136:139]
s_cbranch_vccnz .LS_291_%=
.LB_292_%=:
s_waitcnt vmcnt(2)
v_max3_u32 v18, v201, v203, v205
v_mfma_f32_16x16x32_bf16 v[152:155], v[172:175], a[28:31], v[152:155]
v_max_u32 v18, v18, v207
v_cmp_eq_u32 vcc, -1, v18
v_mfma_f32_16x16x32_bf16 v[104:107], v[176:179], a[32:35], v[104:107]
v_mfma_f32_16x16x32_bf16 v[120:123], v[176:179], a[36:39], v[120:123]
s_cbranch_vccnz .LS_294_%=
.LB_295_%=:
s_waitcnt vmcnt(0)
v_max3_u32 v18, v209, v211, v213
v_mfma_f32_16x16x32_bf16 v[136:139], v[176:179], a[40:43], v[136:139]
v_max_u32 v18, v18, v215
v_cmp_eq_u32 vcc, -1, v18
s_waitcnt lgkmcnt(2)
v_add_f32 v100, v100, v116
v_add_f32 v101, v101, v117
v_mfma_f32_16x16x32_bf16 v[152:155], v[176:179], a[44:47], v[152:155]
v_add_f32 v102, v102, v118
v_add_f32 v103, v103, v119
s_waitcnt lgkmcnt(1)
v_add_f32 v100, v100, v132
v_add_f32 v101, v101, v133
v_mfma_f32_16x16x32_bf16 v[104:107], v[180:183], a[48:51], v[104:107]
s_cbranch_vccnz .LS_297_%=
.LB_298_%=:
v_add_f32 v102, v102, v134
v_add_f32 v103, v103, v135
v_mfma_f32_16x16x32_bf16 v[120:123], v[180:183], a[52:55], v[120:123]
s_waitcnt lgkmcnt(0)
v_add_f32 v100, v100, v148
v_add_f32 v101, v101, v149
v_add_f32 v102, v102, v150
v_add_f32 v103, v103, v151
v_mfma_f32_16x16x32_bf16 v[136:139], v[180:183], a[56:59], v[136:139]
v_cvt_pk_bf16_f32 v226, v100, v101
v_cvt_pk_bf16_f32 v227, v102, v103
s_nop 0
v_permlane16_swap_b32 v224, v226
v_mfma_f32_16x16x32_bf16 v[152:155], v[180:183], a[60:63], v[152:155]
v_permlane16_swap_b32 v225, v227
v_mfma_f32_16x16x32_bf16 v[104:107], v[184:187], v[80:83], v[104:107]
buffer_store_dwordx4 v[224:227], v67, s[32:35], s86 offen offset:0 sc1
v_mfma_f32_16x16x32_bf16 v[120:123], v[184:187], v[84:87], v[120:123]
v_mfma_f32_16x16x32_bf16 v[136:139], v[184:187], v[88:91], v[136:139]
v_mfma_f32_16x16x32_bf16 v[152:155], v[184:187], v[92:95], v[152:155]
v_mfma_f32_16x16x32_bf16 v[124:127], v[188:191], a[116:119], v[124:127]
v_mfma_f32_16x16x32_bf16 v[124:127], v[192:195], a[132:135], v[124:127]
v_mfma_f32_16x16x32_bf16 v[124:127], v[196:199], a[4:7], v[124:127]
s_nop 2
ds_write_b128 v74, v[120:123] offset:26624
v_mfma_f32_16x16x32_bf16 v[124:127], v[200:203], a[20:23], v[124:127]
v_mfma_f32_16x16x32_bf16 v[124:127], v[204:207], a[36:39], v[124:127]
v_mfma_f32_16x16x32_bf16 v[124:127], v[208:211], a[52:55], v[124:127]
ds_write_b128 v75, v[136:139] offset:30720
v_mfma_f32_16x16x32_bf16 v[124:127], v[212:215], v[84:87], v[124:127]
v_mfma_f32_16x16x32_bf16 v[140:143], v[188:191], a[120:123], v[140:143]
v_mfma_f32_16x16x32_bf16 v[140:143], v[192:195], a[136:139], v[140:143]
ds_write_b128 v76, v[152:155] offset:34816
v_mfma_f32_16x16x32_bf16 v[140:143], v[196:199], a[8:11], v[140:143]
v_mfma_f32_16x16x32_bf16 v[140:143], v[200:203], a[24:27], v[140:143]
v_mfma_f32_16x16x32_bf16 v[140:143], v[204:207], a[40:43], v[140:143]
s_nop 1
ds_write_b128 v74, v[124:127] offset:27648
v_mfma_f32_16x16x32_bf16 v[140:143], v[208:211], a[56:59], v[140:143]
v_mfma_f32_16x16x32_bf16 v[140:143], v[212:215], v[88:91], v[140:143]
v_mfma_f32_16x16x32_bf16 v[156:159], v[188:191], a[124:127], v[156:159]
v_mfma_f32_16x16x32_bf16 v[156:159], v[192:195], a[140:143], v[156:159]
v_mfma_f32_16x16x32_bf16 v[156:159], v[196:199], a[12:15], v[156:159]
s_nop 4
ds_write_b128 v75, v[140:143] offset:31744
v_mfma_f32_16x16x32_bf16 v[156:159], v[200:203], a[28:31], v[156:159]
v_mfma_f32_16x16x32_bf16 v[156:159], v[204:207], a[44:47], v[156:159]
v_mfma_f32_16x16x32_bf16 v[156:159], v[208:211], a[60:63], v[156:159]
v_mfma_f32_16x16x32_bf16 v[156:159], v[212:215], v[92:95], v[156:159]
v_mfma_f32_16x16x32_bf16 v[108:111], v[188:191], a[112:115], v[108:111]
v_mfma_f32_16x16x32_bf16 v[108:111], v[192:195], a[128:131], v[108:111]
v_mfma_f32_16x16x32_bf16 v[108:111], v[196:199], a[0:3], v[108:111]
s_nop 4
ds_write_b128 v76, v[156:159] offset:35840
v_mfma_f32_16x16x32_bf16 v[108:111], v[200:203], a[16:19], v[108:111]
v_mfma_f32_16x16x32_bf16 v[108:111], v[204:207], a[32:35], v[108:111]
v_mfma_f32_16x16x32_bf16 v[108:111], v[208:211], a[48:51], v[108:111]
s_waitcnt lgkmcnt(0)
s_barrier
ds_read_b128 v[120:123], v73 offset:26624
ds_read_b128 v[136:139], v73 offset:30720
ds_read_b128 v[152:155], v73 offset:34816
v_mfma_f32_16x16x32_bf16 v[108:111], v[212:215], v[80:83], v[108:111]
ds_read_b128 v[124:127], v73 offset:27648
ds_read_b128 v[140:143], v73 offset:31744
ds_read_b128 v[156:159], v73 offset:35840
s_waitcnt lgkmcnt(3)
v_pk_add_f32 v[136:137], v[136:137], v[152:153]
s_waitcnt lgkmcnt(0)
v_pk_add_f32 v[140:141], v[140:141], v[156:157]
v_pk_add_f32 v[104:105], v[104:105], v[120:121]
v_pk_add_f32 v[108:109], v[108:109], v[124:125]
v_pk_add_f32 v[138:139], v[138:139], v[154:155]
v_pk_add_f32 v[142:143], v[142:143], v[158:159]
v_pk_add_f32 v[106:107], v[106:107], v[122:123]
v_pk_add_f32 v[110:111], v[110:111], v[126:127]
v_pk_add_f32 v[104:105], v[104:105], v[136:137]
)ASM"
R"ASM(
v_pk_add_f32 v[108:109], v[108:109], v[140:141]
v_pk_add_f32 v[106:107], v[106:107], v[138:139]
v_pk_add_f32 v[110:111], v[110:111], v[142:143]
v_cvt_pk_bf16_f32 v228, v104, v105
v_cvt_pk_bf16_f32 v230, v108, v109
v_cvt_pk_bf16_f32 v229, v106, v107
v_cvt_pk_bf16_f32 v231, v110, v111
v_permlane16_swap_b32 v228, v230
s_nop 0
v_permlane16_swap_b32 v229, v231
buffer_store_dwordx4 v[228:231], v67, s[32:35], s86 offen offset:64 sc1
.LEND_%=:
s_or_b32 s70, s42, s54
s_cmp_lg_u32 s70, 0
s_cbranch_scc1 .LFIN_%=
v_mov_b32 v18, s62
v_mov_b32 v19, s63
s_mov_b64 exec, 1
buffer_store_dwordx2 v[18:19], off, s[4:7], 0 sc1
s_mov_b64 exec, -1
.LFIN_%=:
s_waitcnt vmcnt(0) lgkmcnt(0)
s_branch .LDONE_%=
.LIDLE_%=:
s_cmp_eq_u32 s59, 0
s_cbranch_scc1 .LIDLP_%=
s_waitcnt vmcnt(0)
ds_read_b64_tr_b16 a[0:1], v26 offset:4096
ds_read_b64_tr_b16 a[2:3], v26 offset:4608
ds_read_b64_tr_b16 a[4:5], v27 offset:4096
ds_read_b64_tr_b16 a[6:7], v27 offset:4608
ds_read_b64_tr_b16 a[8:9], v28 offset:4096
ds_read_b64_tr_b16 a[10:11], v28 offset:4608
ds_read_b64_tr_b16 a[12:13], v29 offset:4096
ds_read_b64_tr_b16 a[14:15], v29 offset:4608
ds_read_b64_tr_b16 a[16:17], v26 offset:0
ds_read_b64_tr_b16 a[18:19], v26 offset:512
ds_read_b64_tr_b16 a[20:21], v27 offset:0
ds_read_b64_tr_b16 a[22:23], v27 offset:512
ds_read_b64_tr_b16 a[24:25], v28 offset:0
ds_read_b64_tr_b16 a[26:27], v28 offset:512
ds_read_b64_tr_b16 a[28:29], v29 offset:0
ds_read_b64_tr_b16 a[30:31], v29 offset:512
ds_read_b64_tr_b16 a[32:33], v26 offset:8192
ds_read_b64_tr_b16 a[34:35], v26 offset:8704
ds_read_b64_tr_b16 a[36:37], v27 offset:8192
ds_read_b64_tr_b16 a[38:39], v27 offset:8704
ds_read_b64_tr_b16 a[40:41], v28 offset:8192
ds_read_b64_tr_b16 a[42:43], v28 offset:8704
ds_read_b64_tr_b16 a[44:45], v29 offset:8192
ds_read_b64_tr_b16 a[46:47], v29 offset:8704
ds_read_b64_tr_b16 a[48:49], v26 offset:12288
ds_read_b64_tr_b16 a[50:51], v26 offset:12800
ds_read_b64_tr_b16 a[52:53], v27 offset:12288
ds_read_b64_tr_b16 a[54:55], v27 offset:12800
ds_read_b64_tr_b16 a[56:57], v28 offset:12288
ds_read_b64_tr_b16 a[58:59], v28 offset:12800
ds_read_b64_tr_b16 a[60:61], v29 offset:12288
ds_read_b64_tr_b16 a[62:63], v29 offset:12800
ds_read_b64_tr_b16 a[64:65], v26 offset:16384
ds_read_b64_tr_b16 a[66:67], v26 offset:16896
ds_read_b64_tr_b16 a[68:69], v27 offset:16384
ds_read_b64_tr_b16 a[70:71], v27 offset:16896
ds_read_b64_tr_b16 a[72:73], v28 offset:16384
ds_read_b64_tr_b16 a[74:75], v28 offset:16896
ds_read_b64_tr_b16 a[76:77], v29 offset:16384
ds_read_b64_tr_b16 a[78:79], v29 offset:16896
ds_read_b64_tr_b16 a[80:81], v26 offset:32768
ds_read_b64_tr_b16 a[82:83], v26 offset:33280
ds_read_b64_tr_b16 a[84:85], v27 offset:32768
ds_read_b64_tr_b16 a[86:87], v27 offset:33280
ds_read_b64_tr_b16 a[88:89], v28 offset:32768
ds_read_b64_tr_b16 a[90:91], v28 offset:33280
ds_read_b64_tr_b16 a[92:93], v29 offset:32768
ds_read_b64_tr_b16 a[94:95], v29 offset:33280
ds_read_b64_tr_b16 a[96:97], v26 offset:24576
ds_read_b64_tr_b16 a[98:99], v26 offset:25088
ds_read_b64_tr_b16 a[100:101], v27 offset:24576
ds_read_b64_tr_b16 a[102:103], v27 offset:25088
ds_read_b64_tr_b16 a[104:105], v28 offset:24576
ds_read_b64_tr_b16 a[106:107], v28 offset:25088
ds_read_b64_tr_b16 a[108:109], v29 offset:24576
ds_read_b64_tr_b16 a[110:111], v29 offset:25088
ds_read_b64_tr_b16 a[112:113], v26 offset:28672
ds_read_b64_tr_b16 a[114:115], v26 offset:29184
ds_read_b64_tr_b16 a[116:117], v27 offset:28672
ds_read_b64_tr_b16 a[118:119], v27 offset:29184
ds_read_b64_tr_b16 a[120:121], v28 offset:28672
ds_read_b64_tr_b16 a[122:123], v28 offset:29184
ds_read_b64_tr_b16 a[124:125], v29 offset:28672
ds_read_b64_tr_b16 a[126:127], v29 offset:29184
s_waitcnt lgkmcnt(0)
s_add_u32 m0, s60, 16384
s_add_u32 s74, s80, 1179648
buffer_load_dwordx4 v44, s[28:31], s74 offen offset:0 lds
buffer_load_dwordx4 v45, s[28:31], s74 offen offset:1024 lds
buffer_load_dwordx4 v46, s[28:31], s74 offen offset:2048 lds
buffer_load_dwordx4 v47, s[28:31], s74 offen offset:3072 lds
s_add_u32 m0, s60, 0
s_add_u32 s74, s80, 1310720
buffer_load_dwordx4 v44, s[28:31], s74 offen offset:0 lds
buffer_load_dwordx4 v45, s[28:31], s74 offen offset:1024 lds
buffer_load_dwordx4 v46, s[28:31], s74 offen offset:2048 lds
buffer_load_dwordx4 v47, s[28:31], s74 offen offset:3072 lds
s_add_u32 m0, s60, 4096
s_add_u32 s74, s80, 1441792
buffer_load_dwordx4 v44, s[28:31], s74 offen offset:0 lds
buffer_load_dwordx4 v45, s[28:31], s74 offen offset:1024 lds
buffer_load_dwordx4 v46, s[28:31], s74 offen offset:2048 lds
buffer_load_dwordx4 v47, s[28:31], s74 offen offset:3072 lds
s_add_u32 m0, s60, 8192
s_add_u32 s74, s80, 1572864
buffer_load_dwordx4 v44, s[28:31], s74 offen offset:0 lds
buffer_load_dwordx4 v45, s[28:31], s74 offen offset:1024 lds
s_sleep 50
s_branch .LIDLQ_%=
.LIDLP_%=:
s_lshr_b32 s98, s16, 7
s_sub_u32 s98, 3, s98
s_and_b32 s98, s98, 7
s_xor_b32 s99, s98, s56
s_and_b32 s99, s99, 4
s_lshl_b32 s99, s99, 28
s_lshl_b32 s98, s98, 7
s_or_b32 s99, s99, s98
s_mul_i32 s98, s97, 3072
v_bfe_u32 v20, v0, 2, 2
v_mul_u32_u24 v20, 384, v20
v_and_b32 v21, 3, v0
v_min_u32 v21, 2, v21
v_add_lshl_u32 v20, v20, v21, 10
v_or_b32 v20, s99, v20
s_bfm_b64 exec, 16, 0
buffer_load_dword v77, v20, s[16:19], s98 offen
s_mov_b64 exec, -1
v_mov_b32 v252, -1
v_mov_b32 v253, -1
v_mov_b32 v254, -1
v_mov_b32 v255, -1
s_and_b32 s72, s55, -8
s_add_u32 s72, s72, s56
s_lshl_b32 s70, s72, 2
s_add_u32 s70, s70, s54
s_mul_i32 s71, s70, 16384
s_add_u32 s73, s71, 15204352
buffer_store_dwordx4 v[252:255], v1, s[4:7], s73 offen offset:0 sc1
buffer_store_dwordx4 v[252:255], v1, s[4:7], s73 offen offset:1024 sc1
buffer_store_dwordx4 v[252:255], v1, s[4:7], s73 offen offset:2048 sc1
buffer_store_dwordx4 v[252:255], v1, s[4:7], s73 offen offset:3072 sc1
s_add_u32 s73, s71, 15208448
buffer_store_dwordx4 v[252:255], v1, s[4:7], s73 offen offset:0 sc1
buffer_store_dwordx4 v[252:255], v1, s[4:7], s73 offen offset:1024 sc1
buffer_store_dwordx4 v[252:255], v1, s[4:7], s73 offen offset:2048 sc1
buffer_store_dwordx4 v[252:255], v1, s[4:7], s73 offen offset:3072 sc1
s_add_u32 s73, s71, 15212544
buffer_store_dwordx4 v[252:255], v1, s[4:7], s73 offen offset:0 sc1
buffer_store_dwordx4 v[252:255], v1, s[4:7], s73 offen offset:1024 sc1
s_waitcnt vmcnt(11)
ds_read_b64_tr_b16 a[0:1], v26 offset:4096
ds_read_b64_tr_b16 a[2:3], v26 offset:4608
ds_read_b64_tr_b16 a[4:5], v27 offset:4096
ds_read_b64_tr_b16 a[6:7], v27 offset:4608
ds_read_b64_tr_b16 a[8:9], v28 offset:4096
ds_read_b64_tr_b16 a[10:11], v28 offset:4608
ds_read_b64_tr_b16 a[12:13], v29 offset:4096
ds_read_b64_tr_b16 a[14:15], v29 offset:4608
ds_read_b64_tr_b16 a[16:17], v26 offset:0
ds_read_b64_tr_b16 a[18:19], v26 offset:512
ds_read_b64_tr_b16 a[20:21], v27 offset:0
ds_read_b64_tr_b16 a[22:23], v27 offset:512
ds_read_b64_tr_b16 a[24:25], v28 offset:0
ds_read_b64_tr_b16 a[26:27], v28 offset:512
ds_read_b64_tr_b16 a[28:29], v29 offset:0
ds_read_b64_tr_b16 a[30:31], v29 offset:512
ds_read_b64_tr_b16 a[32:33], v26 offset:8192
ds_read_b64_tr_b16 a[34:35], v26 offset:8704
ds_read_b64_tr_b16 a[36:37], v27 offset:8192
ds_read_b64_tr_b16 a[38:39], v27 offset:8704
ds_read_b64_tr_b16 a[40:41], v28 offset:8192
ds_read_b64_tr_b16 a[42:43], v28 offset:8704
ds_read_b64_tr_b16 a[44:45], v29 offset:8192
ds_read_b64_tr_b16 a[46:47], v29 offset:8704
ds_read_b64_tr_b16 a[48:49], v26 offset:12288
ds_read_b64_tr_b16 a[50:51], v26 offset:12800
ds_read_b64_tr_b16 a[52:53], v27 offset:12288
ds_read_b64_tr_b16 a[54:55], v27 offset:12800
ds_read_b64_tr_b16 a[56:57], v28 offset:12288
ds_read_b64_tr_b16 a[58:59], v28 offset:12800
ds_read_b64_tr_b16 a[60:61], v29 offset:12288
ds_read_b64_tr_b16 a[62:63], v29 offset:12800
ds_read_b64_tr_b16 a[64:65], v26 offset:16384
ds_read_b64_tr_b16 a[66:67], v26 offset:16896
ds_read_b64_tr_b16 a[68:69], v27 offset:16384
ds_read_b64_tr_b16 a[70:71], v27 offset:16896
ds_read_b64_tr_b16 a[72:73], v28 offset:16384
ds_read_b64_tr_b16 a[74:75], v28 offset:16896
ds_read_b64_tr_b16 a[76:77], v29 offset:16384
ds_read_b64_tr_b16 a[78:79], v29 offset:16896
ds_read_b64_tr_b16 a[80:81], v26 offset:32768
ds_read_b64_tr_b16 a[82:83], v26 offset:33280
ds_read_b64_tr_b16 a[84:85], v27 offset:32768
ds_read_b64_tr_b16 a[86:87], v27 offset:33280
ds_read_b64_tr_b16 a[88:89], v28 offset:32768
ds_read_b64_tr_b16 a[90:91], v28 offset:33280
ds_read_b64_tr_b16 a[92:93], v29 offset:32768
ds_read_b64_tr_b16 a[94:95], v29 offset:33280
ds_read_b64_tr_b16 a[96:97], v26 offset:24576
ds_read_b64_tr_b16 a[98:99], v26 offset:25088
ds_read_b64_tr_b16 a[100:101], v27 offset:24576
ds_read_b64_tr_b16 a[102:103], v27 offset:25088
ds_read_b64_tr_b16 a[104:105], v28 offset:24576
ds_read_b64_tr_b16 a[106:107], v28 offset:25088
ds_read_b64_tr_b16 a[108:109], v29 offset:24576
ds_read_b64_tr_b16 a[110:111], v29 offset:25088
ds_read_b64_tr_b16 a[112:113], v26 offset:28672
ds_read_b64_tr_b16 a[114:115], v26 offset:29184
ds_read_b64_tr_b16 a[116:117], v27 offset:28672
ds_read_b64_tr_b16 a[118:119], v27 offset:29184
ds_read_b64_tr_b16 a[120:121], v28 offset:28672
ds_read_b64_tr_b16 a[122:123], v28 offset:29184
ds_read_b64_tr_b16 a[124:125], v29 offset:28672
ds_read_b64_tr_b16 a[126:127], v29 offset:29184
s_waitcnt lgkmcnt(0)
buffer_store_dwordx4 v[252:255], v1, s[4:7], s73 offen offset:2048 sc1
buffer_store_dwordx4 v[252:255], v1, s[4:7], s73 offen offset:3072 sc1
s_add_u32 m0, s60, 16384
s_add_u32 s74, s80, 1179648
buffer_load_dwordx4 v44, s[28:31], s74 offen offset:0 lds
s_add_u32 s73, s71, 15216640
buffer_store_dwordx4 v[252:255], v1, s[4:7], s73 offen offset:0 sc1
buffer_load_dwordx4 v45, s[28:31], s74 offen offset:1024 lds
buffer_store_dwordx4 v[252:255], v1, s[4:7], s73 offen offset:1024 sc1
buffer_store_dwordx4 v[252:255], v1, s[4:7], s73 offen offset:2048 sc1
buffer_load_dwordx4 v46, s[28:31], s74 offen offset:2048 lds
buffer_store_dwordx4 v[252:255], v1, s[4:7], s73 offen offset:3072 sc1
s_mul_i32 s71, s70, 16384
buffer_load_dwordx4 v47, s[28:31], s74 offen offset:3072 lds
s_add_u32 s73, s71, 3407872
buffer_store_dwordx4 v[252:255], v1, s[4:7], s73 offen offset:0 sc1
s_add_u32 m0, s60, 0
s_add_u32 s74, s80, 1310720
buffer_load_dwordx4 v44, s[28:31], s74 offen offset:0 lds
buffer_store_dwordx4 v[252:255], v1, s[4:7], s73 offen offset:1024 sc1
buffer_store_dwordx4 v[252:255], v1, s[4:7], s73 offen offset:2048 sc1
buffer_load_dwordx4 v45, s[28:31], s74 offen offset:1024 lds
buffer_store_dwordx4 v[252:255], v1, s[4:7], s73 offen offset:3072 sc1
s_add_u32 s73, s71, 3411968
buffer_load_dwordx4 v46, s[28:31], s74 offen offset:2048 lds
buffer_store_dwordx4 v[252:255], v1, s[4:7], s73 offen offset:0 sc1
buffer_store_dwordx4 v[252:255], v1, s[4:7], s73 offen offset:1024 sc1
buffer_load_dwordx4 v47, s[28:31], s74 offen offset:3072 lds
buffer_store_dwordx4 v[252:255], v1, s[4:7], s73 offen offset:2048 sc1
buffer_store_dwordx4 v[252:255], v1, s[4:7], s73 offen offset:3072 sc1
s_add_u32 m0, s60, 4096
s_add_u32 s74, s80, 1441792
buffer_load_dwordx4 v44, s[28:31], s74 offen offset:0 lds
s_add_u32 s73, s71, 3416064
buffer_store_dwordx4 v[252:255], v1, s[4:7], s73 offen offset:0 sc1
buffer_load_dwordx4 v45, s[28:31], s74 offen offset:1024 lds
buffer_store_dwordx4 v[252:255], v1, s[4:7], s73 offen offset:1024 sc1
buffer_store_dwordx4 v[252:255], v1, s[4:7], s73 offen offset:2048 sc1
buffer_load_dwordx4 v46, s[28:31], s74 offen offset:2048 lds
buffer_store_dwordx4 v[252:255], v1, s[4:7], s73 offen offset:3072 sc1
s_add_u32 s73, s71, 3420160
buffer_load_dwordx4 v47, s[28:31], s74 offen offset:3072 lds
buffer_store_dwordx4 v[252:255], v1, s[4:7], s73 offen offset:0 sc1
buffer_store_dwordx4 v[252:255], v1, s[4:7], s73 offen offset:1024 sc1
s_add_u32 m0, s60, 8192
s_add_u32 s74, s80, 1572864
buffer_load_dwordx4 v44, s[28:31], s74 offen offset:0 lds
buffer_store_dwordx4 v[252:255], v1, s[4:7], s73 offen offset:2048 sc1
buffer_store_dwordx4 v[252:255], v1, s[4:7], s73 offen offset:3072 sc1
buffer_load_dwordx4 v45, s[28:31], s74 offen offset:1024 lds
s_mul_i32 s71, s70, 16384
s_add_u32 s73, s71, 8126464
)ASM"
R"ASM(
buffer_store_dwordx4 v[252:255], v1, s[4:7], s73 offen offset:0 sc1
buffer_store_dwordx4 v[252:255], v1, s[4:7], s73 offen offset:1024 sc1
buffer_store_dwordx4 v[252:255], v1, s[4:7], s73 offen offset:2048 sc1
buffer_store_dwordx4 v[252:255], v1, s[4:7], s73 offen offset:3072 sc1
s_add_u32 s73, s71, 8130560
buffer_store_dwordx4 v[252:255], v1, s[4:7], s73 offen offset:0 sc1
buffer_store_dwordx4 v[252:255], v1, s[4:7], s73 offen offset:1024 sc1
buffer_store_dwordx4 v[252:255], v1, s[4:7], s73 offen offset:2048 sc1
buffer_store_dwordx4 v[252:255], v1, s[4:7], s73 offen offset:3072 sc1
s_add_u32 s73, s71, 8134656
buffer_store_dwordx4 v[252:255], v1, s[4:7], s73 offen offset:0 sc1
buffer_store_dwordx4 v[252:255], v1, s[4:7], s73 offen offset:1024 sc1
buffer_store_dwordx4 v[252:255], v1, s[4:7], s73 offen offset:2048 sc1
buffer_store_dwordx4 v[252:255], v1, s[4:7], s73 offen offset:3072 sc1
s_add_u32 s73, s71, 8138752
buffer_store_dwordx4 v[252:255], v1, s[4:7], s73 offen offset:0 sc1
buffer_store_dwordx4 v[252:255], v1, s[4:7], s73 offen offset:1024 sc1
buffer_store_dwordx4 v[252:255], v1, s[4:7], s73 offen offset:2048 sc1
buffer_store_dwordx4 v[252:255], v1, s[4:7], s73 offen offset:3072 sc1
s_waitcnt vmcnt(0)
s_barrier
s_cmp_lg_u32 s54, 0
s_cbranch_scc1 .LIDLQ_%=
s_lshl_b32 s72, s72, 3
s_add_u32 s72, s72, 69632
v_mov_b32 v18, s62
v_mov_b32 v19, s63
v_lshlrev_b32 v20, 8, v0
s_mov_b64 exec, 1
buffer_store_dwordx2 v[18:19], v20, s[4:7], s72 offen sc1
s_mov_b64 exec, -1
.LIDLQ_%=:
v_lshlrev_b32 v20, 13, v0
v_mov_b32 v21, 0
.LIDLW_%=:
s_sleep 3
s_mov_b64 exec, 0x7f
buffer_load_dword v21, v20, s[4:7], s85 offen offset:4 sc1
s_mov_b64 exec, -1
s_waitcnt vmcnt(0)
v_cmp_eq_u32 vcc, -1, v21
s_cbranch_vccz .LJOIN_%=
s_sub_u32 s76, s76, 1
s_cbranch_scc0 .LIDLW_%=
s_mov_b32 s76, 0
s_branch .LJOIN_%=
.LS_3_%=:
buffer_load_dwordx2 v[252:253], v21, s[4:7], s72 offen sc1
s_waitcnt vmcnt(0)
v_cmp_ne_u64 vcc, s[62:63], v[252:253]
s_cbranch_vccz .LATT_%=
s_sub_u32 s76, s76, 1
s_cbranch_scc0 .LS_3_%=
s_mov_b32 s76, 0
s_branch .LATT_%=
.LS_4_%=:
s_nop 7
s_add_u32 s73, s82, 0
buffer_load_dwordx4 v[96:99], v1, s[4:7], s73 offen sc1
s_add_u32 s73, s82, 4096
buffer_load_dwordx4 v[100:103], v1, s[4:7], s73 offen sc1
s_add_u32 s73, s82, 8192
buffer_load_dwordx4 v[104:107], v1, s[4:7], s73 offen sc1
s_add_u32 s73, s82, 12288
buffer_load_dwordx4 v[108:111], v1, s[4:7], s73 offen sc1
s_waitcnt vmcnt(0)
v_max3_u32 v18, v97, v99, v101
v_max_u32 v18, v18, v103
v_cmp_eq_u32 vcc, -1, v18
s_cbranch_vccz .LB_5_%=
s_sub_u32 s76, s76, 1
s_cbranch_scc0 .LS_4_%=
s_mov_b32 s76, 0
s_branch .LB_5_%=
.LS_7_%=:
s_nop 7
s_add_u32 s73, s82, 8192
buffer_load_dwordx4 v[104:107], v1, s[4:7], s73 offen sc1
s_add_u32 s73, s82, 12288
buffer_load_dwordx4 v[108:111], v1, s[4:7], s73 offen sc1
s_add_u32 s73, s82, 1024
buffer_load_dwordx4 v[112:115], v1, s[4:7], s73 offen sc1
s_add_u32 s73, s82, 5120
buffer_load_dwordx4 v[116:119], v1, s[4:7], s73 offen sc1
s_waitcnt vmcnt(0)
v_max3_u32 v18, v105, v107, v109
v_max_u32 v18, v18, v111
v_cmp_eq_u32 vcc, -1, v18
s_cbranch_vccz .LB_8_%=
s_sub_u32 s76, s76, 1
s_cbranch_scc0 .LS_7_%=
s_mov_b32 s76, 0
s_branch .LB_8_%=
.LS_10_%=:
s_nop 7
s_add_u32 s73, s82, 1024
buffer_load_dwordx4 v[112:115], v1, s[4:7], s73 offen sc1
s_add_u32 s73, s82, 5120
buffer_load_dwordx4 v[116:119], v1, s[4:7], s73 offen sc1
s_add_u32 s73, s82, 9216
buffer_load_dwordx4 v[120:123], v1, s[4:7], s73 offen sc1
s_add_u32 s73, s82, 13312
buffer_load_dwordx4 v[124:127], v1, s[4:7], s73 offen sc1
s_waitcnt vmcnt(0)
v_max3_u32 v18, v113, v115, v117
v_max_u32 v18, v18, v119
v_cmp_eq_u32 vcc, -1, v18
s_cbranch_vccz .LB_11_%=
s_sub_u32 s76, s76, 1
s_cbranch_scc0 .LS_10_%=
s_mov_b32 s76, 0
s_branch .LB_11_%=
.LS_13_%=:
s_nop 7
s_add_u32 s73, s82, 9216
buffer_load_dwordx4 v[120:123], v1, s[4:7], s73 offen sc1
s_add_u32 s73, s82, 13312
buffer_load_dwordx4 v[124:127], v1, s[4:7], s73 offen sc1
s_add_u32 s73, s82, 2048
buffer_load_dwordx4 v[128:131], v1, s[4:7], s73 offen sc1
s_add_u32 s73, s82, 6144
buffer_load_dwordx4 v[132:135], v1, s[4:7], s73 offen sc1
s_waitcnt vmcnt(0)
v_max3_u32 v18, v121, v123, v125
v_max_u32 v18, v18, v127
v_cmp_eq_u32 vcc, -1, v18
s_cbranch_vccz .LB_14_%=
s_sub_u32 s76, s76, 1
s_cbranch_scc0 .LS_13_%=
s_mov_b32 s76, 0
s_branch .LB_14_%=
.LS_16_%=:
s_nop 7
s_add_u32 s73, s82, 2048
buffer_load_dwordx4 v[128:131], v1, s[4:7], s73 offen sc1
s_add_u32 s73, s82, 6144
buffer_load_dwordx4 v[132:135], v1, s[4:7], s73 offen sc1
s_add_u32 s73, s82, 10240
buffer_load_dwordx4 v[136:139], v1, s[4:7], s73 offen sc1
s_add_u32 s73, s82, 14336
buffer_load_dwordx4 v[140:143], v1, s[4:7], s73 offen sc1
s_waitcnt vmcnt(0)
v_max3_u32 v18, v129, v131, v133
v_max_u32 v18, v18, v135
v_cmp_eq_u32 vcc, -1, v18
s_cbranch_vccz .LB_17_%=
s_sub_u32 s76, s76, 1
s_cbranch_scc0 .LS_16_%=
s_mov_b32 s76, 0
s_branch .LB_17_%=
.LS_19_%=:
s_nop 7
s_add_u32 s73, s82, 10240
buffer_load_dwordx4 v[136:139], v1, s[4:7], s73 offen sc1
s_add_u32 s73, s82, 14336
buffer_load_dwordx4 v[140:143], v1, s[4:7], s73 offen sc1
s_add_u32 s73, s82, 3072
buffer_load_dwordx4 v[144:147], v1, s[4:7], s73 offen sc1
s_add_u32 s73, s82, 7168
buffer_load_dwordx4 v[148:151], v1, s[4:7], s73 offen sc1
s_waitcnt vmcnt(0)
v_max3_u32 v18, v137, v139, v141
v_max_u32 v18, v18, v143
v_cmp_eq_u32 vcc, -1, v18
s_cbranch_vccz .LB_20_%=
s_sub_u32 s76, s76, 1
s_cbranch_scc0 .LS_19_%=
s_mov_b32 s76, 0
s_branch .LB_20_%=
.LS_22_%=:
s_nop 7
s_add_u32 s73, s82, 3072
buffer_load_dwordx4 v[144:147], v1, s[4:7], s73 offen sc1
s_add_u32 s73, s82, 7168
buffer_load_dwordx4 v[148:151], v1, s[4:7], s73 offen sc1
s_add_u32 s73, s82, 11264
buffer_load_dwordx4 v[152:155], v1, s[4:7], s73 offen sc1
s_add_u32 s73, s82, 15360
buffer_load_dwordx4 v[156:159], v1, s[4:7], s73 offen sc1
s_waitcnt vmcnt(0)
v_max3_u32 v18, v145, v147, v149
v_max_u32 v18, v18, v151
v_cmp_eq_u32 vcc, -1, v18
s_cbranch_vccz .LB_23_%=
s_sub_u32 s76, s76, 1
s_cbranch_scc0 .LS_22_%=
s_mov_b32 s76, 0
s_branch .LB_23_%=
.LS_25_%=:
s_nop 7
s_add_u32 s73, s82, 11264
buffer_load_dwordx4 v[152:155], v1, s[4:7], s73 offen sc1
s_add_u32 s73, s82, 15360
buffer_load_dwordx4 v[156:159], v1, s[4:7], s73 offen sc1
s_waitcnt vmcnt(0)
v_max3_u32 v18, v153, v155, v157
v_max_u32 v18, v18, v159
v_cmp_eq_u32 vcc, -1, v18
s_cbranch_vccz .LB_26_%=
s_sub_u32 s76, s76, 1
s_cbranch_scc0 .LS_25_%=
s_mov_b32 s76, 0
s_branch .LB_26_%=
.LS_28_%=:
s_nop 7
buffer_load_dwordx4 v[128:131], v70, s[4:7], s87 offen sc1
s_waitcnt vmcnt(0)
v_max3_u32 v18, v128, v129, v130
v_max_u32 v18, v18, v131
v_cmp_eq_u32 vcc, -1, v18
s_cbranch_vccz .LB_29_%=
s_sub_u32 s76, s76, 1
s_cbranch_scc0 .LS_28_%=
s_mov_b32 s76, 0
s_branch .LB_29_%=
.LS_33_%=:
s_nop 7
s_add_u32 s73, s92, 12288
buffer_load_dwordx4 v[160:163], v1, s[8:11], s73 offen offset:0 sc1
s_add_u32 s73, s92, 12288
buffer_load_dwordx4 v[164:167], v1, s[8:11], s73 offen offset:1024 sc1
s_add_u32 s73, s92, 12288
buffer_load_dwordx4 v[168:171], v1, s[8:11], s73 offen offset:2048 sc1
s_add_u32 s73, s92, 12288
buffer_load_dwordx4 v[172:175], v1, s[8:11], s73 offen offset:3072 sc1
s_add_u32 s73, s92, 16384
buffer_load_dwordx4 v[176:179], v1, s[8:11], s73 offen offset:0 sc1
s_add_u32 s73, s92, 16384
buffer_load_dwordx4 v[180:183], v1, s[8:11], s73 offen offset:1024 sc1
s_add_u32 s73, s92, 16384
buffer_load_dwordx4 v[184:187], v1, s[8:11], s73 offen offset:2048 sc1
s_add_u32 s73, s92, 16384
buffer_load_dwordx4 v[188:191], v1, s[8:11], s73 offen offset:3072 sc1
s_waitcnt vmcnt(0)
v_max3_u32 v18, v161, v163, v165
v_max_u32 v18, v18, v167
v_cmp_eq_u32 vcc, -1, v18
s_cbranch_vccz .LB_34_%=
s_sub_u32 s76, s76, 1
s_cbranch_scc0 .LS_33_%=
s_mov_b32 s76, 0
s_branch .LB_34_%=
.LS_36_%=:
s_nop 7
s_add_u32 s73, s92, 12288
buffer_load_dwordx4 v[168:171], v1, s[8:11], s73 offen offset:2048 sc1
s_add_u32 s73, s92, 12288
buffer_load_dwordx4 v[172:175], v1, s[8:11], s73 offen offset:3072 sc1
s_add_u32 s73, s92, 16384
buffer_load_dwordx4 v[176:179], v1, s[8:11], s73 offen offset:0 sc1
s_add_u32 s73, s92, 16384
buffer_load_dwordx4 v[180:183], v1, s[8:11], s73 offen offset:1024 sc1
s_add_u32 s73, s92, 16384
buffer_load_dwordx4 v[184:187], v1, s[8:11], s73 offen offset:2048 sc1
s_add_u32 s73, s92, 16384
buffer_load_dwordx4 v[188:191], v1, s[8:11], s73 offen offset:3072 sc1
s_waitcnt vmcnt(0)
v_max3_u32 v18, v169, v171, v173
v_max_u32 v18, v18, v175
v_cmp_eq_u32 vcc, -1, v18
s_cbranch_vccz .LB_37_%=
s_sub_u32 s76, s76, 1
s_cbranch_scc0 .LS_36_%=
s_mov_b32 s76, 0
s_branch .LB_37_%=
.LS_39_%=:
s_nop 7
s_add_u32 s73, s92, 16384
buffer_load_dwordx4 v[176:179], v1, s[8:11], s73 offen offset:0 sc1
s_add_u32 s73, s92, 16384
buffer_load_dwordx4 v[180:183], v1, s[8:11], s73 offen offset:1024 sc1
s_add_u32 s73, s92, 16384
buffer_load_dwordx4 v[184:187], v1, s[8:11], s73 offen offset:2048 sc1
s_add_u32 s73, s92, 16384
buffer_load_dwordx4 v[188:191], v1, s[8:11], s73 offen offset:3072 sc1
s_waitcnt vmcnt(0)
v_max3_u32 v18, v177, v179, v181
v_max_u32 v18, v18, v183
v_cmp_eq_u32 vcc, -1, v18
s_cbranch_vccz .LB_40_%=
s_sub_u32 s76, s76, 1
s_cbranch_scc0 .LS_39_%=
s_mov_b32 s76, 0
s_branch .LB_40_%=
.LS_42_%=:
s_nop 7
s_add_u32 s73, s92, 16384
buffer_load_dwordx4 v[184:187], v1, s[8:11], s73 offen offset:2048 sc1
s_add_u32 s73, s92, 16384
buffer_load_dwordx4 v[188:191], v1, s[8:11], s73 offen offset:3072 sc1
s_waitcnt vmcnt(0)
v_max3_u32 v18, v185, v187, v189
v_max_u32 v18, v18, v191
v_cmp_eq_u32 vcc, -1, v18
s_cbranch_vccz .LB_43_%=
s_sub_u32 s76, s76, 1
s_cbranch_scc0 .LS_42_%=
s_mov_b32 s76, 0
s_branch .LB_43_%=
.LS_45_%=:
s_nop 7
s_add_u32 s73, s92, 20480
buffer_load_dwordx4 v[192:195], v1, s[8:11], s73 offen offset:0 sc1
s_add_u32 s73, s92, 20480
buffer_load_dwordx4 v[196:199], v1, s[8:11], s73 offen offset:1024 sc1
s_add_u32 s73, s92, 20480
buffer_load_dwordx4 v[200:203], v1, s[8:11], s73 offen offset:2048 sc1
s_add_u32 s73, s92, 20480
buffer_load_dwordx4 v[204:207], v1, s[8:11], s73 offen offset:3072 sc1
s_add_u32 s73, s92, 24576
buffer_load_dwordx4 v[208:211], v1, s[8:11], s73 offen offset:0 sc1
s_add_u32 s73, s92, 24576
buffer_load_dwordx4 v[212:215], v1, s[8:11], s73 offen offset:1024 sc1
s_add_u32 s73, s92, 24576
buffer_load_dwordx4 v[160:163], v1, s[8:11], s73 offen offset:2048 sc1
s_add_u32 s73, s92, 24576
buffer_load_dwordx4 v[164:167], v1, s[8:11], s73 offen offset:3072 sc1
s_waitcnt vmcnt(0)
v_max3_u32 v18, v193, v195, v197
v_max_u32 v18, v18, v199
v_cmp_eq_u32 vcc, -1, v18
s_cbranch_vccz .LB_46_%=
s_sub_u32 s76, s76, 1
s_cbranch_scc0 .LS_45_%=
s_mov_b32 s76, 0
s_branch .LB_46_%=
.LS_48_%=:
s_nop 7
s_add_u32 s73, s92, 20480
buffer_load_dwordx4 v[200:203], v1, s[8:11], s73 offen offset:2048 sc1
s_add_u32 s73, s92, 20480
buffer_load_dwordx4 v[204:207], v1, s[8:11], s73 offen offset:3072 sc1
s_add_u32 s73, s92, 24576
buffer_load_dwordx4 v[208:211], v1, s[8:11], s73 offen offset:0 sc1
s_add_u32 s73, s92, 24576
buffer_load_dwordx4 v[212:215], v1, s[8:11], s73 offen offset:1024 sc1
s_add_u32 s73, s92, 24576
buffer_load_dwordx4 v[160:163], v1, s[8:11], s73 offen offset:2048 sc1
s_add_u32 s73, s92, 24576
buffer_load_dwordx4 v[164:167], v1, s[8:11], s73 offen offset:3072 sc1
s_waitcnt vmcnt(0)
v_max3_u32 v18, v201, v203, v205
v_max_u32 v18, v18, v207
v_cmp_eq_u32 vcc, -1, v18
s_cbranch_vccz .LB_49_%=
s_sub_u32 s76, s76, 1
s_cbranch_scc0 .LS_48_%=
s_mov_b32 s76, 0
s_branch .LB_49_%=
.LS_51_%=:
s_nop 7
s_add_u32 s73, s92, 24576
buffer_load_dwordx4 v[208:211], v1, s[8:11], s73 offen offset:0 sc1
s_add_u32 s73, s92, 24576
buffer_load_dwordx4 v[212:215], v1, s[8:11], s73 offen offset:1024 sc1
s_add_u32 s73, s92, 24576
buffer_load_dwordx4 v[160:163], v1, s[8:11], s73 offen offset:2048 sc1
s_add_u32 s73, s92, 24576
buffer_load_dwordx4 v[164:167], v1, s[8:11], s73 offen offset:3072 sc1
s_waitcnt vmcnt(0)
v_max3_u32 v18, v209, v211, v213
v_max_u32 v18, v18, v215
v_cmp_eq_u32 vcc, -1, v18
s_cbranch_vccz .LB_52_%=
s_sub_u32 s76, s76, 1
s_cbranch_scc0 .LS_51_%=
s_mov_b32 s76, 0
s_branch .LB_52_%=
.LS_54_%=:
s_nop 7
s_add_u32 s73, s92, 24576
buffer_load_dwordx4 v[160:163], v1, s[8:11], s73 offen offset:2048 sc1
)ASM"
R"ASM(
s_add_u32 s73, s92, 24576
buffer_load_dwordx4 v[164:167], v1, s[8:11], s73 offen offset:3072 sc1
s_waitcnt vmcnt(0)
v_max3_u32 v18, v161, v163, v165
v_max_u32 v18, v18, v167
v_cmp_eq_u32 vcc, -1, v18
s_cbranch_vccz .LB_55_%=
s_sub_u32 s76, s76, 1
s_cbranch_scc0 .LS_54_%=
s_mov_b32 s76, 0
s_branch .LB_55_%=
.LS_57_%=:
s_nop 7
s_add_u32 s73, s92, 28672
buffer_load_dwordx4 v[168:171], v1, s[8:11], s73 offen offset:0 sc1
s_add_u32 s73, s92, 28672
buffer_load_dwordx4 v[172:175], v1, s[8:11], s73 offen offset:1024 sc1
s_add_u32 s73, s92, 28672
buffer_load_dwordx4 v[176:179], v1, s[8:11], s73 offen offset:2048 sc1
s_add_u32 s73, s92, 28672
buffer_load_dwordx4 v[180:183], v1, s[8:11], s73 offen offset:3072 sc1
s_add_u32 s73, s92, 32768
buffer_load_dwordx4 v[184:187], v1, s[8:11], s73 offen offset:0 sc1
s_add_u32 s73, s92, 32768
buffer_load_dwordx4 v[188:191], v1, s[8:11], s73 offen offset:1024 sc1
s_add_u32 s73, s92, 32768
buffer_load_dwordx4 v[192:195], v1, s[8:11], s73 offen offset:2048 sc1
s_add_u32 s73, s92, 32768
buffer_load_dwordx4 v[196:199], v1, s[8:11], s73 offen offset:3072 sc1
s_waitcnt vmcnt(0)
v_max3_u32 v18, v169, v171, v173
v_max_u32 v18, v18, v175
v_cmp_eq_u32 vcc, -1, v18
s_cbranch_vccz .LB_58_%=
s_sub_u32 s76, s76, 1
s_cbranch_scc0 .LS_57_%=
s_mov_b32 s76, 0
s_branch .LB_58_%=
.LS_60_%=:
s_nop 7
s_add_u32 s73, s92, 28672
buffer_load_dwordx4 v[176:179], v1, s[8:11], s73 offen offset:2048 sc1
s_add_u32 s73, s92, 28672
buffer_load_dwordx4 v[180:183], v1, s[8:11], s73 offen offset:3072 sc1
s_add_u32 s73, s92, 32768
buffer_load_dwordx4 v[184:187], v1, s[8:11], s73 offen offset:0 sc1
s_add_u32 s73, s92, 32768
buffer_load_dwordx4 v[188:191], v1, s[8:11], s73 offen offset:1024 sc1
s_add_u32 s73, s92, 32768
buffer_load_dwordx4 v[192:195], v1, s[8:11], s73 offen offset:2048 sc1
s_add_u32 s73, s92, 32768
buffer_load_dwordx4 v[196:199], v1, s[8:11], s73 offen offset:3072 sc1
s_waitcnt vmcnt(0)
v_max3_u32 v18, v177, v179, v181
v_max_u32 v18, v18, v183
v_cmp_eq_u32 vcc, -1, v18
s_cbranch_vccz .LB_61_%=
s_sub_u32 s76, s76, 1
s_cbranch_scc0 .LS_60_%=
s_mov_b32 s76, 0
s_branch .LB_61_%=
.LS_63_%=:
s_nop 7
s_add_u32 s73, s92, 32768
buffer_load_dwordx4 v[184:187], v1, s[8:11], s73 offen offset:0 sc1
s_add_u32 s73, s92, 32768
buffer_load_dwordx4 v[188:191], v1, s[8:11], s73 offen offset:1024 sc1
s_add_u32 s73, s92, 32768
buffer_load_dwordx4 v[192:195], v1, s[8:11], s73 offen offset:2048 sc1
s_add_u32 s73, s92, 32768
buffer_load_dwordx4 v[196:199], v1, s[8:11], s73 offen offset:3072 sc1
s_waitcnt vmcnt(0)
v_max3_u32 v18, v185, v187, v189
v_max_u32 v18, v18, v191
v_cmp_eq_u32 vcc, -1, v18
s_cbranch_vccz .LB_64_%=
s_sub_u32 s76, s76, 1
s_cbranch_scc0 .LS_63_%=
s_mov_b32 s76, 0
s_branch .LB_64_%=
.LS_66_%=:
s_nop 7
s_add_u32 s73, s92, 32768
buffer_load_dwordx4 v[192:195], v1, s[8:11], s73 offen offset:2048 sc1
s_add_u32 s73, s92, 32768
buffer_load_dwordx4 v[196:199], v1, s[8:11], s73 offen offset:3072 sc1
s_waitcnt vmcnt(0)
v_max3_u32 v18, v193, v195, v197
v_max_u32 v18, v18, v199
v_cmp_eq_u32 vcc, -1, v18
s_cbranch_vccz .LB_67_%=
s_sub_u32 s76, s76, 1
s_cbranch_scc0 .LS_66_%=
s_mov_b32 s76, 0
s_branch .LB_67_%=
.LS_69_%=:
s_nop 7
s_add_u32 s73, s92, 36864
buffer_load_dwordx4 v[200:203], v1, s[8:11], s73 offen offset:0 sc1
s_add_u32 s73, s92, 36864
buffer_load_dwordx4 v[204:207], v1, s[8:11], s73 offen offset:1024 sc1
s_add_u32 s73, s92, 36864
buffer_load_dwordx4 v[208:211], v1, s[8:11], s73 offen offset:2048 sc1
s_add_u32 s73, s92, 36864
buffer_load_dwordx4 v[212:215], v1, s[8:11], s73 offen offset:3072 sc1
s_add_u32 s73, s92, 40960
buffer_load_dwordx4 v[160:163], v1, s[8:11], s73 offen offset:0 sc1
s_add_u32 s73, s92, 40960
buffer_load_dwordx4 v[164:167], v1, s[8:11], s73 offen offset:1024 sc1
s_add_u32 s73, s92, 40960
buffer_load_dwordx4 v[168:171], v1, s[8:11], s73 offen offset:2048 sc1
s_add_u32 s73, s92, 40960
buffer_load_dwordx4 v[172:175], v1, s[8:11], s73 offen offset:3072 sc1
s_waitcnt vmcnt(0)
v_max3_u32 v18, v201, v203, v205
v_max_u32 v18, v18, v207
v_cmp_eq_u32 vcc, -1, v18
s_cbranch_vccz .LB_70_%=
s_sub_u32 s76, s76, 1
s_cbranch_scc0 .LS_69_%=
s_mov_b32 s76, 0
s_branch .LB_70_%=
.LS_72_%=:
s_nop 7
s_add_u32 s73, s92, 36864
buffer_load_dwordx4 v[208:211], v1, s[8:11], s73 offen offset:2048 sc1
s_add_u32 s73, s92, 36864
buffer_load_dwordx4 v[212:215], v1, s[8:11], s73 offen offset:3072 sc1
s_add_u32 s73, s92, 40960
buffer_load_dwordx4 v[160:163], v1, s[8:11], s73 offen offset:0 sc1
s_add_u32 s73, s92, 40960
buffer_load_dwordx4 v[164:167], v1, s[8:11], s73 offen offset:1024 sc1
s_add_u32 s73, s92, 40960
buffer_load_dwordx4 v[168:171], v1, s[8:11], s73 offen offset:2048 sc1
s_add_u32 s73, s92, 40960
buffer_load_dwordx4 v[172:175], v1, s[8:11], s73 offen offset:3072 sc1
s_waitcnt vmcnt(0)
v_max3_u32 v18, v209, v211, v213
v_max_u32 v18, v18, v215
v_cmp_eq_u32 vcc, -1, v18
s_cbranch_vccz .LB_73_%=
s_sub_u32 s76, s76, 1
s_cbranch_scc0 .LS_72_%=
s_mov_b32 s76, 0
s_branch .LB_73_%=
.LS_75_%=:
s_nop 7
s_add_u32 s73, s92, 40960
buffer_load_dwordx4 v[160:163], v1, s[8:11], s73 offen offset:0 sc1
s_add_u32 s73, s92, 40960
buffer_load_dwordx4 v[164:167], v1, s[8:11], s73 offen offset:1024 sc1
s_add_u32 s73, s92, 40960
buffer_load_dwordx4 v[168:171], v1, s[8:11], s73 offen offset:2048 sc1
s_add_u32 s73, s92, 40960
buffer_load_dwordx4 v[172:175], v1, s[8:11], s73 offen offset:3072 sc1
s_waitcnt vmcnt(0)
v_max3_u32 v18, v161, v163, v165
v_max_u32 v18, v18, v167
v_cmp_eq_u32 vcc, -1, v18
s_cbranch_vccz .LB_76_%=
s_sub_u32 s76, s76, 1
s_cbranch_scc0 .LS_75_%=
s_mov_b32 s76, 0
s_branch .LB_76_%=
.LS_78_%=:
s_nop 7
s_add_u32 s73, s92, 40960
buffer_load_dwordx4 v[168:171], v1, s[8:11], s73 offen offset:2048 sc1
s_add_u32 s73, s92, 40960
buffer_load_dwordx4 v[172:175], v1, s[8:11], s73 offen offset:3072 sc1
s_waitcnt vmcnt(0)
v_max3_u32 v18, v169, v171, v173
v_max_u32 v18, v18, v175
v_cmp_eq_u32 vcc, -1, v18
s_cbranch_vccz .LB_79_%=
s_sub_u32 s76, s76, 1
s_cbranch_scc0 .LS_78_%=
s_mov_b32 s76, 0
s_branch .LB_79_%=
.LS_81_%=:
s_nop 7
s_add_u32 s73, s92, 45056
buffer_load_dwordx4 v[176:179], v1, s[8:11], s73 offen offset:0 sc1
s_add_u32 s73, s92, 49152
buffer_load_dwordx4 v[180:183], v1, s[8:11], s73 offen offset:0 sc1
s_add_u32 s73, s92, 53248
buffer_load_dwordx4 v[184:187], v1, s[8:11], s73 offen offset:0 sc1
s_add_u32 s73, s92, 57344
buffer_load_dwordx4 v[188:191], v1, s[8:11], s73 offen offset:0 sc1
s_add_u32 s73, s92, 61440
buffer_load_dwordx4 v[192:195], v1, s[8:11], s73 offen offset:0 sc1
s_add_u32 s73, s92, 0
buffer_load_dwordx4 v[196:199], v1, s[8:11], s73 offen offset:0 sc1
s_add_u32 s73, s92, 4096
buffer_load_dwordx4 v[200:203], v1, s[8:11], s73 offen offset:0 sc1
s_add_u32 s73, s92, 8192
buffer_load_dwordx4 v[204:207], v1, s[8:11], s73 offen offset:0 sc1
s_waitcnt vmcnt(0)
v_max3_u32 v18, v177, v179, v181
v_max_u32 v18, v18, v183
v_cmp_eq_u32 vcc, -1, v18
s_cbranch_vccz .LB_82_%=
s_sub_u32 s76, s76, 1
s_cbranch_scc0 .LS_81_%=
s_mov_b32 s76, 0
s_branch .LB_82_%=
.LS_84_%=:
s_nop 7
s_add_u32 s73, s92, 53248
buffer_load_dwordx4 v[184:187], v1, s[8:11], s73 offen offset:0 sc1
s_add_u32 s73, s92, 57344
buffer_load_dwordx4 v[188:191], v1, s[8:11], s73 offen offset:0 sc1
s_add_u32 s73, s92, 61440
buffer_load_dwordx4 v[192:195], v1, s[8:11], s73 offen offset:0 sc1
s_add_u32 s73, s92, 0
buffer_load_dwordx4 v[196:199], v1, s[8:11], s73 offen offset:0 sc1
s_add_u32 s73, s92, 4096
buffer_load_dwordx4 v[200:203], v1, s[8:11], s73 offen offset:0 sc1
s_add_u32 s73, s92, 8192
buffer_load_dwordx4 v[204:207], v1, s[8:11], s73 offen offset:0 sc1
s_waitcnt vmcnt(0)
v_max3_u32 v18, v185, v187, v189
v_max_u32 v18, v18, v191
v_cmp_eq_u32 vcc, -1, v18
s_cbranch_vccz .LB_85_%=
s_sub_u32 s76, s76, 1
s_cbranch_scc0 .LS_84_%=
s_mov_b32 s76, 0
s_branch .LB_85_%=
.LS_87_%=:
s_nop 7
s_add_u32 s73, s92, 61440
buffer_load_dwordx4 v[192:195], v1, s[8:11], s73 offen offset:0 sc1
s_add_u32 s73, s92, 0
buffer_load_dwordx4 v[196:199], v1, s[8:11], s73 offen offset:0 sc1
s_add_u32 s73, s92, 4096
buffer_load_dwordx4 v[200:203], v1, s[8:11], s73 offen offset:0 sc1
s_add_u32 s73, s92, 8192
buffer_load_dwordx4 v[204:207], v1, s[8:11], s73 offen offset:0 sc1
s_waitcnt vmcnt(0)
v_max3_u32 v18, v193, v195, v197
v_max_u32 v18, v18, v199
v_cmp_eq_u32 vcc, -1, v18
s_cbranch_vccz .LB_88_%=
s_sub_u32 s76, s76, 1
s_cbranch_scc0 .LS_87_%=
s_mov_b32 s76, 0
s_branch .LB_88_%=
.LS_90_%=:
s_nop 7
s_add_u32 s73, s92, 4096
buffer_load_dwordx4 v[200:203], v1, s[8:11], s73 offen offset:0 sc1
s_add_u32 s73, s92, 8192
buffer_load_dwordx4 v[204:207], v1, s[8:11], s73 offen offset:0 sc1
s_waitcnt vmcnt(0)
v_max3_u32 v18, v201, v203, v205
v_max_u32 v18, v18, v207
v_cmp_eq_u32 vcc, -1, v18
s_cbranch_vccz .LB_91_%=
s_sub_u32 s76, s76, 1
s_cbranch_scc0 .LS_90_%=
s_mov_b32 s76, 0
s_branch .LB_91_%=
.LS_93_%=:
s_nop 7
s_add_u32 s73, s92, 45056
buffer_load_dwordx4 v[208:211], v1, s[8:11], s73 offen offset:1024 sc1
s_add_u32 s73, s92, 49152
buffer_load_dwordx4 v[212:215], v1, s[8:11], s73 offen offset:1024 sc1
s_add_u32 s73, s92, 53248
buffer_load_dwordx4 v[160:163], v1, s[8:11], s73 offen offset:1024 sc1
s_add_u32 s73, s92, 57344
buffer_load_dwordx4 v[164:167], v1, s[8:11], s73 offen offset:1024 sc1
s_add_u32 s73, s92, 61440
buffer_load_dwordx4 v[168:171], v1, s[8:11], s73 offen offset:1024 sc1
s_add_u32 s73, s92, 0
buffer_load_dwordx4 v[172:175], v1, s[8:11], s73 offen offset:1024 sc1
s_add_u32 s73, s92, 4096
buffer_load_dwordx4 v[176:179], v1, s[8:11], s73 offen offset:1024 sc1
s_add_u32 s73, s92, 8192
buffer_load_dwordx4 v[180:183], v1, s[8:11], s73 offen offset:1024 sc1
s_waitcnt vmcnt(0)
v_max3_u32 v18, v209, v211, v213
v_max_u32 v18, v18, v215
v_cmp_eq_u32 vcc, -1, v18
s_cbranch_vccz .LB_94_%=
s_sub_u32 s76, s76, 1
s_cbranch_scc0 .LS_93_%=
s_mov_b32 s76, 0
s_branch .LB_94_%=
.LS_96_%=:
s_nop 7
s_add_u32 s73, s92, 53248
buffer_load_dwordx4 v[160:163], v1, s[8:11], s73 offen offset:1024 sc1
s_add_u32 s73, s92, 57344
buffer_load_dwordx4 v[164:167], v1, s[8:11], s73 offen offset:1024 sc1
s_add_u32 s73, s92, 61440
buffer_load_dwordx4 v[168:171], v1, s[8:11], s73 offen offset:1024 sc1
s_add_u32 s73, s92, 0
buffer_load_dwordx4 v[172:175], v1, s[8:11], s73 offen offset:1024 sc1
s_add_u32 s73, s92, 4096
buffer_load_dwordx4 v[176:179], v1, s[8:11], s73 offen offset:1024 sc1
s_add_u32 s73, s92, 8192
buffer_load_dwordx4 v[180:183], v1, s[8:11], s73 offen offset:1024 sc1
s_waitcnt vmcnt(0)
v_max3_u32 v18, v161, v163, v165
v_max_u32 v18, v18, v167
v_cmp_eq_u32 vcc, -1, v18
s_cbranch_vccz .LB_97_%=
s_sub_u32 s76, s76, 1
s_cbranch_scc0 .LS_96_%=
s_mov_b32 s76, 0
s_branch .LB_97_%=
.LS_99_%=:
s_nop 7
s_add_u32 s73, s92, 61440
buffer_load_dwordx4 v[168:171], v1, s[8:11], s73 offen offset:1024 sc1
s_add_u32 s73, s92, 0
buffer_load_dwordx4 v[172:175], v1, s[8:11], s73 offen offset:1024 sc1
s_add_u32 s73, s92, 4096
buffer_load_dwordx4 v[176:179], v1, s[8:11], s73 offen offset:1024 sc1
s_add_u32 s73, s92, 8192
buffer_load_dwordx4 v[180:183], v1, s[8:11], s73 offen offset:1024 sc1
s_waitcnt vmcnt(0)
v_max3_u32 v18, v169, v171, v173
v_max_u32 v18, v18, v175
v_cmp_eq_u32 vcc, -1, v18
s_cbranch_vccz .LB_100_%=
s_sub_u32 s76, s76, 1
s_cbranch_scc0 .LS_99_%=
s_mov_b32 s76, 0
s_branch .LB_100_%=
.LS_102_%=:
s_nop 7
s_add_u32 s73, s92, 4096
buffer_load_dwordx4 v[176:179], v1, s[8:11], s73 offen offset:1024 sc1
s_add_u32 s73, s92, 8192
buffer_load_dwordx4 v[180:183], v1, s[8:11], s73 offen offset:1024 sc1
s_waitcnt vmcnt(0)
v_max3_u32 v18, v177, v179, v181
v_max_u32 v18, v18, v183
v_cmp_eq_u32 vcc, -1, v18
s_cbranch_vccz .LB_103_%=
s_sub_u32 s76, s76, 1
s_cbranch_scc0 .LS_102_%=
s_mov_b32 s76, 0
s_branch .LB_103_%=
.LS_105_%=:
s_nop 7
s_add_u32 s73, s92, 45056
buffer_load_dwordx4 v[184:187], v1, s[8:11], s73 offen offset:2048 sc1
s_add_u32 s73, s92, 49152
buffer_load_dwordx4 v[188:191], v1, s[8:11], s73 offen offset:2048 sc1
s_add_u32 s73, s92, 53248
buffer_load_dwordx4 v[192:195], v1, s[8:11], s73 offen offset:2048 sc1
s_add_u32 s73, s92, 57344
buffer_load_dwordx4 v[196:199], v1, s[8:11], s73 offen offset:2048 sc1
)ASM"
R"ASM(
s_add_u32 s73, s92, 61440
buffer_load_dwordx4 v[200:203], v1, s[8:11], s73 offen offset:2048 sc1
s_add_u32 s73, s92, 0
buffer_load_dwordx4 v[204:207], v1, s[8:11], s73 offen offset:2048 sc1
s_add_u32 s73, s92, 4096
buffer_load_dwordx4 v[208:211], v1, s[8:11], s73 offen offset:2048 sc1
s_add_u32 s73, s92, 8192
buffer_load_dwordx4 v[212:215], v1, s[8:11], s73 offen offset:2048 sc1
s_waitcnt vmcnt(0)
v_max3_u32 v18, v185, v187, v189
v_max_u32 v18, v18, v191
v_cmp_eq_u32 vcc, -1, v18
s_cbranch_vccz .LB_106_%=
s_sub_u32 s76, s76, 1
s_cbranch_scc0 .LS_105_%=
s_mov_b32 s76, 0
s_branch .LB_106_%=
.LS_108_%=:
s_nop 7
s_add_u32 s73, s92, 53248
buffer_load_dwordx4 v[192:195], v1, s[8:11], s73 offen offset:2048 sc1
s_add_u32 s73, s92, 57344
buffer_load_dwordx4 v[196:199], v1, s[8:11], s73 offen offset:2048 sc1
s_add_u32 s73, s92, 61440
buffer_load_dwordx4 v[200:203], v1, s[8:11], s73 offen offset:2048 sc1
s_add_u32 s73, s92, 0
buffer_load_dwordx4 v[204:207], v1, s[8:11], s73 offen offset:2048 sc1
s_add_u32 s73, s92, 4096
buffer_load_dwordx4 v[208:211], v1, s[8:11], s73 offen offset:2048 sc1
s_add_u32 s73, s92, 8192
buffer_load_dwordx4 v[212:215], v1, s[8:11], s73 offen offset:2048 sc1
s_waitcnt vmcnt(0)
v_max3_u32 v18, v193, v195, v197
v_max_u32 v18, v18, v199
v_cmp_eq_u32 vcc, -1, v18
s_cbranch_vccz .LB_109_%=
s_sub_u32 s76, s76, 1
s_cbranch_scc0 .LS_108_%=
s_mov_b32 s76, 0
s_branch .LB_109_%=
.LS_111_%=:
s_nop 7
s_add_u32 s73, s92, 61440
buffer_load_dwordx4 v[200:203], v1, s[8:11], s73 offen offset:2048 sc1
s_add_u32 s73, s92, 0
buffer_load_dwordx4 v[204:207], v1, s[8:11], s73 offen offset:2048 sc1
s_add_u32 s73, s92, 4096
buffer_load_dwordx4 v[208:211], v1, s[8:11], s73 offen offset:2048 sc1
s_add_u32 s73, s92, 8192
buffer_load_dwordx4 v[212:215], v1, s[8:11], s73 offen offset:2048 sc1
s_waitcnt vmcnt(0)
v_max3_u32 v18, v201, v203, v205
v_max_u32 v18, v18, v207
v_cmp_eq_u32 vcc, -1, v18
s_cbranch_vccz .LB_112_%=
s_sub_u32 s76, s76, 1
s_cbranch_scc0 .LS_111_%=
s_mov_b32 s76, 0
s_branch .LB_112_%=
.LS_114_%=:
s_nop 7
s_add_u32 s73, s92, 4096
buffer_load_dwordx4 v[208:211], v1, s[8:11], s73 offen offset:2048 sc1
s_add_u32 s73, s92, 8192
buffer_load_dwordx4 v[212:215], v1, s[8:11], s73 offen offset:2048 sc1
s_waitcnt vmcnt(0)
v_max3_u32 v18, v209, v211, v213
v_max_u32 v18, v18, v215
v_cmp_eq_u32 vcc, -1, v18
s_cbranch_vccz .LB_115_%=
s_sub_u32 s76, s76, 1
s_cbranch_scc0 .LS_114_%=
s_mov_b32 s76, 0
s_branch .LB_115_%=
.LS_117_%=:
s_nop 7
s_add_u32 s73, s92, 45056
buffer_load_dwordx4 v[160:163], v1, s[8:11], s73 offen offset:3072 sc1
s_add_u32 s73, s92, 49152
buffer_load_dwordx4 v[164:167], v1, s[8:11], s73 offen offset:3072 sc1
s_add_u32 s73, s92, 53248
buffer_load_dwordx4 v[168:171], v1, s[8:11], s73 offen offset:3072 sc1
s_add_u32 s73, s92, 57344
buffer_load_dwordx4 v[172:175], v1, s[8:11], s73 offen offset:3072 sc1
s_add_u32 s73, s92, 61440
buffer_load_dwordx4 v[176:179], v1, s[8:11], s73 offen offset:3072 sc1
s_add_u32 s73, s92, 0
buffer_load_dwordx4 v[180:183], v1, s[8:11], s73 offen offset:3072 sc1
s_add_u32 s73, s92, 4096
buffer_load_dwordx4 v[184:187], v1, s[8:11], s73 offen offset:3072 sc1
s_add_u32 s73, s92, 8192
buffer_load_dwordx4 v[188:191], v1, s[8:11], s73 offen offset:3072 sc1
s_waitcnt vmcnt(0)
v_max3_u32 v18, v161, v163, v165
v_max_u32 v18, v18, v167
v_cmp_eq_u32 vcc, -1, v18
s_cbranch_vccz .LB_118_%=
s_sub_u32 s76, s76, 1
s_cbranch_scc0 .LS_117_%=
s_mov_b32 s76, 0
s_branch .LB_118_%=
.LS_120_%=:
s_nop 7
s_add_u32 s73, s92, 53248
buffer_load_dwordx4 v[168:171], v1, s[8:11], s73 offen offset:3072 sc1
s_add_u32 s73, s92, 57344
buffer_load_dwordx4 v[172:175], v1, s[8:11], s73 offen offset:3072 sc1
s_add_u32 s73, s92, 61440
buffer_load_dwordx4 v[176:179], v1, s[8:11], s73 offen offset:3072 sc1
s_add_u32 s73, s92, 0
buffer_load_dwordx4 v[180:183], v1, s[8:11], s73 offen offset:3072 sc1
s_add_u32 s73, s92, 4096
buffer_load_dwordx4 v[184:187], v1, s[8:11], s73 offen offset:3072 sc1
s_add_u32 s73, s92, 8192
buffer_load_dwordx4 v[188:191], v1, s[8:11], s73 offen offset:3072 sc1
s_waitcnt vmcnt(0)
v_max3_u32 v18, v169, v171, v173
v_max_u32 v18, v18, v175
v_cmp_eq_u32 vcc, -1, v18
s_cbranch_vccz .LB_121_%=
s_sub_u32 s76, s76, 1
s_cbranch_scc0 .LS_120_%=
s_mov_b32 s76, 0
s_branch .LB_121_%=
.LS_123_%=:
s_nop 7
s_add_u32 s73, s92, 61440
buffer_load_dwordx4 v[176:179], v1, s[8:11], s73 offen offset:3072 sc1
s_add_u32 s73, s92, 0
buffer_load_dwordx4 v[180:183], v1, s[8:11], s73 offen offset:3072 sc1
s_add_u32 s73, s92, 4096
buffer_load_dwordx4 v[184:187], v1, s[8:11], s73 offen offset:3072 sc1
s_add_u32 s73, s92, 8192
buffer_load_dwordx4 v[188:191], v1, s[8:11], s73 offen offset:3072 sc1
s_waitcnt vmcnt(0)
v_max3_u32 v18, v177, v179, v181
v_max_u32 v18, v18, v183
v_cmp_eq_u32 vcc, -1, v18
s_cbranch_vccz .LB_124_%=
s_sub_u32 s76, s76, 1
s_cbranch_scc0 .LS_123_%=
s_mov_b32 s76, 0
s_branch .LB_124_%=
.LS_126_%=:
s_nop 7
s_add_u32 s73, s92, 4096
buffer_load_dwordx4 v[184:187], v1, s[8:11], s73 offen offset:3072 sc1
s_add_u32 s73, s92, 8192
buffer_load_dwordx4 v[188:191], v1, s[8:11], s73 offen offset:3072 sc1
s_waitcnt vmcnt(0)
v_max3_u32 v18, v185, v187, v189
v_max_u32 v18, v18, v191
v_cmp_eq_u32 vcc, -1, v18
s_cbranch_vccz .LB_127_%=
s_sub_u32 s76, s76, 1
s_cbranch_scc0 .LS_126_%=
s_mov_b32 s76, 0
s_branch .LB_127_%=
.LS_130_%=:
buffer_load_dwordx2 v[22:23], v24, s[4:7], s72 offen sc1
s_waitcnt vmcnt(0)
v_cmp_ne_u64 s[68:69], s[62:63], v[22:23]
s_nop 4
s_cmp_eq_u64 s[68:69], 0
s_cbranch_scc1 .LD_131_%=
s_sub_u32 s76, s76, 1
s_cbranch_scc0 .LS_130_%=
s_mov_b32 s76, 0
s_branch .LD_131_%=
.LS_132_%=:
s_nop 7
s_add_u32 s73, s85, 0
buffer_load_dwordx4 v[160:163], v1, s[4:7], s73 offen offset:0 sc1
s_add_u32 s73, s85, 0
buffer_load_dwordx4 v[164:167], v1, s[4:7], s73 offen offset:1024 sc1
s_add_u32 s73, s85, 4096
buffer_load_dwordx4 v[168:171], v1, s[4:7], s73 offen offset:0 sc1
s_add_u32 s73, s85, 4096
buffer_load_dwordx4 v[172:175], v1, s[4:7], s73 offen offset:1024 sc1
s_add_u32 s73, s85, 8192
buffer_load_dwordx4 v[176:179], v1, s[4:7], s73 offen offset:0 sc1
s_add_u32 s73, s85, 8192
buffer_load_dwordx4 v[180:183], v1, s[4:7], s73 offen offset:1024 sc1
s_add_u32 s73, s85, 12288
buffer_load_dwordx4 v[184:187], v1, s[4:7], s73 offen offset:0 sc1
s_add_u32 s73, s85, 12288
buffer_load_dwordx4 v[188:191], v1, s[4:7], s73 offen offset:1024 sc1
s_waitcnt vmcnt(0)
v_max3_u32 v18, v161, v163, v165
v_max_u32 v18, v18, v167
v_cmp_eq_u32 vcc, -1, v18
s_cbranch_vccz .LB_133_%=
s_sub_u32 s76, s76, 1
s_cbranch_scc0 .LS_132_%=
s_mov_b32 s76, 0
s_branch .LB_133_%=
.LS_135_%=:
s_nop 7
s_add_u32 s73, s85, 4096
buffer_load_dwordx4 v[168:171], v1, s[4:7], s73 offen offset:0 sc1
s_add_u32 s73, s85, 4096
buffer_load_dwordx4 v[172:175], v1, s[4:7], s73 offen offset:1024 sc1
s_add_u32 s73, s85, 8192
buffer_load_dwordx4 v[176:179], v1, s[4:7], s73 offen offset:0 sc1
s_add_u32 s73, s85, 8192
buffer_load_dwordx4 v[180:183], v1, s[4:7], s73 offen offset:1024 sc1
s_add_u32 s73, s85, 12288
buffer_load_dwordx4 v[184:187], v1, s[4:7], s73 offen offset:0 sc1
s_add_u32 s73, s85, 12288
buffer_load_dwordx4 v[188:191], v1, s[4:7], s73 offen offset:1024 sc1
s_waitcnt vmcnt(0)
v_max3_u32 v18, v169, v171, v173
v_max_u32 v18, v18, v175
v_cmp_eq_u32 vcc, -1, v18
s_cbranch_vccz .LB_136_%=
s_sub_u32 s76, s76, 1
s_cbranch_scc0 .LS_135_%=
s_mov_b32 s76, 0
s_branch .LB_136_%=
.LS_138_%=:
s_nop 7
s_add_u32 s73, s85, 8192
buffer_load_dwordx4 v[176:179], v1, s[4:7], s73 offen offset:0 sc1
s_add_u32 s73, s85, 8192
buffer_load_dwordx4 v[180:183], v1, s[4:7], s73 offen offset:1024 sc1
s_add_u32 s73, s85, 12288
buffer_load_dwordx4 v[184:187], v1, s[4:7], s73 offen offset:0 sc1
s_add_u32 s73, s85, 12288
buffer_load_dwordx4 v[188:191], v1, s[4:7], s73 offen offset:1024 sc1
s_waitcnt vmcnt(0)
v_max3_u32 v18, v177, v179, v181
v_max_u32 v18, v18, v183
v_cmp_eq_u32 vcc, -1, v18
s_cbranch_vccz .LB_139_%=
s_sub_u32 s76, s76, 1
s_cbranch_scc0 .LS_138_%=
s_mov_b32 s76, 0
s_branch .LB_139_%=
.LS_141_%=:
s_nop 7
s_add_u32 s73, s85, 12288
buffer_load_dwordx4 v[184:187], v1, s[4:7], s73 offen offset:0 sc1
s_add_u32 s73, s85, 12288
buffer_load_dwordx4 v[188:191], v1, s[4:7], s73 offen offset:1024 sc1
s_waitcnt vmcnt(0)
v_max3_u32 v18, v185, v187, v189
v_max_u32 v18, v18, v191
v_cmp_eq_u32 vcc, -1, v18
s_cbranch_vccz .LB_142_%=
s_sub_u32 s76, s76, 1
s_cbranch_scc0 .LS_141_%=
s_mov_b32 s76, 0
s_branch .LB_142_%=
.LS_144_%=:
s_nop 7
s_add_u32 s73, s85, 16384
buffer_load_dwordx4 v[192:195], v1, s[4:7], s73 offen offset:0 sc1
s_add_u32 s73, s85, 16384
buffer_load_dwordx4 v[196:199], v1, s[4:7], s73 offen offset:1024 sc1
s_add_u32 s73, s85, 20480
buffer_load_dwordx4 v[200:203], v1, s[4:7], s73 offen offset:0 sc1
s_add_u32 s73, s85, 20480
buffer_load_dwordx4 v[204:207], v1, s[4:7], s73 offen offset:1024 sc1
s_add_u32 s73, s85, 24576
buffer_load_dwordx4 v[208:211], v1, s[4:7], s73 offen offset:0 sc1
s_add_u32 s73, s85, 24576
buffer_load_dwordx4 v[212:215], v1, s[4:7], s73 offen offset:1024 sc1
s_add_u32 s73, s85, 28672
buffer_load_dwordx4 v[160:163], v1, s[4:7], s73 offen offset:0 sc1
s_add_u32 s73, s85, 32768
buffer_load_dwordx4 v[164:167], v1, s[4:7], s73 offen offset:0 sc1
s_waitcnt vmcnt(0)
v_max3_u32 v18, v193, v195, v197
v_max_u32 v18, v18, v199
v_cmp_eq_u32 vcc, -1, v18
s_cbranch_vccz .LB_145_%=
s_sub_u32 s76, s76, 1
s_cbranch_scc0 .LS_144_%=
s_mov_b32 s76, 0
s_branch .LB_145_%=
.LS_147_%=:
s_nop 7
s_add_u32 s73, s85, 20480
buffer_load_dwordx4 v[200:203], v1, s[4:7], s73 offen offset:0 sc1
s_add_u32 s73, s85, 20480
buffer_load_dwordx4 v[204:207], v1, s[4:7], s73 offen offset:1024 sc1
s_add_u32 s73, s85, 24576
buffer_load_dwordx4 v[208:211], v1, s[4:7], s73 offen offset:0 sc1
s_add_u32 s73, s85, 24576
buffer_load_dwordx4 v[212:215], v1, s[4:7], s73 offen offset:1024 sc1
s_add_u32 s73, s85, 28672
buffer_load_dwordx4 v[160:163], v1, s[4:7], s73 offen offset:0 sc1
s_add_u32 s73, s85, 32768
buffer_load_dwordx4 v[164:167], v1, s[4:7], s73 offen offset:0 sc1
s_waitcnt vmcnt(0)
v_max3_u32 v18, v201, v203, v205
v_max_u32 v18, v18, v207
v_cmp_eq_u32 vcc, -1, v18
s_cbranch_vccz .LB_148_%=
s_sub_u32 s76, s76, 1
s_cbranch_scc0 .LS_147_%=
s_mov_b32 s76, 0
s_branch .LB_148_%=
.LS_150_%=:
s_nop 7
s_add_u32 s73, s85, 24576
buffer_load_dwordx4 v[208:211], v1, s[4:7], s73 offen offset:0 sc1
s_add_u32 s73, s85, 24576
buffer_load_dwordx4 v[212:215], v1, s[4:7], s73 offen offset:1024 sc1
s_add_u32 s73, s85, 28672
buffer_load_dwordx4 v[160:163], v1, s[4:7], s73 offen offset:0 sc1
s_add_u32 s73, s85, 32768
buffer_load_dwordx4 v[164:167], v1, s[4:7], s73 offen offset:0 sc1
s_waitcnt vmcnt(0)
v_max3_u32 v18, v209, v211, v213
v_max_u32 v18, v18, v215
v_cmp_eq_u32 vcc, -1, v18
s_cbranch_vccz .LB_151_%=
s_sub_u32 s76, s76, 1
s_cbranch_scc0 .LS_150_%=
s_mov_b32 s76, 0
s_branch .LB_151_%=
.LS_153_%=:
s_nop 7
s_add_u32 s73, s85, 28672
buffer_load_dwordx4 v[160:163], v1, s[4:7], s73 offen offset:0 sc1
s_add_u32 s73, s85, 32768
buffer_load_dwordx4 v[164:167], v1, s[4:7], s73 offen offset:0 sc1
s_waitcnt vmcnt(0)
v_max3_u32 v18, v161, v163, v165
v_max_u32 v18, v18, v167
v_cmp_eq_u32 vcc, -1, v18
s_cbranch_vccz .LB_154_%=
s_sub_u32 s76, s76, 1
s_cbranch_scc0 .LS_153_%=
s_mov_b32 s76, 0
s_branch .LB_154_%=
.LS_156_%=:
s_nop 7
s_add_u32 s73, s85, 0
buffer_load_dwordx4 v[168:171], v1, s[4:7], s73 offen offset:2048 sc1
s_add_u32 s73, s85, 0
buffer_load_dwordx4 v[172:175], v1, s[4:7], s73 offen offset:3072 sc1
s_add_u32 s73, s85, 4096
buffer_load_dwordx4 v[176:179], v1, s[4:7], s73 offen offset:2048 sc1
s_add_u32 s73, s85, 4096
buffer_load_dwordx4 v[180:183], v1, s[4:7], s73 offen offset:3072 sc1
s_add_u32 s73, s85, 8192
buffer_load_dwordx4 v[184:187], v1, s[4:7], s73 offen offset:2048 sc1
s_add_u32 s73, s85, 8192
buffer_load_dwordx4 v[188:191], v1, s[4:7], s73 offen offset:3072 sc1
s_add_u32 s73, s85, 12288
buffer_load_dwordx4 v[192:195], v1, s[4:7], s73 offen offset:2048 sc1
s_add_u32 s73, s85, 12288
buffer_load_dwordx4 v[196:199], v1, s[4:7], s73 offen offset:3072 sc1
s_waitcnt vmcnt(0)
v_max3_u32 v18, v169, v171, v173
)ASM"
R"ASM(
v_max_u32 v18, v18, v175
v_cmp_eq_u32 vcc, -1, v18
s_cbranch_vccz .LB_157_%=
s_sub_u32 s76, s76, 1
s_cbranch_scc0 .LS_156_%=
s_mov_b32 s76, 0
s_branch .LB_157_%=
.LS_159_%=:
s_nop 7
s_add_u32 s73, s85, 4096
buffer_load_dwordx4 v[176:179], v1, s[4:7], s73 offen offset:2048 sc1
s_add_u32 s73, s85, 4096
buffer_load_dwordx4 v[180:183], v1, s[4:7], s73 offen offset:3072 sc1
s_add_u32 s73, s85, 8192
buffer_load_dwordx4 v[184:187], v1, s[4:7], s73 offen offset:2048 sc1
s_add_u32 s73, s85, 8192
buffer_load_dwordx4 v[188:191], v1, s[4:7], s73 offen offset:3072 sc1
s_add_u32 s73, s85, 12288
buffer_load_dwordx4 v[192:195], v1, s[4:7], s73 offen offset:2048 sc1
s_add_u32 s73, s85, 12288
buffer_load_dwordx4 v[196:199], v1, s[4:7], s73 offen offset:3072 sc1
s_waitcnt vmcnt(0)
v_max3_u32 v18, v177, v179, v181
v_max_u32 v18, v18, v183
v_cmp_eq_u32 vcc, -1, v18
s_cbranch_vccz .LB_160_%=
s_sub_u32 s76, s76, 1
s_cbranch_scc0 .LS_159_%=
s_mov_b32 s76, 0
s_branch .LB_160_%=
.LS_162_%=:
s_nop 7
s_add_u32 s73, s85, 8192
buffer_load_dwordx4 v[184:187], v1, s[4:7], s73 offen offset:2048 sc1
s_add_u32 s73, s85, 8192
buffer_load_dwordx4 v[188:191], v1, s[4:7], s73 offen offset:3072 sc1
s_add_u32 s73, s85, 12288
buffer_load_dwordx4 v[192:195], v1, s[4:7], s73 offen offset:2048 sc1
s_add_u32 s73, s85, 12288
buffer_load_dwordx4 v[196:199], v1, s[4:7], s73 offen offset:3072 sc1
s_waitcnt vmcnt(0)
v_max3_u32 v18, v185, v187, v189
v_max_u32 v18, v18, v191
v_cmp_eq_u32 vcc, -1, v18
s_cbranch_vccz .LB_163_%=
s_sub_u32 s76, s76, 1
s_cbranch_scc0 .LS_162_%=
s_mov_b32 s76, 0
s_branch .LB_163_%=
.LS_165_%=:
s_nop 7
s_add_u32 s73, s85, 12288
buffer_load_dwordx4 v[192:195], v1, s[4:7], s73 offen offset:2048 sc1
s_add_u32 s73, s85, 12288
buffer_load_dwordx4 v[196:199], v1, s[4:7], s73 offen offset:3072 sc1
s_waitcnt vmcnt(0)
v_max3_u32 v18, v193, v195, v197
v_max_u32 v18, v18, v199
v_cmp_eq_u32 vcc, -1, v18
s_cbranch_vccz .LB_166_%=
s_sub_u32 s76, s76, 1
s_cbranch_scc0 .LS_165_%=
s_mov_b32 s76, 0
s_branch .LB_166_%=
.LS_168_%=:
s_nop 7
s_add_u32 s73, s85, 16384
buffer_load_dwordx4 v[200:203], v1, s[4:7], s73 offen offset:2048 sc1
s_add_u32 s73, s85, 16384
buffer_load_dwordx4 v[204:207], v1, s[4:7], s73 offen offset:3072 sc1
s_add_u32 s73, s85, 20480
buffer_load_dwordx4 v[208:211], v1, s[4:7], s73 offen offset:2048 sc1
s_add_u32 s73, s85, 20480
buffer_load_dwordx4 v[212:215], v1, s[4:7], s73 offen offset:3072 sc1
s_add_u32 s73, s85, 24576
buffer_load_dwordx4 v[160:163], v1, s[4:7], s73 offen offset:2048 sc1
s_add_u32 s73, s85, 24576
buffer_load_dwordx4 v[164:167], v1, s[4:7], s73 offen offset:3072 sc1
s_add_u32 s73, s85, 36864
buffer_load_dwordx4 v[168:171], v1, s[4:7], s73 offen offset:0 sc1
s_add_u32 s73, s85, 40960
buffer_load_dwordx4 v[172:175], v1, s[4:7], s73 offen offset:0 sc1
s_waitcnt vmcnt(0)
v_max3_u32 v18, v201, v203, v205
v_max_u32 v18, v18, v207
v_cmp_eq_u32 vcc, -1, v18
s_cbranch_vccz .LB_169_%=
s_sub_u32 s76, s76, 1
s_cbranch_scc0 .LS_168_%=
s_mov_b32 s76, 0
s_branch .LB_169_%=
.LS_171_%=:
s_nop 7
s_add_u32 s73, s85, 20480
buffer_load_dwordx4 v[208:211], v1, s[4:7], s73 offen offset:2048 sc1
s_add_u32 s73, s85, 20480
buffer_load_dwordx4 v[212:215], v1, s[4:7], s73 offen offset:3072 sc1
s_add_u32 s73, s85, 24576
buffer_load_dwordx4 v[160:163], v1, s[4:7], s73 offen offset:2048 sc1
s_add_u32 s73, s85, 24576
buffer_load_dwordx4 v[164:167], v1, s[4:7], s73 offen offset:3072 sc1
s_add_u32 s73, s85, 36864
buffer_load_dwordx4 v[168:171], v1, s[4:7], s73 offen offset:0 sc1
s_add_u32 s73, s85, 40960
buffer_load_dwordx4 v[172:175], v1, s[4:7], s73 offen offset:0 sc1
s_waitcnt vmcnt(0)
v_max3_u32 v18, v209, v211, v213
v_max_u32 v18, v18, v215
v_cmp_eq_u32 vcc, -1, v18
s_cbranch_vccz .LB_172_%=
s_sub_u32 s76, s76, 1
s_cbranch_scc0 .LS_171_%=
s_mov_b32 s76, 0
s_branch .LB_172_%=
.LS_174_%=:
s_nop 7
s_add_u32 s73, s85, 24576
buffer_load_dwordx4 v[160:163], v1, s[4:7], s73 offen offset:2048 sc1
s_add_u32 s73, s85, 24576
buffer_load_dwordx4 v[164:167], v1, s[4:7], s73 offen offset:3072 sc1
s_add_u32 s73, s85, 36864
buffer_load_dwordx4 v[168:171], v1, s[4:7], s73 offen offset:0 sc1
s_add_u32 s73, s85, 40960
buffer_load_dwordx4 v[172:175], v1, s[4:7], s73 offen offset:0 sc1
s_waitcnt vmcnt(0)
v_max3_u32 v18, v161, v163, v165
v_max_u32 v18, v18, v167
v_cmp_eq_u32 vcc, -1, v18
s_cbranch_vccz .LB_175_%=
s_sub_u32 s76, s76, 1
s_cbranch_scc0 .LS_174_%=
s_mov_b32 s76, 0
s_branch .LB_175_%=
.LS_177_%=:
s_nop 7
s_add_u32 s73, s85, 36864
buffer_load_dwordx4 v[168:171], v1, s[4:7], s73 offen offset:0 sc1
s_add_u32 s73, s85, 40960
buffer_load_dwordx4 v[172:175], v1, s[4:7], s73 offen offset:0 sc1
s_waitcnt vmcnt(0)
v_max3_u32 v18, v169, v171, v173
v_max_u32 v18, v18, v175
v_cmp_eq_u32 vcc, -1, v18
s_cbranch_vccz .LB_178_%=
s_sub_u32 s76, s76, 1
s_cbranch_scc0 .LS_177_%=
s_mov_b32 s76, 0
s_branch .LB_178_%=
.LS_180_%=:
s_nop 7
s_add_u32 s73, s85, 45056
buffer_load_dwordx4 v[176:179], v1, s[4:7], s73 offen offset:0 sc1
s_add_u32 s73, s85, 49152
buffer_load_dwordx4 v[180:183], v1, s[4:7], s73 offen offset:0 sc1
s_add_u32 s73, s85, 53248
buffer_load_dwordx4 v[184:187], v1, s[4:7], s73 offen offset:0 sc1
s_add_u32 s73, s85, 28672
buffer_load_dwordx4 v[188:191], v1, s[4:7], s73 offen offset:1024 sc1
s_add_u32 s73, s85, 32768
buffer_load_dwordx4 v[192:195], v1, s[4:7], s73 offen offset:1024 sc1
s_add_u32 s73, s85, 36864
buffer_load_dwordx4 v[196:199], v1, s[4:7], s73 offen offset:1024 sc1
s_add_u32 s73, s85, 40960
buffer_load_dwordx4 v[200:203], v1, s[4:7], s73 offen offset:1024 sc1
s_add_u32 s73, s85, 45056
buffer_load_dwordx4 v[204:207], v1, s[4:7], s73 offen offset:1024 sc1
s_waitcnt vmcnt(0)
v_max3_u32 v18, v177, v179, v181
v_max_u32 v18, v18, v183
v_cmp_eq_u32 vcc, -1, v18
s_cbranch_vccz .LB_181_%=
s_sub_u32 s76, s76, 1
s_cbranch_scc0 .LS_180_%=
s_mov_b32 s76, 0
s_branch .LB_181_%=
.LS_183_%=:
s_nop 7
s_add_u32 s73, s85, 53248
buffer_load_dwordx4 v[184:187], v1, s[4:7], s73 offen offset:0 sc1
s_add_u32 s73, s85, 28672
buffer_load_dwordx4 v[188:191], v1, s[4:7], s73 offen offset:1024 sc1
s_add_u32 s73, s85, 32768
buffer_load_dwordx4 v[192:195], v1, s[4:7], s73 offen offset:1024 sc1
s_add_u32 s73, s85, 36864
buffer_load_dwordx4 v[196:199], v1, s[4:7], s73 offen offset:1024 sc1
s_add_u32 s73, s85, 40960
buffer_load_dwordx4 v[200:203], v1, s[4:7], s73 offen offset:1024 sc1
s_add_u32 s73, s85, 45056
buffer_load_dwordx4 v[204:207], v1, s[4:7], s73 offen offset:1024 sc1
s_waitcnt vmcnt(0)
v_max3_u32 v18, v185, v187, v189
v_max_u32 v18, v18, v191
v_cmp_eq_u32 vcc, -1, v18
s_cbranch_vccz .LB_184_%=
s_sub_u32 s76, s76, 1
s_cbranch_scc0 .LS_183_%=
s_mov_b32 s76, 0
s_branch .LB_184_%=
.LS_186_%=:
s_nop 7
s_add_u32 s73, s85, 32768
buffer_load_dwordx4 v[192:195], v1, s[4:7], s73 offen offset:1024 sc1
s_add_u32 s73, s85, 36864
buffer_load_dwordx4 v[196:199], v1, s[4:7], s73 offen offset:1024 sc1
s_add_u32 s73, s85, 40960
buffer_load_dwordx4 v[200:203], v1, s[4:7], s73 offen offset:1024 sc1
s_add_u32 s73, s85, 45056
buffer_load_dwordx4 v[204:207], v1, s[4:7], s73 offen offset:1024 sc1
s_waitcnt vmcnt(0)
v_max3_u32 v18, v193, v195, v197
v_max_u32 v18, v18, v199
v_cmp_eq_u32 vcc, -1, v18
s_cbranch_vccz .LB_187_%=
s_sub_u32 s76, s76, 1
s_cbranch_scc0 .LS_186_%=
s_mov_b32 s76, 0
s_branch .LB_187_%=
.LS_189_%=:
s_nop 7
s_add_u32 s73, s85, 40960
buffer_load_dwordx4 v[200:203], v1, s[4:7], s73 offen offset:1024 sc1
s_add_u32 s73, s85, 45056
buffer_load_dwordx4 v[204:207], v1, s[4:7], s73 offen offset:1024 sc1
s_waitcnt vmcnt(0)
v_max3_u32 v18, v201, v203, v205
v_max_u32 v18, v18, v207
v_cmp_eq_u32 vcc, -1, v18
s_cbranch_vccz .LB_190_%=
s_sub_u32 s76, s76, 1
s_cbranch_scc0 .LS_189_%=
s_mov_b32 s76, 0
s_branch .LB_190_%=
.LS_192_%=:
s_nop 7
s_add_u32 s73, s85, 49152
buffer_load_dwordx4 v[208:211], v1, s[4:7], s73 offen offset:1024 sc1
s_add_u32 s73, s85, 53248
buffer_load_dwordx4 v[212:215], v1, s[4:7], s73 offen offset:1024 sc1
s_add_u32 s73, s85, 28672
buffer_load_dwordx4 v[160:163], v1, s[4:7], s73 offen offset:2048 sc1
s_add_u32 s73, s85, 32768
buffer_load_dwordx4 v[164:167], v1, s[4:7], s73 offen offset:2048 sc1
s_add_u32 s73, s85, 36864
buffer_load_dwordx4 v[168:171], v1, s[4:7], s73 offen offset:2048 sc1
s_add_u32 s73, s85, 40960
buffer_load_dwordx4 v[172:175], v1, s[4:7], s73 offen offset:2048 sc1
s_add_u32 s73, s85, 45056
buffer_load_dwordx4 v[176:179], v1, s[4:7], s73 offen offset:2048 sc1
s_add_u32 s73, s85, 49152
buffer_load_dwordx4 v[180:183], v1, s[4:7], s73 offen offset:2048 sc1
s_waitcnt vmcnt(0)
v_max3_u32 v18, v209, v211, v213
v_max_u32 v18, v18, v215
v_cmp_eq_u32 vcc, -1, v18
s_cbranch_vccz .LB_193_%=
s_sub_u32 s76, s76, 1
s_cbranch_scc0 .LS_192_%=
s_mov_b32 s76, 0
s_branch .LB_193_%=
.LS_195_%=:
s_nop 7
s_add_u32 s73, s85, 28672
buffer_load_dwordx4 v[160:163], v1, s[4:7], s73 offen offset:2048 sc1
s_add_u32 s73, s85, 32768
buffer_load_dwordx4 v[164:167], v1, s[4:7], s73 offen offset:2048 sc1
s_add_u32 s73, s85, 36864
buffer_load_dwordx4 v[168:171], v1, s[4:7], s73 offen offset:2048 sc1
s_add_u32 s73, s85, 40960
buffer_load_dwordx4 v[172:175], v1, s[4:7], s73 offen offset:2048 sc1
s_add_u32 s73, s85, 45056
buffer_load_dwordx4 v[176:179], v1, s[4:7], s73 offen offset:2048 sc1
s_add_u32 s73, s85, 49152
buffer_load_dwordx4 v[180:183], v1, s[4:7], s73 offen offset:2048 sc1
s_waitcnt vmcnt(0)
v_max3_u32 v18, v161, v163, v165
v_max_u32 v18, v18, v167
v_cmp_eq_u32 vcc, -1, v18
s_cbranch_vccz .LB_196_%=
s_sub_u32 s76, s76, 1
s_cbranch_scc0 .LS_195_%=
s_mov_b32 s76, 0
s_branch .LB_196_%=
.LS_198_%=:
s_nop 7
s_add_u32 s73, s85, 36864
buffer_load_dwordx4 v[168:171], v1, s[4:7], s73 offen offset:2048 sc1
s_add_u32 s73, s85, 40960
buffer_load_dwordx4 v[172:175], v1, s[4:7], s73 offen offset:2048 sc1
s_add_u32 s73, s85, 45056
buffer_load_dwordx4 v[176:179], v1, s[4:7], s73 offen offset:2048 sc1
s_add_u32 s73, s85, 49152
buffer_load_dwordx4 v[180:183], v1, s[4:7], s73 offen offset:2048 sc1
s_waitcnt vmcnt(0)
v_max3_u32 v18, v169, v171, v173
v_max_u32 v18, v18, v175
v_cmp_eq_u32 vcc, -1, v18
s_cbranch_vccz .LB_199_%=
s_sub_u32 s76, s76, 1
s_cbranch_scc0 .LS_198_%=
s_mov_b32 s76, 0
s_branch .LB_199_%=
.LS_201_%=:
s_nop 7
s_add_u32 s73, s85, 45056
buffer_load_dwordx4 v[176:179], v1, s[4:7], s73 offen offset:2048 sc1
s_add_u32 s73, s85, 49152
buffer_load_dwordx4 v[180:183], v1, s[4:7], s73 offen offset:2048 sc1
s_waitcnt vmcnt(0)
v_max3_u32 v18, v177, v179, v181
v_max_u32 v18, v18, v183
v_cmp_eq_u32 vcc, -1, v18
s_cbranch_vccz .LB_202_%=
s_sub_u32 s76, s76, 1
s_cbranch_scc0 .LS_201_%=
s_mov_b32 s76, 0
s_branch .LB_202_%=
.LS_204_%=:
s_nop 7
s_add_u32 s73, s85, 53248
buffer_load_dwordx4 v[184:187], v1, s[4:7], s73 offen offset:2048 sc1
s_add_u32 s73, s85, 28672
buffer_load_dwordx4 v[188:191], v1, s[4:7], s73 offen offset:3072 sc1
s_add_u32 s73, s85, 32768
buffer_load_dwordx4 v[192:195], v1, s[4:7], s73 offen offset:3072 sc1
s_add_u32 s73, s85, 36864
buffer_load_dwordx4 v[196:199], v1, s[4:7], s73 offen offset:3072 sc1
s_add_u32 s73, s85, 40960
buffer_load_dwordx4 v[200:203], v1, s[4:7], s73 offen offset:3072 sc1
s_add_u32 s73, s85, 45056
buffer_load_dwordx4 v[204:207], v1, s[4:7], s73 offen offset:3072 sc1
s_add_u32 s73, s85, 49152
buffer_load_dwordx4 v[208:211], v1, s[4:7], s73 offen offset:3072 sc1
s_add_u32 s73, s85, 53248
buffer_load_dwordx4 v[212:215], v1, s[4:7], s73 offen offset:3072 sc1
s_waitcnt vmcnt(0)
v_max3_u32 v18, v185, v187, v189
v_max_u32 v18, v18, v191
v_cmp_eq_u32 vcc, -1, v18
s_cbranch_vccz .LB_205_%=
s_sub_u32 s76, s76, 1
s_cbranch_scc0 .LS_204_%=
s_mov_b32 s76, 0
s_branch .LB_205_%=
.LS_207_%=:
s_nop 7
s_add_u32 s73, s85, 32768
buffer_load_dwordx4 v[192:195], v1, s[4:7], s73 offen offset:3072 sc1
s_add_u32 s73, s85, 36864
buffer_load_dwordx4 v[196:199], v1, s[4:7], s73 offen offset:3072 sc1
s_add_u32 s73, s85, 40960
buffer_load_dwordx4 v[200:203], v1, s[4:7], s73 offen offset:3072 sc1
s_add_u32 s73, s85, 45056
buffer_load_dwordx4 v[204:207], v1, s[4:7], s73 offen offset:3072 sc1
s_add_u32 s73, s85, 49152
buffer_load_dwordx4 v[208:211], v1, s[4:7], s73 offen offset:3072 sc1
)ASM"
R"ASM(
s_add_u32 s73, s85, 53248
buffer_load_dwordx4 v[212:215], v1, s[4:7], s73 offen offset:3072 sc1
s_waitcnt vmcnt(0)
v_max3_u32 v18, v193, v195, v197
v_max_u32 v18, v18, v199
v_cmp_eq_u32 vcc, -1, v18
s_cbranch_vccz .LB_208_%=
s_sub_u32 s76, s76, 1
s_cbranch_scc0 .LS_207_%=
s_mov_b32 s76, 0
s_branch .LB_208_%=
.LS_210_%=:
s_nop 7
s_add_u32 s73, s85, 40960
buffer_load_dwordx4 v[200:203], v1, s[4:7], s73 offen offset:3072 sc1
s_add_u32 s73, s85, 45056
buffer_load_dwordx4 v[204:207], v1, s[4:7], s73 offen offset:3072 sc1
s_add_u32 s73, s85, 49152
buffer_load_dwordx4 v[208:211], v1, s[4:7], s73 offen offset:3072 sc1
s_add_u32 s73, s85, 53248
buffer_load_dwordx4 v[212:215], v1, s[4:7], s73 offen offset:3072 sc1
s_waitcnt vmcnt(0)
v_max3_u32 v18, v201, v203, v205
v_max_u32 v18, v18, v207
v_cmp_eq_u32 vcc, -1, v18
s_cbranch_vccz .LB_211_%=
s_sub_u32 s76, s76, 1
s_cbranch_scc0 .LS_210_%=
s_mov_b32 s76, 0
s_branch .LB_211_%=
.LS_213_%=:
s_nop 7
s_add_u32 s73, s85, 49152
buffer_load_dwordx4 v[208:211], v1, s[4:7], s73 offen offset:3072 sc1
s_add_u32 s73, s85, 53248
buffer_load_dwordx4 v[212:215], v1, s[4:7], s73 offen offset:3072 sc1
s_waitcnt vmcnt(0)
v_max3_u32 v18, v209, v211, v213
v_max_u32 v18, v18, v215
v_cmp_eq_u32 vcc, -1, v18
s_cbranch_vccz .LB_214_%=
s_sub_u32 s76, s76, 1
s_cbranch_scc0 .LS_213_%=
s_mov_b32 s76, 0
s_branch .LB_214_%=
.LS_216_%=:
s_nop 7
s_add_u32 s73, s85, 0
buffer_load_dwordx4 v[160:163], v1, s[4:7], s73 offen offset:0 sc1
s_add_u32 s73, s85, 0
buffer_load_dwordx4 v[164:167], v1, s[4:7], s73 offen offset:1024 sc1
s_add_u32 s73, s85, 4096
buffer_load_dwordx4 v[168:171], v1, s[4:7], s73 offen offset:0 sc1
s_add_u32 s73, s85, 4096
buffer_load_dwordx4 v[172:175], v1, s[4:7], s73 offen offset:1024 sc1
s_add_u32 s73, s85, 8192
buffer_load_dwordx4 v[176:179], v1, s[4:7], s73 offen offset:0 sc1
s_add_u32 s73, s85, 8192
buffer_load_dwordx4 v[180:183], v1, s[4:7], s73 offen offset:1024 sc1
s_add_u32 s73, s85, 12288
buffer_load_dwordx4 v[184:187], v1, s[4:7], s73 offen offset:0 sc1
s_add_u32 s73, s85, 12288
buffer_load_dwordx4 v[188:191], v1, s[4:7], s73 offen offset:1024 sc1
s_waitcnt vmcnt(0)
v_max3_u32 v18, v161, v163, v165
v_max_u32 v18, v18, v167
v_cmp_eq_u32 vcc, -1, v18
s_cbranch_vccz .LB_217_%=
s_sub_u32 s76, s76, 1
s_cbranch_scc0 .LS_216_%=
s_mov_b32 s76, 0
s_branch .LB_217_%=
.LS_219_%=:
s_nop 7
s_add_u32 s73, s85, 4096
buffer_load_dwordx4 v[168:171], v1, s[4:7], s73 offen offset:0 sc1
s_add_u32 s73, s85, 4096
buffer_load_dwordx4 v[172:175], v1, s[4:7], s73 offen offset:1024 sc1
s_add_u32 s73, s85, 8192
buffer_load_dwordx4 v[176:179], v1, s[4:7], s73 offen offset:0 sc1
s_add_u32 s73, s85, 8192
buffer_load_dwordx4 v[180:183], v1, s[4:7], s73 offen offset:1024 sc1
s_add_u32 s73, s85, 12288
buffer_load_dwordx4 v[184:187], v1, s[4:7], s73 offen offset:0 sc1
s_add_u32 s73, s85, 12288
buffer_load_dwordx4 v[188:191], v1, s[4:7], s73 offen offset:1024 sc1
s_waitcnt vmcnt(0)
v_max3_u32 v18, v169, v171, v173
v_max_u32 v18, v18, v175
v_cmp_eq_u32 vcc, -1, v18
s_cbranch_vccz .LB_220_%=
s_sub_u32 s76, s76, 1
s_cbranch_scc0 .LS_219_%=
s_mov_b32 s76, 0
s_branch .LB_220_%=
.LS_222_%=:
s_nop 7
s_add_u32 s73, s85, 8192
buffer_load_dwordx4 v[176:179], v1, s[4:7], s73 offen offset:0 sc1
s_add_u32 s73, s85, 8192
buffer_load_dwordx4 v[180:183], v1, s[4:7], s73 offen offset:1024 sc1
s_add_u32 s73, s85, 12288
buffer_load_dwordx4 v[184:187], v1, s[4:7], s73 offen offset:0 sc1
s_add_u32 s73, s85, 12288
buffer_load_dwordx4 v[188:191], v1, s[4:7], s73 offen offset:1024 sc1
s_waitcnt vmcnt(0)
v_max3_u32 v18, v177, v179, v181
v_max_u32 v18, v18, v183
v_cmp_eq_u32 vcc, -1, v18
s_cbranch_vccz .LB_223_%=
s_sub_u32 s76, s76, 1
s_cbranch_scc0 .LS_222_%=
s_mov_b32 s76, 0
s_branch .LB_223_%=
.LS_225_%=:
s_nop 7
s_add_u32 s73, s85, 12288
buffer_load_dwordx4 v[184:187], v1, s[4:7], s73 offen offset:0 sc1
s_add_u32 s73, s85, 12288
buffer_load_dwordx4 v[188:191], v1, s[4:7], s73 offen offset:1024 sc1
s_waitcnt vmcnt(0)
v_max3_u32 v18, v185, v187, v189
v_max_u32 v18, v18, v191
v_cmp_eq_u32 vcc, -1, v18
s_cbranch_vccz .LB_226_%=
s_sub_u32 s76, s76, 1
s_cbranch_scc0 .LS_225_%=
s_mov_b32 s76, 0
s_branch .LB_226_%=
.LS_228_%=:
s_nop 7
s_add_u32 s73, s85, 16384
buffer_load_dwordx4 v[192:195], v1, s[4:7], s73 offen offset:0 sc1
s_add_u32 s73, s85, 16384
buffer_load_dwordx4 v[196:199], v1, s[4:7], s73 offen offset:1024 sc1
s_add_u32 s73, s85, 20480
buffer_load_dwordx4 v[200:203], v1, s[4:7], s73 offen offset:0 sc1
s_add_u32 s73, s85, 20480
buffer_load_dwordx4 v[204:207], v1, s[4:7], s73 offen offset:1024 sc1
s_add_u32 s73, s85, 24576
buffer_load_dwordx4 v[208:211], v1, s[4:7], s73 offen offset:0 sc1
s_add_u32 s73, s85, 24576
buffer_load_dwordx4 v[212:215], v1, s[4:7], s73 offen offset:1024 sc1
s_add_u32 s73, s85, 28672
buffer_load_dwordx4 v[160:163], v1, s[4:7], s73 offen offset:0 sc1
s_add_u32 s73, s85, 32768
buffer_load_dwordx4 v[164:167], v1, s[4:7], s73 offen offset:0 sc1
s_waitcnt vmcnt(0)
v_max3_u32 v18, v193, v195, v197
v_max_u32 v18, v18, v199
v_cmp_eq_u32 vcc, -1, v18
s_cbranch_vccz .LB_229_%=
s_sub_u32 s76, s76, 1
s_cbranch_scc0 .LS_228_%=
s_mov_b32 s76, 0
s_branch .LB_229_%=
.LS_231_%=:
s_nop 7
s_add_u32 s73, s85, 20480
buffer_load_dwordx4 v[200:203], v1, s[4:7], s73 offen offset:0 sc1
s_add_u32 s73, s85, 20480
buffer_load_dwordx4 v[204:207], v1, s[4:7], s73 offen offset:1024 sc1
s_add_u32 s73, s85, 24576
buffer_load_dwordx4 v[208:211], v1, s[4:7], s73 offen offset:0 sc1
s_add_u32 s73, s85, 24576
buffer_load_dwordx4 v[212:215], v1, s[4:7], s73 offen offset:1024 sc1
s_add_u32 s73, s85, 28672
buffer_load_dwordx4 v[160:163], v1, s[4:7], s73 offen offset:0 sc1
s_add_u32 s73, s85, 32768
buffer_load_dwordx4 v[164:167], v1, s[4:7], s73 offen offset:0 sc1
s_waitcnt vmcnt(0)
v_max3_u32 v18, v201, v203, v205
v_max_u32 v18, v18, v207
v_cmp_eq_u32 vcc, -1, v18
s_cbranch_vccz .LB_232_%=
s_sub_u32 s76, s76, 1
s_cbranch_scc0 .LS_231_%=
s_mov_b32 s76, 0
s_branch .LB_232_%=
.LS_234_%=:
s_nop 7
s_add_u32 s73, s85, 24576
buffer_load_dwordx4 v[208:211], v1, s[4:7], s73 offen offset:0 sc1
s_add_u32 s73, s85, 24576
buffer_load_dwordx4 v[212:215], v1, s[4:7], s73 offen offset:1024 sc1
s_add_u32 s73, s85, 28672
buffer_load_dwordx4 v[160:163], v1, s[4:7], s73 offen offset:0 sc1
s_add_u32 s73, s85, 32768
buffer_load_dwordx4 v[164:167], v1, s[4:7], s73 offen offset:0 sc1
s_waitcnt vmcnt(0)
v_max3_u32 v18, v209, v211, v213
v_max_u32 v18, v18, v215
v_cmp_eq_u32 vcc, -1, v18
s_cbranch_vccz .LB_235_%=
s_sub_u32 s76, s76, 1
s_cbranch_scc0 .LS_234_%=
s_mov_b32 s76, 0
s_branch .LB_235_%=
.LS_237_%=:
s_nop 7
s_add_u32 s73, s85, 28672
buffer_load_dwordx4 v[160:163], v1, s[4:7], s73 offen offset:0 sc1
s_add_u32 s73, s85, 32768
buffer_load_dwordx4 v[164:167], v1, s[4:7], s73 offen offset:0 sc1
s_waitcnt vmcnt(0)
v_max3_u32 v18, v161, v163, v165
v_max_u32 v18, v18, v167
v_cmp_eq_u32 vcc, -1, v18
s_cbranch_vccz .LB_238_%=
s_sub_u32 s76, s76, 1
s_cbranch_scc0 .LS_237_%=
s_mov_b32 s76, 0
s_branch .LB_238_%=
.LS_240_%=:
s_nop 7
s_add_u32 s73, s85, 0
buffer_load_dwordx4 v[168:171], v1, s[4:7], s73 offen offset:2048 sc1
s_add_u32 s73, s85, 0
buffer_load_dwordx4 v[172:175], v1, s[4:7], s73 offen offset:3072 sc1
s_add_u32 s73, s85, 4096
buffer_load_dwordx4 v[176:179], v1, s[4:7], s73 offen offset:2048 sc1
s_add_u32 s73, s85, 4096
buffer_load_dwordx4 v[180:183], v1, s[4:7], s73 offen offset:3072 sc1
s_add_u32 s73, s85, 8192
buffer_load_dwordx4 v[184:187], v1, s[4:7], s73 offen offset:2048 sc1
s_add_u32 s73, s85, 8192
buffer_load_dwordx4 v[188:191], v1, s[4:7], s73 offen offset:3072 sc1
s_add_u32 s73, s85, 12288
buffer_load_dwordx4 v[192:195], v1, s[4:7], s73 offen offset:2048 sc1
s_add_u32 s73, s85, 12288
buffer_load_dwordx4 v[196:199], v1, s[4:7], s73 offen offset:3072 sc1
s_waitcnt vmcnt(0)
v_max3_u32 v18, v169, v171, v173
v_max_u32 v18, v18, v175
v_cmp_eq_u32 vcc, -1, v18
s_cbranch_vccz .LB_241_%=
s_sub_u32 s76, s76, 1
s_cbranch_scc0 .LS_240_%=
s_mov_b32 s76, 0
s_branch .LB_241_%=
.LS_243_%=:
s_nop 7
s_add_u32 s73, s85, 4096
buffer_load_dwordx4 v[176:179], v1, s[4:7], s73 offen offset:2048 sc1
s_add_u32 s73, s85, 4096
buffer_load_dwordx4 v[180:183], v1, s[4:7], s73 offen offset:3072 sc1
s_add_u32 s73, s85, 8192
buffer_load_dwordx4 v[184:187], v1, s[4:7], s73 offen offset:2048 sc1
s_add_u32 s73, s85, 8192
buffer_load_dwordx4 v[188:191], v1, s[4:7], s73 offen offset:3072 sc1
s_add_u32 s73, s85, 12288
buffer_load_dwordx4 v[192:195], v1, s[4:7], s73 offen offset:2048 sc1
s_add_u32 s73, s85, 12288
buffer_load_dwordx4 v[196:199], v1, s[4:7], s73 offen offset:3072 sc1
s_waitcnt vmcnt(0)
v_max3_u32 v18, v177, v179, v181
v_max_u32 v18, v18, v183
v_cmp_eq_u32 vcc, -1, v18
s_cbranch_vccz .LB_244_%=
s_sub_u32 s76, s76, 1
s_cbranch_scc0 .LS_243_%=
s_mov_b32 s76, 0
s_branch .LB_244_%=
.LS_246_%=:
s_nop 7
s_add_u32 s73, s85, 8192
buffer_load_dwordx4 v[184:187], v1, s[4:7], s73 offen offset:2048 sc1
s_add_u32 s73, s85, 8192
buffer_load_dwordx4 v[188:191], v1, s[4:7], s73 offen offset:3072 sc1
s_add_u32 s73, s85, 12288
buffer_load_dwordx4 v[192:195], v1, s[4:7], s73 offen offset:2048 sc1
s_add_u32 s73, s85, 12288
buffer_load_dwordx4 v[196:199], v1, s[4:7], s73 offen offset:3072 sc1
s_waitcnt vmcnt(0)
v_max3_u32 v18, v185, v187, v189
v_max_u32 v18, v18, v191
v_cmp_eq_u32 vcc, -1, v18
s_cbranch_vccz .LB_247_%=
s_sub_u32 s76, s76, 1
s_cbranch_scc0 .LS_246_%=
s_mov_b32 s76, 0
s_branch .LB_247_%=
.LS_249_%=:
s_nop 7
s_add_u32 s73, s85, 12288
buffer_load_dwordx4 v[192:195], v1, s[4:7], s73 offen offset:2048 sc1
s_add_u32 s73, s85, 12288
buffer_load_dwordx4 v[196:199], v1, s[4:7], s73 offen offset:3072 sc1
s_waitcnt vmcnt(0)
v_max3_u32 v18, v193, v195, v197
v_max_u32 v18, v18, v199
v_cmp_eq_u32 vcc, -1, v18
s_cbranch_vccz .LB_250_%=
s_sub_u32 s76, s76, 1
s_cbranch_scc0 .LS_249_%=
s_mov_b32 s76, 0
s_branch .LB_250_%=
.LS_252_%=:
s_nop 7
s_add_u32 s73, s85, 16384
buffer_load_dwordx4 v[200:203], v1, s[4:7], s73 offen offset:2048 sc1
s_add_u32 s73, s85, 16384
buffer_load_dwordx4 v[204:207], v1, s[4:7], s73 offen offset:3072 sc1
s_add_u32 s73, s85, 20480
buffer_load_dwordx4 v[208:211], v1, s[4:7], s73 offen offset:2048 sc1
s_add_u32 s73, s85, 20480
buffer_load_dwordx4 v[212:215], v1, s[4:7], s73 offen offset:3072 sc1
s_add_u32 s73, s85, 24576
buffer_load_dwordx4 v[160:163], v1, s[4:7], s73 offen offset:2048 sc1
s_add_u32 s73, s85, 24576
buffer_load_dwordx4 v[164:167], v1, s[4:7], s73 offen offset:3072 sc1
s_add_u32 s73, s85, 36864
buffer_load_dwordx4 v[168:171], v1, s[4:7], s73 offen offset:0 sc1
s_add_u32 s73, s85, 40960
buffer_load_dwordx4 v[172:175], v1, s[4:7], s73 offen offset:0 sc1
s_waitcnt vmcnt(0)
v_max3_u32 v18, v201, v203, v205
v_max_u32 v18, v18, v207
v_cmp_eq_u32 vcc, -1, v18
s_cbranch_vccz .LB_253_%=
s_sub_u32 s76, s76, 1
s_cbranch_scc0 .LS_252_%=
s_mov_b32 s76, 0
s_branch .LB_253_%=
.LS_255_%=:
s_nop 7
s_add_u32 s73, s85, 20480
buffer_load_dwordx4 v[208:211], v1, s[4:7], s73 offen offset:2048 sc1
s_add_u32 s73, s85, 20480
buffer_load_dwordx4 v[212:215], v1, s[4:7], s73 offen offset:3072 sc1
s_add_u32 s73, s85, 24576
buffer_load_dwordx4 v[160:163], v1, s[4:7], s73 offen offset:2048 sc1
s_add_u32 s73, s85, 24576
buffer_load_dwordx4 v[164:167], v1, s[4:7], s73 offen offset:3072 sc1
s_add_u32 s73, s85, 36864
buffer_load_dwordx4 v[168:171], v1, s[4:7], s73 offen offset:0 sc1
s_add_u32 s73, s85, 40960
buffer_load_dwordx4 v[172:175], v1, s[4:7], s73 offen offset:0 sc1
s_waitcnt vmcnt(0)
v_max3_u32 v18, v209, v211, v213
v_max_u32 v18, v18, v215
v_cmp_eq_u32 vcc, -1, v18
s_cbranch_vccz .LB_256_%=
s_sub_u32 s76, s76, 1
s_cbranch_scc0 .LS_255_%=
s_mov_b32 s76, 0
s_branch .LB_256_%=
.LS_258_%=:
s_nop 7
s_add_u32 s73, s85, 24576
buffer_load_dwordx4 v[160:163], v1, s[4:7], s73 offen offset:2048 sc1
s_add_u32 s73, s85, 24576
buffer_load_dwordx4 v[164:167], v1, s[4:7], s73 offen offset:3072 sc1
s_add_u32 s73, s85, 36864
buffer_load_dwordx4 v[168:171], v1, s[4:7], s73 offen offset:0 sc1
s_add_u32 s73, s85, 40960
buffer_load_dwordx4 v[172:175], v1, s[4:7], s73 offen offset:0 sc1
)ASM"
R"ASM(
s_waitcnt vmcnt(0)
v_max3_u32 v18, v161, v163, v165
v_max_u32 v18, v18, v167
v_cmp_eq_u32 vcc, -1, v18
s_cbranch_vccz .LB_259_%=
s_sub_u32 s76, s76, 1
s_cbranch_scc0 .LS_258_%=
s_mov_b32 s76, 0
s_branch .LB_259_%=
.LS_261_%=:
s_nop 7
s_add_u32 s73, s85, 36864
buffer_load_dwordx4 v[168:171], v1, s[4:7], s73 offen offset:0 sc1
s_add_u32 s73, s85, 40960
buffer_load_dwordx4 v[172:175], v1, s[4:7], s73 offen offset:0 sc1
s_waitcnt vmcnt(0)
v_max3_u32 v18, v169, v171, v173
v_max_u32 v18, v18, v175
v_cmp_eq_u32 vcc, -1, v18
s_cbranch_vccz .LB_262_%=
s_sub_u32 s76, s76, 1
s_cbranch_scc0 .LS_261_%=
s_mov_b32 s76, 0
s_branch .LB_262_%=
.LS_264_%=:
s_nop 7
s_add_u32 s73, s85, 45056
buffer_load_dwordx4 v[176:179], v1, s[4:7], s73 offen offset:0 sc1
s_add_u32 s73, s85, 49152
buffer_load_dwordx4 v[180:183], v1, s[4:7], s73 offen offset:0 sc1
s_add_u32 s73, s85, 53248
buffer_load_dwordx4 v[184:187], v1, s[4:7], s73 offen offset:0 sc1
s_add_u32 s73, s85, 28672
buffer_load_dwordx4 v[188:191], v1, s[4:7], s73 offen offset:1024 sc1
s_add_u32 s73, s85, 32768
buffer_load_dwordx4 v[192:195], v1, s[4:7], s73 offen offset:1024 sc1
s_add_u32 s73, s85, 36864
buffer_load_dwordx4 v[196:199], v1, s[4:7], s73 offen offset:1024 sc1
s_add_u32 s73, s85, 40960
buffer_load_dwordx4 v[200:203], v1, s[4:7], s73 offen offset:1024 sc1
s_add_u32 s73, s85, 45056
buffer_load_dwordx4 v[204:207], v1, s[4:7], s73 offen offset:1024 sc1
s_waitcnt vmcnt(0)
v_max3_u32 v18, v177, v179, v181
v_max_u32 v18, v18, v183
v_cmp_eq_u32 vcc, -1, v18
s_cbranch_vccz .LB_265_%=
s_sub_u32 s76, s76, 1
s_cbranch_scc0 .LS_264_%=
s_mov_b32 s76, 0
s_branch .LB_265_%=
.LS_267_%=:
s_nop 7
s_add_u32 s73, s85, 53248
buffer_load_dwordx4 v[184:187], v1, s[4:7], s73 offen offset:0 sc1
s_add_u32 s73, s85, 28672
buffer_load_dwordx4 v[188:191], v1, s[4:7], s73 offen offset:1024 sc1
s_add_u32 s73, s85, 32768
buffer_load_dwordx4 v[192:195], v1, s[4:7], s73 offen offset:1024 sc1
s_add_u32 s73, s85, 36864
buffer_load_dwordx4 v[196:199], v1, s[4:7], s73 offen offset:1024 sc1
s_add_u32 s73, s85, 40960
buffer_load_dwordx4 v[200:203], v1, s[4:7], s73 offen offset:1024 sc1
s_add_u32 s73, s85, 45056
buffer_load_dwordx4 v[204:207], v1, s[4:7], s73 offen offset:1024 sc1
s_waitcnt vmcnt(0)
v_max3_u32 v18, v185, v187, v189
v_max_u32 v18, v18, v191
v_cmp_eq_u32 vcc, -1, v18
s_cbranch_vccz .LB_268_%=
s_sub_u32 s76, s76, 1
s_cbranch_scc0 .LS_267_%=
s_mov_b32 s76, 0
s_branch .LB_268_%=
.LS_270_%=:
s_nop 7
s_add_u32 s73, s85, 32768
buffer_load_dwordx4 v[192:195], v1, s[4:7], s73 offen offset:1024 sc1
s_add_u32 s73, s85, 36864
buffer_load_dwordx4 v[196:199], v1, s[4:7], s73 offen offset:1024 sc1
s_add_u32 s73, s85, 40960
buffer_load_dwordx4 v[200:203], v1, s[4:7], s73 offen offset:1024 sc1
s_add_u32 s73, s85, 45056
buffer_load_dwordx4 v[204:207], v1, s[4:7], s73 offen offset:1024 sc1
s_waitcnt vmcnt(0)
v_max3_u32 v18, v193, v195, v197
v_max_u32 v18, v18, v199
v_cmp_eq_u32 vcc, -1, v18
s_cbranch_vccz .LB_271_%=
s_sub_u32 s76, s76, 1
s_cbranch_scc0 .LS_270_%=
s_mov_b32 s76, 0
s_branch .LB_271_%=
.LS_273_%=:
s_nop 7
s_add_u32 s73, s85, 40960
buffer_load_dwordx4 v[200:203], v1, s[4:7], s73 offen offset:1024 sc1
s_add_u32 s73, s85, 45056
buffer_load_dwordx4 v[204:207], v1, s[4:7], s73 offen offset:1024 sc1
s_waitcnt vmcnt(0)
v_max3_u32 v18, v201, v203, v205
v_max_u32 v18, v18, v207
v_cmp_eq_u32 vcc, -1, v18
s_cbranch_vccz .LB_274_%=
s_sub_u32 s76, s76, 1
s_cbranch_scc0 .LS_273_%=
s_mov_b32 s76, 0
s_branch .LB_274_%=
.LS_276_%=:
s_nop 7
s_add_u32 s73, s85, 49152
buffer_load_dwordx4 v[208:211], v1, s[4:7], s73 offen offset:1024 sc1
s_add_u32 s73, s85, 53248
buffer_load_dwordx4 v[212:215], v1, s[4:7], s73 offen offset:1024 sc1
s_add_u32 s73, s85, 28672
buffer_load_dwordx4 v[160:163], v1, s[4:7], s73 offen offset:2048 sc1
s_add_u32 s73, s85, 32768
buffer_load_dwordx4 v[164:167], v1, s[4:7], s73 offen offset:2048 sc1
s_add_u32 s73, s85, 36864
buffer_load_dwordx4 v[168:171], v1, s[4:7], s73 offen offset:2048 sc1
s_add_u32 s73, s85, 40960
buffer_load_dwordx4 v[172:175], v1, s[4:7], s73 offen offset:2048 sc1
s_add_u32 s73, s85, 45056
buffer_load_dwordx4 v[176:179], v1, s[4:7], s73 offen offset:2048 sc1
s_add_u32 s73, s85, 49152
buffer_load_dwordx4 v[180:183], v1, s[4:7], s73 offen offset:2048 sc1
s_waitcnt vmcnt(0)
v_max3_u32 v18, v209, v211, v213
v_max_u32 v18, v18, v215
v_cmp_eq_u32 vcc, -1, v18
s_cbranch_vccz .LB_277_%=
s_sub_u32 s76, s76, 1
s_cbranch_scc0 .LS_276_%=
s_mov_b32 s76, 0
s_branch .LB_277_%=
.LS_279_%=:
s_nop 7
s_add_u32 s73, s85, 28672
buffer_load_dwordx4 v[160:163], v1, s[4:7], s73 offen offset:2048 sc1
s_add_u32 s73, s85, 32768
buffer_load_dwordx4 v[164:167], v1, s[4:7], s73 offen offset:2048 sc1
s_add_u32 s73, s85, 36864
buffer_load_dwordx4 v[168:171], v1, s[4:7], s73 offen offset:2048 sc1
s_add_u32 s73, s85, 40960
buffer_load_dwordx4 v[172:175], v1, s[4:7], s73 offen offset:2048 sc1
s_add_u32 s73, s85, 45056
buffer_load_dwordx4 v[176:179], v1, s[4:7], s73 offen offset:2048 sc1
s_add_u32 s73, s85, 49152
buffer_load_dwordx4 v[180:183], v1, s[4:7], s73 offen offset:2048 sc1
s_waitcnt vmcnt(0)
v_max3_u32 v18, v161, v163, v165
v_max_u32 v18, v18, v167
v_cmp_eq_u32 vcc, -1, v18
s_cbranch_vccz .LB_280_%=
s_sub_u32 s76, s76, 1
s_cbranch_scc0 .LS_279_%=
s_mov_b32 s76, 0
s_branch .LB_280_%=
.LS_282_%=:
s_nop 7
s_add_u32 s73, s85, 36864
buffer_load_dwordx4 v[168:171], v1, s[4:7], s73 offen offset:2048 sc1
s_add_u32 s73, s85, 40960
buffer_load_dwordx4 v[172:175], v1, s[4:7], s73 offen offset:2048 sc1
s_add_u32 s73, s85, 45056
buffer_load_dwordx4 v[176:179], v1, s[4:7], s73 offen offset:2048 sc1
s_add_u32 s73, s85, 49152
buffer_load_dwordx4 v[180:183], v1, s[4:7], s73 offen offset:2048 sc1
s_waitcnt vmcnt(0)
v_max3_u32 v18, v169, v171, v173
v_max_u32 v18, v18, v175
v_cmp_eq_u32 vcc, -1, v18
s_cbranch_vccz .LB_283_%=
s_sub_u32 s76, s76, 1
s_cbranch_scc0 .LS_282_%=
s_mov_b32 s76, 0
s_branch .LB_283_%=
.LS_285_%=:
s_nop 7
s_add_u32 s73, s85, 45056
buffer_load_dwordx4 v[176:179], v1, s[4:7], s73 offen offset:2048 sc1
s_add_u32 s73, s85, 49152
buffer_load_dwordx4 v[180:183], v1, s[4:7], s73 offen offset:2048 sc1
s_waitcnt vmcnt(0)
v_max3_u32 v18, v177, v179, v181
v_max_u32 v18, v18, v183
v_cmp_eq_u32 vcc, -1, v18
s_cbranch_vccz .LB_286_%=
s_sub_u32 s76, s76, 1
s_cbranch_scc0 .LS_285_%=
s_mov_b32 s76, 0
s_branch .LB_286_%=
.LS_288_%=:
s_nop 7
s_add_u32 s73, s85, 53248
buffer_load_dwordx4 v[184:187], v1, s[4:7], s73 offen offset:2048 sc1
s_add_u32 s73, s85, 28672
buffer_load_dwordx4 v[188:191], v1, s[4:7], s73 offen offset:3072 sc1
s_add_u32 s73, s85, 32768
buffer_load_dwordx4 v[192:195], v1, s[4:7], s73 offen offset:3072 sc1
s_add_u32 s73, s85, 36864
buffer_load_dwordx4 v[196:199], v1, s[4:7], s73 offen offset:3072 sc1
s_add_u32 s73, s85, 40960
buffer_load_dwordx4 v[200:203], v1, s[4:7], s73 offen offset:3072 sc1
s_add_u32 s73, s85, 45056
buffer_load_dwordx4 v[204:207], v1, s[4:7], s73 offen offset:3072 sc1
s_add_u32 s73, s85, 49152
buffer_load_dwordx4 v[208:211], v1, s[4:7], s73 offen offset:3072 sc1
s_add_u32 s73, s85, 53248
buffer_load_dwordx4 v[212:215], v1, s[4:7], s73 offen offset:3072 sc1
s_waitcnt vmcnt(0)
v_max3_u32 v18, v185, v187, v189
v_max_u32 v18, v18, v191
v_cmp_eq_u32 vcc, -1, v18
s_cbranch_vccz .LB_289_%=
s_sub_u32 s76, s76, 1
s_cbranch_scc0 .LS_288_%=
s_mov_b32 s76, 0
s_branch .LB_289_%=
.LS_291_%=:
s_nop 7
s_add_u32 s73, s85, 32768
buffer_load_dwordx4 v[192:195], v1, s[4:7], s73 offen offset:3072 sc1
s_add_u32 s73, s85, 36864
buffer_load_dwordx4 v[196:199], v1, s[4:7], s73 offen offset:3072 sc1
s_add_u32 s73, s85, 40960
buffer_load_dwordx4 v[200:203], v1, s[4:7], s73 offen offset:3072 sc1
s_add_u32 s73, s85, 45056
buffer_load_dwordx4 v[204:207], v1, s[4:7], s73 offen offset:3072 sc1
s_add_u32 s73, s85, 49152
buffer_load_dwordx4 v[208:211], v1, s[4:7], s73 offen offset:3072 sc1
s_add_u32 s73, s85, 53248
buffer_load_dwordx4 v[212:215], v1, s[4:7], s73 offen offset:3072 sc1
s_waitcnt vmcnt(0)
v_max3_u32 v18, v193, v195, v197
v_max_u32 v18, v18, v199
v_cmp_eq_u32 vcc, -1, v18
s_cbranch_vccz .LB_292_%=
s_sub_u32 s76, s76, 1
s_cbranch_scc0 .LS_291_%=
s_mov_b32 s76, 0
s_branch .LB_292_%=
.LS_294_%=:
s_nop 7
s_add_u32 s73, s85, 40960
buffer_load_dwordx4 v[200:203], v1, s[4:7], s73 offen offset:3072 sc1
s_add_u32 s73, s85, 45056
buffer_load_dwordx4 v[204:207], v1, s[4:7], s73 offen offset:3072 sc1
s_add_u32 s73, s85, 49152
buffer_load_dwordx4 v[208:211], v1, s[4:7], s73 offen offset:3072 sc1
s_add_u32 s73, s85, 53248
buffer_load_dwordx4 v[212:215], v1, s[4:7], s73 offen offset:3072 sc1
s_waitcnt vmcnt(0)
v_max3_u32 v18, v201, v203, v205
v_max_u32 v18, v18, v207
v_cmp_eq_u32 vcc, -1, v18
s_cbranch_vccz .LB_295_%=
s_sub_u32 s76, s76, 1
s_cbranch_scc0 .LS_294_%=
s_mov_b32 s76, 0
s_branch .LB_295_%=
.LS_297_%=:
s_nop 7
s_add_u32 s73, s85, 49152
buffer_load_dwordx4 v[208:211], v1, s[4:7], s73 offen offset:3072 sc1
s_add_u32 s73, s85, 53248
buffer_load_dwordx4 v[212:215], v1, s[4:7], s73 offen offset:3072 sc1
s_waitcnt vmcnt(0)
v_max3_u32 v18, v209, v211, v213
v_max_u32 v18, v18, v215
v_cmp_eq_u32 vcc, -1, v18
s_cbranch_vccz .LB_298_%=
s_sub_u32 s76, s76, 1
s_cbranch_scc0 .LS_297_%=
s_mov_b32 s76, 0
s_branch .LB_298_%=
.LS_300_%=:
s_nop 7
buffer_load_dwordx4 v[216:219], v70, s[4:7], s94 offen sc1
buffer_load_dwordx4 v[220:223], v70, s[4:7], s95 offen sc1
s_waitcnt vmcnt(0)
v_max3_u32 v20, v217, v219, v221
v_max_u32 v20, v20, v223
v_cmp_eq_u32 vcc, -1, v20
s_cbranch_vccz .LB_301_%=
s_sub_u32 s76, s76, 1
s_cbranch_scc0 .LS_300_%=
s_mov_b32 s76, 0
s_branch .LB_301_%=
.LS_303_%=:
s_nop 7
s_mov_b32 s71, m0
s_add_u32 m0, s60, 0
s_nop 0
buffer_load_dwordx4 v62, s[12:15], s90 offen offset:0 sc1 lds
s_add_u32 s73, s91, 0
s_add_u32 m0, s60, 1024
s_nop 0
buffer_load_dwordx4 v62, s[12:15], s73 offen offset:0 sc1 lds
s_add_u32 s73, s91, 16384
s_add_u32 m0, s60, 2048
s_nop 0
buffer_load_dwordx4 v62, s[12:15], s73 offen offset:0 sc1 lds
s_add_u32 s73, s91, 32768
s_add_u32 m0, s60, 3072
s_nop 0
buffer_load_dwordx4 v62, s[12:15], s73 offen offset:0 sc1 lds
s_add_u32 s73, s91, 49152
s_add_u32 m0, s60, 4096
s_nop 0
buffer_load_dwordx4 v62, s[12:15], s73 offen offset:0 sc1 lds
s_add_u32 m0, s60, 4864
s_nop 0
buffer_load_dwordx4 v62, s[12:15], s90 offen offset:256 sc1 lds
s_add_u32 s73, s91, 0
s_add_u32 m0, s60, 5888
s_nop 0
buffer_load_dwordx4 v62, s[12:15], s73 offen offset:256 sc1 lds
s_add_u32 s73, s91, 16384
s_add_u32 m0, s60, 6912
s_nop 0
buffer_load_dwordx4 v62, s[12:15], s73 offen offset:256 sc1 lds
s_add_u32 s73, s91, 32768
s_add_u32 m0, s60, 7936
s_nop 0
buffer_load_dwordx4 v62, s[12:15], s73 offen offset:256 sc1 lds
s_add_u32 s73, s91, 49152
s_add_u32 m0, s60, 8960
s_nop 0
buffer_load_dwordx4 v62, s[12:15], s73 offen offset:256 sc1 lds
s_waitcnt vmcnt(0)
s_mov_b32 m0, s71
ds_read_b128 v[96:99], v73 offset:0
ds_read_b128 v[100:103], v73 offset:1024
ds_read_b128 v[104:107], v73 offset:2048
ds_read_b128 v[108:111], v73 offset:3072
ds_read_b128 v[112:115], v73 offset:4096
s_waitcnt lgkmcnt(0)
s_waitcnt vmcnt(0)
v_max3_u32 v226, v97, v99, v101
v_max3_u32 v226, v226, v103, v105
v_max3_u32 v226, v226, v107, v109
v_max3_u32 v226, v226, v111, v113
v_max_u32 v226, v226, v115
v_cmp_eq_u32 vcc, -1, v226
s_cbranch_vccz .LB_304_%=
s_sub_u32 s76, s76, 1
s_cbranch_scc0 .LS_303_%=
s_mov_b32 s76, 0
s_branch .LB_304_%=
.LS_306_%=:
s_nop 7
s_mov_b32 s71, m0
s_add_u32 m0, s60, 4864
s_nop 0
buffer_load_dwordx4 v62, s[12:15], s90 offen offset:256 sc1 lds
s_add_u32 s73, s91, 0
s_add_u32 m0, s60, 5888
s_nop 0
buffer_load_dwordx4 v62, s[12:15], s73 offen offset:256 sc1 lds
s_add_u32 s73, s91, 16384
s_add_u32 m0, s60, 6912
s_nop 0
buffer_load_dwordx4 v62, s[12:15], s73 offen offset:256 sc1 lds
s_add_u32 s73, s91, 32768
s_add_u32 m0, s60, 7936
s_nop 0
buffer_load_dwordx4 v62, s[12:15], s73 offen offset:256 sc1 lds
s_add_u32 s73, s91, 49152
s_add_u32 m0, s60, 8960
s_nop 0
buffer_load_dwordx4 v62, s[12:15], s73 offen offset:256 sc1 lds
s_add_u32 m0, s60, 9728
s_nop 0
buffer_load_dwordx4 v62, s[12:15], s90 offen offset:512 sc1 lds
)ASM"
R"ASM(
s_add_u32 s73, s91, 0
s_add_u32 m0, s60, 10752
s_nop 0
buffer_load_dwordx4 v62, s[12:15], s73 offen offset:512 sc1 lds
s_add_u32 s73, s91, 16384
s_add_u32 m0, s60, 11776
s_nop 0
buffer_load_dwordx4 v62, s[12:15], s73 offen offset:512 sc1 lds
s_add_u32 s73, s91, 32768
s_add_u32 m0, s60, 12800
s_nop 0
buffer_load_dwordx4 v62, s[12:15], s73 offen offset:512 sc1 lds
s_add_u32 s73, s91, 49152
s_add_u32 m0, s60, 13824
s_nop 0
buffer_load_dwordx4 v62, s[12:15], s73 offen offset:512 sc1 lds
s_waitcnt vmcnt(0)
s_mov_b32 m0, s71
ds_read_b128 v[96:99], v73 offset:5120
ds_read_b128 v[100:103], v73 offset:6144
ds_read_b128 v[104:107], v73 offset:7168
ds_read_b128 v[108:111], v73 offset:8192
ds_read_b128 v[112:115], v73 offset:9216
s_waitcnt lgkmcnt(0)
s_waitcnt vmcnt(0)
v_max3_u32 v226, v97, v99, v101
v_max3_u32 v226, v226, v103, v105
v_max3_u32 v226, v226, v107, v109
v_max3_u32 v226, v226, v111, v113
v_max_u32 v226, v226, v115
v_cmp_eq_u32 vcc, -1, v226
s_cbranch_vccz .LB_307_%=
s_sub_u32 s76, s76, 1
s_cbranch_scc0 .LS_306_%=
s_mov_b32 s76, 0
s_branch .LB_307_%=
.LS_309_%=:
s_nop 7
s_mov_b32 s71, m0
s_add_u32 m0, s60, 9728
s_nop 0
buffer_load_dwordx4 v62, s[12:15], s90 offen offset:512 sc1 lds
s_add_u32 s73, s91, 0
s_add_u32 m0, s60, 10752
s_nop 0
buffer_load_dwordx4 v62, s[12:15], s73 offen offset:512 sc1 lds
s_add_u32 s73, s91, 16384
s_add_u32 m0, s60, 11776
s_nop 0
buffer_load_dwordx4 v62, s[12:15], s73 offen offset:512 sc1 lds
s_add_u32 s73, s91, 32768
s_add_u32 m0, s60, 12800
s_nop 0
buffer_load_dwordx4 v62, s[12:15], s73 offen offset:512 sc1 lds
s_add_u32 s73, s91, 49152
s_add_u32 m0, s60, 13824
s_nop 0
buffer_load_dwordx4 v62, s[12:15], s73 offen offset:512 sc1 lds
s_add_u32 m0, s60, 14592
s_nop 0
buffer_load_dwordx4 v62, s[12:15], s90 offen offset:768 sc1 lds
s_add_u32 s73, s91, 0
s_add_u32 m0, s60, 15616
s_nop 0
buffer_load_dwordx4 v62, s[12:15], s73 offen offset:768 sc1 lds
s_add_u32 s73, s91, 16384
s_add_u32 m0, s60, 16640
s_nop 0
buffer_load_dwordx4 v62, s[12:15], s73 offen offset:768 sc1 lds
s_add_u32 s73, s91, 32768
s_add_u32 m0, s60, 17664
s_nop 0
buffer_load_dwordx4 v62, s[12:15], s73 offen offset:768 sc1 lds
s_add_u32 s73, s91, 49152
s_add_u32 m0, s60, 18688
s_nop 0
buffer_load_dwordx4 v62, s[12:15], s73 offen offset:768 sc1 lds
s_waitcnt vmcnt(0)
s_mov_b32 m0, s71
ds_read_b128 v[96:99], v73 offset:10240
ds_read_b128 v[100:103], v73 offset:11264
ds_read_b128 v[104:107], v73 offset:12288
ds_read_b128 v[108:111], v73 offset:13312
ds_read_b128 v[112:115], v73 offset:14336
s_waitcnt lgkmcnt(0)
s_waitcnt vmcnt(0)
v_max3_u32 v226, v97, v99, v101
v_max3_u32 v226, v226, v103, v105
v_max3_u32 v226, v226, v107, v109
v_max3_u32 v226, v226, v111, v113
v_max_u32 v226, v226, v115
v_cmp_eq_u32 vcc, -1, v226
s_cbranch_vccz .LB_310_%=
s_sub_u32 s76, s76, 1
s_cbranch_scc0 .LS_309_%=
s_mov_b32 s76, 0
s_branch .LB_310_%=
.LS_312_%=:
s_nop 7
s_mov_b32 s71, m0
s_add_u32 m0, s60, 14592
s_nop 0
buffer_load_dwordx4 v62, s[12:15], s90 offen offset:768 sc1 lds
s_add_u32 s73, s91, 0
s_add_u32 m0, s60, 15616
s_nop 0
buffer_load_dwordx4 v62, s[12:15], s73 offen offset:768 sc1 lds
s_add_u32 s73, s91, 16384
s_add_u32 m0, s60, 16640
s_nop 0
buffer_load_dwordx4 v62, s[12:15], s73 offen offset:768 sc1 lds
s_add_u32 s73, s91, 32768
s_add_u32 m0, s60, 17664
s_nop 0
buffer_load_dwordx4 v62, s[12:15], s73 offen offset:768 sc1 lds
s_add_u32 s73, s91, 49152
s_add_u32 m0, s60, 18688
s_nop 0
buffer_load_dwordx4 v62, s[12:15], s73 offen offset:768 sc1 lds
s_waitcnt vmcnt(0)
s_mov_b32 m0, s71
ds_read_b128 v[96:99], v73 offset:15360
ds_read_b128 v[100:103], v73 offset:16384
ds_read_b128 v[104:107], v73 offset:17408
ds_read_b128 v[108:111], v73 offset:18432
ds_read_b128 v[112:115], v73 offset:19456
s_waitcnt lgkmcnt(0)
s_waitcnt vmcnt(0)
v_max3_u32 v226, v97, v99, v101
v_max3_u32 v226, v226, v103, v105
v_max3_u32 v226, v226, v107, v109
v_max3_u32 v226, v226, v111, v113
v_max_u32 v226, v226, v115
v_cmp_eq_u32 vcc, -1, v226
s_cbranch_vccz .LB_313_%=
s_sub_u32 s76, s76, 1
s_cbranch_scc0 .LS_312_%=
s_mov_b32 s76, 0
s_branch .LB_313_%=
.LDONE_%=:
)ASM"
      :
      : "s"(__builtin_amdgcn_kernarg_segment_ptr()), "s"(blockIdx.x), "v"(threadIdx.x), "s"((__attribute__((address_space(3))) char*)smem)
      : "v1", "v2", "v3", "v4", "v5", "v6", "v7", "v8", "v9", "v10", "v11", "v12", "v13", "v14", "v15", "v16", "v17", "v18", "v19", "v20", "v21", "v22", "v23", "v24", "v25", "v26", "v27", "v28", "v29", "v30", "v31", "v32", "v33", "v34", "v35", "v36", "v37", "v38", "v39", "v40", "v41", "v42", "v43", "v44", "v45", "v46", "v47", "v48", "v49", "v50", "v51", "v52", "v53", "v54", "v55", "v56", "v57", "v58", "v59", "v60", "v61", "v62", "v63", "v64", "v65", "v66", "v67", "v68", "v69", "v70", "v71", "v72", "v73", "v74", "v75", "v76", "v77", "v78", "v79", "v80", "v81", "v82", "v83", "v84", "v85", "v86", "v87", "v88", "v89", "v90", "v91", "v92", "v93", "v94", "v95", "v96", "v97", "v98", "v99", "v100", "v101", "v102", "v103", "v104", "v105", "v106", "v107", "v108", "v109", "v110", "v111", "v112", "v113", "v114", "v115", "v116", "v117", "v118", "v119", "v120", "v121", "v122", "v123", "v124", "v125", "v126", "v127", "v128", "v129", "v130", "v131", "v132", "v133", "v134", "v135", "v136", "v137", "v138", "v139", "v140", "v141", "v142", "v143", "v144", "v145", "v146", "v147", "v148", "v149", "v150", "v151", "v152", "v153", "v154", "v155", "v156", "v157", "v158", "v159", "v160", "v161", "v162", "v163", "v164", "v165", "v166", "v167", "v168", "v169", "v170", "v171", "v172", "v173", "v174", "v175", "v176", "v177", "v178", "v179", "v180", "v181", "v182", "v183", "v184", "v185", "v186", "v187", "v188", "v189", "v190", "v191", "v192", "v193", "v194", "v195", "v196", "v197", "v198", "v199", "v200", "v201", "v202", "v203", "v204", "v205", "v206", "v207", "v208", "v209", "v210", "v211", "v212", "v213", "v214", "v215", "v216", "v217", "v218", "v219", "v220", "v221", "v222", "v223", "v224", "v225", "v226", "v227", "v228", "v229", "v230", "v231", "v232", "v233", "v234", "v235", "v236", "v237", "v238", "v239", "v240", "v241", "v242", "v243", "v244", "v245", "v246", "v247", "v248", "v249", "v250", "v251", "v252", "v253", "v254", "v255", "a0", "a1", "a2", "a3", "a4", "a5", "a6", "a7", "a8", "a9", "a10", "a11", "a12", "a13", "a14", "a15", "a16", "a17", "a18", "a19", "a20", "a21", "a22", "a23", "a24", "a25", "a26", "a27", "a28", "a29", "a30", "a31", "a32", "a33", "a34", "a35", "a36", "a37", "a38", "a39", "a40", "a41", "a42", "a43", "a44", "a45", "a46", "a47", "a48", "a49", "a50", "a51", "a52", "a53", "a54", "a55", "a56", "a57", "a58", "a59", "a60", "a61", "a62", "a63", "a64", "a65", "a66", "a67", "a68", "a69", "a70", "a71", "a72", "a73", "a74", "a75", "a76", "a77", "a78", "a79", "a80", "a81", "a82", "a83", "a84", "a85", "a86", "a87", "a88", "a89", "a90", "a91", "a92", "a93", "a94", "a95", "a96", "a97", "a98", "a99", "a100", "a101", "a102", "a103", "a104", "a105", "a106", "a107", "a108", "a109", "a110", "a111", "a112", "a113", "a114", "a115", "a116", "a117", "a118", "a119", "a120", "a121", "a122", "a123", "a124", "a125", "a126", "a127", "a128", "a129", "a130", "a131", "a132", "a133", "a134", "a135", "a136", "a137", "a138", "a139", "a140", "a141", "a142", "a143", "a144", "a145", "a146", "a147", "a148", "a149", "a150", "a151", "a152", "a153", "a154", "a155", "a156", "a157", "a158", "a159", "a160", "a161", "a162", "a163", "a164", "a165", "a166", "a167", "a168", "a169", "a170", "a171", "a172", "a173", "a174", "a175", "a176", "a177", "a178", "a179", "a180", "a181", "a182", "a183", "a184", "a185", "a186", "a187", "a188", "a189", "a190", "a191", "a192", "a193", "a194", "a195", "a196", "a197", "a198", "a199", "a200", "a201", "a202", "a203", "a204", "a205", "a206", "a207", "a208", "a209", "a210", "a211", "a212", "a213", "a214", "a215", "a216", "a217", "a218", "a219", "a220", "a221", "a222", "a223", "a224", "a225", "a226", "a227", "a228", "a229", "a230", "a231", "a232", "a233", "a234", "a235", "a236", "a237", "a238", "a239", "a240", "a241", "a242", "a243", "a244", "a245", "a246", "a247", "a248", "a249", "a250", "a251", "a252", "a253", "a254", "a255", "s4", "s5", "s6", "s7", "s8", "s9", "s10", "s11", "s12", "s13", "s14", "s15", "s16", "s17", "s18", "s19", "s20", "s21", "s22", "s23", "s24", "s25", "s26", "s27", "s28", "s29", "s30", "s31", "s32", "s33", "s34", "s35", "s36", "s37", "s38", "s39", "s40", "s41", "s42", "s43", "s44", "s45", "s46", "s47", "s48", "s49", "s50", "s51", "s52", "s53", "s54", "s55", "s56", "s57", "s58", "s59", "s60", "s61", "s62", "s63", "s64", "s65", "s66", "s67", "s68", "s69", "s70", "s71", "s72", "s73", "s74", "s75", "s76", "s77", "s78", "s79", "s80", "s81", "s82", "s83", "s84", "s85", "s86", "s87", "s88", "s89", "s90", "s91", "s92", "s93", "s94", "s95", "s96", "s97", "s98", "s99", "vcc", "scc", "memory");
#endif
}

extern "C" void kernel_launch(void* const* d_in, const int* in_sizes, int n_in, void* d_out, int out_size, void* d_ws, size_t ws_size,
                              hipStream_t stream) {
  fused_kernel<<<256, 256, 0, stream>>>((const u16*)d_in[0], (const u16*)d_in[1], (const u16*)d_in[2], (const u16*)d_in[4], (const u16*)d_in[5],
                                        (const u16*)d_in[6], (const u16*)d_in[7], (u16*)d_out, (char*)d_ws);
}
```
